```python
import torch

def _softmax(x, axis=-1):
    return torch.softmax(x, dim=axis)
def _gelu(x):
    return torch.nn.functional.gelu(x, approximate="tanh")
def _silu(x):
    return torch.nn.functional.silu(x)
def _sigmoid(x):
    return torch.sigmoid(x)
def _layernorm(x, axis=-1, eps=1e-5):
    m = x.mean(dim=axis, keepdim=True)
    v = x.var(dim=axis, keepdim=True, unbiased=False)
    return (x - m) / torch.sqrt(v + eps)
def _scatter_add(tgt, idx, src, axis=0):
    return tgt.clone().index_add_(axis, idx, src)
def _segment_sum(src, seg, n):
    out = torch.zeros((n,) + src.shape[1:], dtype=src.dtype)
    return out.index_add_(0, seg, src)
def _pad(x, pw):
    flat = tuple(v for pair in reversed(pw) for v in pair)
    return torch.nn.functional.pad(x, flat)

kernel_name = 'standalone__mha_softmax__near_ridge'

def setup_inputs():

    _w = (lambda a: (lambda t: t.to(torch.bfloat16) if t.is_floating_point() else t)(torch.as_tensor(a)))
    B, H, Sq, Sk, D = 8, 32, 512, 512, 192
    return {
        "q": _w((lambda *s: torch.randn(*s, dtype=torch.bfloat16) * 0.8)(B, H, Sq, D)),
        "k": _w((lambda *s: torch.randn(*s, dtype=torch.bfloat16) * 0.8)(B, H, Sk, D)),
        "v": _w((lambda *s: torch.randn(*s, dtype=torch.bfloat16) * 0.05)(B, H, Sk, D)),
    }

def reference(q, k, v):
    scale = q.shape[-1] ** -0.5
    scores = torch.einsum("bhqd,bhkd->bhqk", q, k) * scale
    p = _softmax(scores, axis=-1)
    return torch.einsum("bhqk,bhkd->bhqd", p, v)
```

```cpp
#include <hip/hip_runtime.h>
#include <hip/hip_bf16.h>
#define LOADPOL ""
#define STORE_NT 0
typedef float f32x16 __attribute__((ext_vector_type(16)));
typedef float f32x4 __attribute__((ext_vector_type(4)));
typedef float f32x2 __attribute__((ext_vector_type(2)));
typedef __bf16 bf16x8 __attribute__((ext_vector_type(8)));
typedef __bf16 bf16x4 __attribute__((ext_vector_type(4)));
typedef __bf16 bf16x2 __attribute__((ext_vector_type(2)));
typedef unsigned u32x4 __attribute__((ext_vector_type(4)));
typedef unsigned u32x2 __attribute__((ext_vector_type(2)));
#define DI __device__ __forceinline__
constexpr int D = 192, SQ = 512, SK = 512, ROWB = 384, UNITB = 32 * ROWB;
constexpr int NSLOT = 3;
DI unsigned rfl(unsigned x) { return __builtin_amdgcn_readfirstlane(x); }
DI unsigned cvtpk(float lo, float hi) { f32x2 v = {lo, hi}; bf16x2 b = __builtin_convertvector(v, bf16x2); return __builtin_bit_cast(unsigned, b); }

DI void dma3(unsigned m0v, const unsigned (&voff)[3], __amdgpu_buffer_rsrc_t srd, unsigned soff) {
  asm volatile("s_nop 4\n\ts_mov_b32 m0, %0\n\ts_nop 0\n\t"
               "buffer_load_dwordx4 %1, %4, %5 offen " LOADPOL " lds\n\t"
               "buffer_load_dwordx4 %2, %4, %5 offen offset:1024 " LOADPOL " lds\n\t"
               "buffer_load_dwordx4 %3, %4, %5 offen offset:2048 " LOADPOL " lds"
               :: "s"(m0v), "v"(voff[0]), "v"(voff[1]), "v"(voff[2]), "s"(srd), "s"(soff) : "m0", "memory");
}

__global__ __launch_bounds__(512, 1) void attn_base(const unsigned short* __restrict__ Q, const unsigned short* __restrict__ K, const unsigned short* __restrict__ V, unsigned short* __restrict__ O) {
  extern __shared__ __attribute__((aligned(16))) char lds[];
  const unsigned tid = threadIdx.x, lane = tid & 63u, l32 = lane & 31u, hi = lane >> 5;
  const unsigned wid = rfl(tid >> 6);
  const unsigned bid = blockIdx.x, xg = bid & 7u, j = bid >> 3;
  const unsigned head = xg * 32u + (j >> 1), half = j & 1u;
  const unsigned ldsb = (unsigned)(size_t)lds;
  const char* Kb = (const char*)K + (size_t)head * SK * ROWB;
  const char* Vb = (const char*)V + (size_t)head * SK * ROWB;
  const char* Qb = (const char*)Q + ((size_t)head * SQ + half * 256 + wid * 32) * ROWB;
  __amdgpu_buffer_rsrc_t srdK = __builtin_amdgcn_make_buffer_rsrc((void*)Kb, (short)0, SK * ROWB, 0x00020000);
  __amdgpu_buffer_rsrc_t srdV = __builtin_amdgcn_make_buffer_rsrc((void*)Vb, (short)0, SK * ROWB, 0x00020000);
  unsigned voffK[3], voffV[3];
  #pragma unroll
  for (int i = 0; i < 3; ++i) {
    unsigned n = 64u * i + lane, r = n / 24u, cp = n % 24u;
    unsigned gK = (4u * (wid & 1u)) ^ (r >> 1);
    unsigned gV = 4u * ((r >> 1) & 1u);
    voffK[i] = 384u * r + 16u * (cp ^ gK) - 1024u * i;
    voffV[i] = 384u * r + 16u * (cp ^ gV) - 1024u * i;
  }
  bf16x8 qf[1][12];
  #pragma unroll
  for (int qb = 0; qb < 1; ++qb)
    #pragma unroll
    for (int ds = 0; ds < 12; ++ds)
      qf[qb][ds] = *(const bf16x8*)(Qb + (size_t)(32 * qb + l32) * ROWB + 32 * ds + 16 * hi);
  #pragma unroll
  for (int qb = 0; qb < 1; ++qb)
    #pragma unroll
    for (int ds = 0; ds < 12; ++ds) asm volatile("" : "+v"(qf[qb][ds]));
  auto issue = [&](int u) {
    unsigned slot = (unsigned)(u % NSLOT);
    const unsigned kg = wid & 3u;
    if (wid < 4) dma3(rfl(ldsb + slot * 2 * UNITB + 3072u * kg), voffK, srdK, rfl((unsigned)u * UNITB + 3072u * kg));
    else dma3(rfl(ldsb + slot * 2 * UNITB + UNITB + 3072u * kg), voffV, srdV, rfl((unsigned)u * UNITB + 3072u * kg));
  };
  issue(0); issue(1);
  unsigned kb[4];
  #pragma unroll
  for (int m = 0; m < 4; ++m) kb[m] = 384u * l32 + 16u * ((2u * m + hi) ^ ((l32 >> 1) & 7u));
  const unsigned qp = (lane & 15u) >> 2, pp = lane & 3u, blk = (lane >> 4) & 1u, sv = (qp >> 1) & 1u;
  const unsigned vb_e = 384u * (4u * hi + qp) + 32u * blk + 8u * pp + 64u * sv;
  const unsigned vb_o = 384u * (4u * hi + qp) + 32u * blk + 8u * pp + 64u * (1u - sv);
  f32x16 o[6][1];
  #pragma unroll
  for (int db = 0; db < 6; ++db) for (int qb = 0; qb < 1; ++qb) for (int r = 0; r < 16; ++r) o[db][qb][r] = 0.f;
  float m[1] = {-1e30f}, l[1] = {0.f};
  const float C = 0.10411754829f;
  for (int u = 0; u < 16; ++u) {
    if (u + 1 < 16) asm volatile("s_waitcnt vmcnt(3)" ::: "memory"); else asm volatile("s_waitcnt vmcnt(0)" ::: "memory");
    __builtin_amdgcn_s_barrier();
    asm volatile("" ::: "memory");
    if (u + 2 < 16) issue(u + 2);
    const unsigned slot = (unsigned)(u % NSLOT);
    const char* Ks = lds + slot * 2 * UNITB;
    const unsigned Vs = ldsb + slot * 2 * UNITB + UNITB;
    f32x16 s[1];
    #pragma unroll
    for (int qb = 0; qb < 1; ++qb) for (int r = 0; r < 16; ++r) s[qb][r] = 0.f;
    #pragma unroll
    for (int ds = 0; ds < 12; ++ds) {
      bf16x8 kf = *(const bf16x8*)(Ks + kb[ds & 3] + 128 * (ds >> 2));
      s[0] = __builtin_amdgcn_mfma_f32_32x32x16_bf16(kf, qf[0][ds], s[0], 0, 0, 0);
    }
    bf16x8 pf[1][2];
    #pragma unroll
    for (int qb = 0; qb < 1; ++qb) {
      float mx = s[qb][0];
      #pragma unroll
      for (int r = 1; r < 16; ++r) mx = fmaxf(mx, s[qb][r]);
      auto pr = __builtin_amdgcn_permlane32_swap(__float_as_uint(mx), __float_as_uint(mx), false, false);
      mx = fmaxf(__uint_as_float(pr[0]), __uint_as_float(pr[1]));
      if (__any(mx > m[qb])) {
        float mn = fmaxf(m[qb], mx);
        float al = __builtin_amdgcn_exp2f((m[qb] - mn) * C);
        m[qb] = mn; l[qb] *= al;
        #pragma unroll
        for (int db = 0; db < 6; ++db) for (int r = 0; r < 16; ++r) o[db][qb][r] *= al;
      }
      const float nm = -m[qb] * C;
      float ps = 0.f;
      #pragma unroll
      for (int r = 0; r < 16; ++r) { s[qb][r] = __builtin_amdgcn_exp2f(fmaf(s[qb][r], C, nm)); ps += s[qb][r]; }
      l[qb] += ps;
      #pragma unroll
      for (int ks = 0; ks < 2; ++ks) {
        u32x4 w;
        #pragma unroll
        for (int e = 0; e < 4; ++e) w[e] = cvtpk(s[qb][8 * ks + 2 * e], s[qb][8 * ks + 2 * e + 1]);
        pf[qb][ks] = __builtin_bit_cast(bf16x8, w);
      }
    }
    #pragma unroll
    for (int ks = 0; ks < 2; ++ks) {
      bf16x4 vl[6], vh[6];
      #pragma unroll
      for (int db = 0; db < 6; ++db) {
        const unsigned base = Vs + ((db & 1) ? vb_o : vb_e);
        asm volatile("ds_read_b64_tr_b16 %0, %1 offset:%2" : "=v"(vl[db]) : "v"(base), "i"(384 * 16 * ks + 64 * (db & ~1)) : "memory");
        asm volatile("ds_read_b64_tr_b16 %0, %1 offset:%2" : "=v"(vh[db]) : "v"(base), "i"(384 * (16 * ks + 8) + 64 * (db & ~1)) : "memory");
      }
      asm volatile("s_waitcnt lgkmcnt(0)" : "+v"(vl[0]), "+v"(vl[1]), "+v"(vl[2]), "+v"(vl[3]), "+v"(vl[4]), "+v"(vl[5]), "+v"(vh[0]), "+v"(vh[1]), "+v"(vh[2]), "+v"(vh[3]), "+v"(vh[4]), "+v"(vh[5]) :: "memory");
      #pragma unroll
      for (int db = 0; db < 6; ++db) {
        bf16x8 vf = __builtin_shufflevector(vl[db], vh[db], 0, 1, 2, 3, 4, 5, 6, 7);
        o[db][0] = __builtin_amdgcn_mfma_f32_32x32x16_bf16(vf, pf[0][ks], o[db][0], 0, 0, 0);
      }
    }
    asm volatile("s_waitcnt lgkmcnt(0)" ::: "memory");
  }
  __builtin_amdgcn_s_barrier();
  asm volatile("" ::: "memory");
  char* stg = lds + wid * 12800;
  char* Ob = (char*)O + ((size_t)head * SQ + half * 256 + wid * 32) * ROWB;
  #pragma unroll
  for (int qb = 0; qb < 1; ++qb) {
    auto pr = __builtin_amdgcn_permlane32_swap(__float_as_uint(l[qb]), __float_as_uint(l[qb]), false, false);
    const float il = 1.0f / (__uint_as_float(pr[0]) + __uint_as_float(pr[1]));
    #pragma unroll
    for (int db = 0; db < 6; ++db)
      #pragma unroll
      for (int g = 0; g < 4; ++g) {
        u32x2 w = {cvtpk(o[db][qb][4 * g] * il, o[db][qb][4 * g + 1] * il), cvtpk(o[db][qb][4 * g + 2] * il, o[db][qb][4 * g + 3] * il)};
        *(u32x2*)(stg + 400 * l32 + 64 * db + 16 * g + 8 * hi) = w;
      }
    asm volatile("s_waitcnt lgkmcnt(0)" ::: "memory");
    #pragma unroll
    for (int i = 0; i < 12; ++i) {
      unsigned n = 64u * i + lane, r = n / 24u, c = n % 24u;
      u32x4 w = *(const u32x4*)(stg + 400 * r + 16 * c);
      u32x4* dst = (u32x4*)(Ob + (size_t)qb * 32 * ROWB + 16 * n);
      if (STORE_NT) __builtin_nontemporal_store(w, dst); else *dst = w;
    }
    asm volatile("s_waitcnt lgkmcnt(0)" ::: "memory");
  }
}

extern "C" void kernel_launch(void* const* d_in, const int* in_sizes, int n_in, void* d_out, int out_size, void* d_ws, size_t ws_size, hipStream_t stream) {
  constexpr int SHM = 8 * 12800;
  hipFuncSetAttribute((const void*)attn_base, hipFuncAttributeMaxDynamicSharedMemorySize, SHM);
  hipLaunchKernelGGL(attn_base, dim3(512), dim3(512), SHM, stream, (const unsigned short*)d_in[0], (const unsigned short*)d_in[1], (const unsigned short*)d_in[2], (unsigned short*)d_out);
}
```

```cpp
#include <hip/hip_runtime.h>
#include <hip/hip_bf16.h>
#define LOADPOL ""
#define STORE_NT 0
typedef float f32x16 __attribute__((ext_vector_type(16)));
typedef float f32x4 __attribute__((ext_vector_type(4)));
typedef float f32x2 __attribute__((ext_vector_type(2)));
typedef __bf16 bf16x8 __attribute__((ext_vector_type(8)));
typedef __bf16 bf16x4 __attribute__((ext_vector_type(4)));
typedef __bf16 bf16x2 __attribute__((ext_vector_type(2)));
typedef unsigned u32x4 __attribute__((ext_vector_type(4)));
typedef unsigned u32x2 __attribute__((ext_vector_type(2)));
#define DI __device__ __forceinline__
constexpr int D = 192, SQ = 512, SK = 512, ROWB = 384, UNITB = 32 * ROWB;
constexpr int NSLOT = 3;
DI unsigned rfl(unsigned x) { return __builtin_amdgcn_readfirstlane(x); }
DI unsigned cvtpk(float lo, float hi) { f32x2 v = {lo, hi}; bf16x2 b = __builtin_convertvector(v, bf16x2); return __builtin_bit_cast(unsigned, b); }

DI void dma3(unsigned m0v, const unsigned (&voff)[3], __amdgpu_buffer_rsrc_t srd, unsigned soff) {
  asm volatile("s_nop 4\n\ts_mov_b32 m0, %0\n\ts_nop 0\n\t"
               "buffer_load_dwordx4 %1, %4, %5 offen " LOADPOL " lds\n\t"
               "buffer_load_dwordx4 %2, %4, %5 offen offset:1024 " LOADPOL " lds\n\t"
               "buffer_load_dwordx4 %3, %4, %5 offen offset:2048 " LOADPOL " lds"
               :: "s"(m0v), "v"(voff[0]), "v"(voff[1]), "v"(voff[2]), "s"(srd), "s"(soff) : "m0", "memory");
}

__global__ __launch_bounds__(512, 1) void attn_base(const unsigned short* __restrict__ Q, const unsigned short* __restrict__ K, const unsigned short* __restrict__ V, unsigned short* __restrict__ O) {
  extern __shared__ __attribute__((aligned(16))) char lds[];
  const unsigned tid = threadIdx.x, lane = tid & 63u, l32 = lane & 31u, hi = lane >> 5;
  const unsigned wid = rfl(tid >> 6);
  const unsigned bid = blockIdx.x, xg = bid & 7u, j = bid >> 3;
  const unsigned head = xg * 32u + (j >> 1), half = j & 1u;
  const unsigned ldsb = (unsigned)(size_t)lds;
  const char* Kb = (const char*)K + (size_t)head * SK * ROWB;
  const char* Vb = (const char*)V + (size_t)head * SK * ROWB;
  const char* Qb = (const char*)Q + ((size_t)head * SQ + half * 256 + wid * 32) * ROWB;
  __amdgpu_buffer_rsrc_t srdK = __builtin_amdgcn_make_buffer_rsrc((void*)Kb, (short)0, SK * ROWB, 0x00020000);
  __amdgpu_buffer_rsrc_t srdV = __builtin_amdgcn_make_buffer_rsrc((void*)Vb, (short)0, SK * ROWB, 0x00020000);
  unsigned voffK[3], voffV[3];
  #pragma unroll
  for (int i = 0; i < 3; ++i) {
    unsigned n = 64u * i + lane, r = n / 24u, cp = n % 24u;
    unsigned gK = (4u * (wid & 1u)) ^ (r >> 1);
    unsigned gV = 4u * ((r >> 1) & 1u);
    voffK[i] = 384u * r + 16u * (cp ^ gK) - 1024u * i;
    voffV[i] = 384u * r + 16u * (cp ^ gV) - 1024u * i;
  }
  bf16x8 qf[1][12];
  #pragma unroll
  for (int qb = 0; qb < 1; ++qb)
    #pragma unroll
    for (int ds = 0; ds < 12; ++ds)
      qf[qb][ds] = *(const bf16x8*)(Qb + (size_t)(32 * qb + l32) * ROWB + 32 * ds + 16 * hi);
  #pragma unroll
  for (int qb = 0; qb < 1; ++qb)
    #pragma unroll
    for (int ds = 0; ds < 12; ++ds) asm volatile("" : "+v"(qf[qb][ds]));
  auto issue = [&](int u) {
    unsigned slot = (unsigned)(u % NSLOT);
    const unsigned kg = wid & 3u;
    if (wid < 4) dma3(rfl(ldsb + slot * 2 * UNITB + 3072u * kg), voffK, srdK, rfl((unsigned)u * UNITB + 3072u * kg));
    else dma3(rfl(ldsb + slot * 2 * UNITB + UNITB + 3072u * kg), voffV, srdV, rfl((unsigned)u * UNITB + 3072u * kg));
  };
  issue(0); issue(1);
  unsigned kb[4];
  #pragma unroll
  for (int m = 0; m < 4; ++m) kb[m] = 384u * l32 + 16u * ((2u * m + hi) ^ ((l32 >> 1) & 7u));
  const unsigned qp = (lane & 15u) >> 2, pp = lane & 3u, blk = (lane >> 4) & 1u, sv = (qp >> 1) & 1u;
  const unsigned vb_e = 384u * (4u * hi + qp) + 32u * blk + 8u * pp + 64u * sv;
  const unsigned vb_o = 384u * (4u * hi + qp) + 32u * blk + 8u * pp + 64u * (1u - sv);
  f32x16 o[6][1];
  #pragma unroll
  for (int db = 0; db < 6; ++db) for (int qb = 0; qb < 1; ++qb) for (int r = 0; r < 16; ++r) o[db][qb][r] = 0.f;
  float m[1] = {-1e30f}, l[1] = {0.f};
  const float C = 0.10411754829f;
  for (int u = 0; u < 16; ++u) {
    if (u + 1 < 16) asm volatile("s_waitcnt vmcnt(3)" ::: "memory"); else asm volatile("s_waitcnt vmcnt(0)" ::: "memory");
    __builtin_amdgcn_s_barrier();
    asm volatile("" ::: "memory");
    if (u + 2 < 16) issue(u + 2);
    const unsigned slot = (unsigned)(u % NSLOT);
    const char* Ks = lds + slot * 2 * UNITB;
    const unsigned Vs = ldsb + slot * 2 * UNITB + UNITB;
    f32x16 s[1];
    #pragma unroll
    for (int qb = 0; qb < 1; ++qb) for (int r = 0; r < 16; ++r) s[qb][r] = 0.f;
    #pragma unroll
    for (int ds = 0; ds < 12; ++ds) {
      bf16x8 kf = *(const bf16x8*)(Ks + kb[ds & 3] + 128 * (ds >> 2));
      s[0] = __builtin_amdgcn_mfma_f32_32x32x16_bf16(kf, qf[0][ds], s[0], 0, 0, 0);
    }
    bf16x8 pf[1][2];
    #pragma unroll
    for (int qb = 0; qb < 1; ++qb) {
      float mx = s[qb][0];
      #pragma unroll
      for (int r = 1; r < 16; ++r) mx = fmaxf(mx, s[qb][r]);
      auto pr = __builtin_amdgcn_permlane32_swap(__float_as_uint(mx), __float_as_uint(mx), false, false);
      mx = fmaxf(__uint_as_float(pr[0]), __uint_as_float(pr[1]));
      if (__any(mx > m[qb])) {
        float mn = fmaxf(m[qb], mx);
        float al = __builtin_amdgcn_exp2f((m[qb] - mn) * C);
        m[qb] = mn; l[qb] *= al;
        #pragma unroll
        for (int db = 0; db < 6; ++db) for (int r = 0; r < 16; ++r) o[db][qb][r] *= al;
      }
      const float nm = -m[qb] * C;
      float ps = 0.f;
      #pragma unroll
      for (int r = 0; r < 16; ++r) { s[qb][r] = __builtin_amdgcn_exp2f(fmaf(s[qb][r], C, nm)); ps += s[qb][r]; }
      l[qb] += ps;
      #pragma unroll
      for (int ks = 0; ks < 2; ++ks) {
        u32x4 w;
        #pragma unroll
        for (int e = 0; e < 4; ++e) w[e] = cvtpk(s[qb][8 * ks + 2 * e], s[qb][8 * ks + 2 * e + 1]);
        pf[qb][ks] = __builtin_bit_cast(bf16x8, w);
      }
    }
    #pragma unroll
    for (int ks = 0; ks < 2; ++ks) {
      bf16x4 vl[6], vh[6];
      #pragma unroll
      for (int db = 0; db < 6; ++db) {
        const unsigned base = Vs + ((db & 1) ? vb_o : vb_e);
        asm volatile("ds_read_b64_tr_b16 %0, %1 offset:%2" : "=v"(vl[db]) : "v"(base), "i"(384 * 16 * ks + 64 * (db & ~1)) : "memory");
        asm volatile("ds_read_b64_tr_b16 %0, %1 offset:%2" : "=v"(vh[db]) : "v"(base), "i"(384 * (16 * ks + 8) + 64 * (db & ~1)) : "memory");
      }
      asm volatile("s_waitcnt lgkmcnt(0)" : "+v"(vl[0]), "+v"(vl[1]), "+v"(vl[2]), "+v"(vl[3]), "+v"(vl[4]), "+v"(vl[5]), "+v"(vh[0]), "+v"(vh[1]), "+v"(vh[2]), "+v"(vh[3]), "+v"(vh[4]), "+v"(vh[5]) :: "memory");
      #pragma unroll
      for (int db = 0; db < 6; ++db) {
        bf16x8 vf = __builtin_shufflevector(vl[db], vh[db], 0, 1, 2, 3, 4, 5, 6, 7);
        o[db][0] = __builtin_amdgcn_mfma_f32_32x32x16_bf16(vf, pf[0][ks], o[db][0], 0, 0, 0);
      }
    }
    asm volatile("s_waitcnt lgkmcnt(0)" ::: "memory");
  }
  __builtin_amdgcn_s_barrier();
  asm volatile("" ::: "memory");
  char* stg = lds + wid * 12800;
  char* Ob = (char*)O + ((size_t)head * SQ + half * 256 + wid * 32) * ROWB;
  #pragma unroll
  for (int qb = 0; qb < 1; ++qb) {
    auto pr = __builtin_amdgcn_permlane32_swap(__float_as_uint(l[qb]), __float_as_uint(l[qb]), false, false);
    const float il = 1.0f / (__uint_as_float(pr[0]) + __uint_as_float(pr[1]));
    #pragma unroll
    for (int db = 0; db < 6; ++db)
      #pragma unroll
      for (int g = 0; g < 4; ++g) {
        u32x2 w = {cvtpk(o[db][qb][4 * g] * il, o[db][qb][4 * g + 1] * il), cvtpk(o[db][qb][4 * g + 2] * il, o[db][qb][4 * g + 3] * il)};
        *(u32x2*)(stg + 400 * l32 + 64 * db + 16 * g + 8 * hi) = w;
      }
    asm volatile("s_waitcnt lgkmcnt(0)" ::: "memory");
    #pragma unroll
    for (int i = 0; i < 12; ++i) {
      unsigned n = 64u * i + lane, r = n / 24u, c = n % 24u;
      u32x4 w = *(const u32x4*)(stg + 400 * r + 16 * c);
      u32x4* dst = (u32x4*)(Ob + (size_t)qb * 32 * ROWB + 16 * n);
      if (STORE_NT) __builtin_nontemporal_store(w, dst); else *dst = w;
    }
    asm volatile("s_waitcnt lgkmcnt(0)" ::: "memory");
  }
}

template <int MODE>
__global__ __launch_bounds__(256) void probe_kernel(const u32x4* __restrict__ q, const u32x4* __restrict__ k, const u32x4* __restrict__ v, u32x4* __restrict__ o, int n16, unsigned long long* ts) {
  unsigned long long t0 = __builtin_amdgcn_s_memrealtime();
  int i = blockIdx.x * 256 + threadIdx.x;
  int stride = gridDim.x * 256;
  u32x4 acc = {0,0,0,0};
  for (; i < n16; i += stride) {
    u32x4 a, b, c;
    if (MODE & 1) { a = __builtin_nontemporal_load(q + i); b = __builtin_nontemporal_load(k + i); c = __builtin_nontemporal_load(v + i); }
    else { a = q[i]; b = k[i]; c = v[i]; }
    if (MODE & 2) { if (MODE & 4) __builtin_nontemporal_store(a ^ b ^ c, o + i); else o[i] = a ^ b ^ c; }
    else acc ^= a ^ b ^ c;
  }
  if (!(MODE & 2)) o[blockIdx.x * 256 + threadIdx.x] = acc;
  __syncthreads();
  if (threadIdx.x == 0) { unsigned long long t1 = __builtin_amdgcn_s_memrealtime(); ts[2 * blockIdx.x] = t0; ts[2 * blockIdx.x + 1] = t1; }
}
__global__ __launch_bounds__(256) void spin_kernel(const unsigned long long* ts, int nb, int mult) {
  __shared__ unsigned long long smn[256], smx[256];
  unsigned long long mn = ~0ull, mx = 0;
  for (int b = threadIdx.x; b < nb; b += 256) { unsigned long long a = ts[2 * b], c = ts[2 * b + 1]; mn = a < mn ? a : mn; mx = c > mx ? c : mx; }
  smn[threadIdx.x] = mn; smx[threadIdx.x] = mx; __syncthreads();
  if (threadIdx.x == 0) {
    for (int i = 0; i < 256; ++i) { mn = smn[i] < mn ? smn[i] : mn; mx = smx[i] > mx ? smx[i] : mx; }
    unsigned long long span = mx - mn; if (span > 20000ull) span = 20000ull;
    unsigned long long t0 = __builtin_amdgcn_s_memrealtime();
    while (__builtin_amdgcn_s_memrealtime() - t0 < span * (unsigned long long)mult) __builtin_amdgcn_s_sleep(8);
  }
}
extern "C" void kernel_launch(void* const* d_in, const int* in_sizes, int n_in, void* d_out, int out_size, void* d_ws, size_t ws_size, hipStream_t stream) {
  constexpr int SHM = 8 * 12800;
  hipFuncSetAttribute((const void*)attn_base, hipFuncAttributeMaxDynamicSharedMemorySize, SHM);
  unsigned long long* ts = (unsigned long long*)((char*)d_ws + (128u << 20));
  probe_kernel<1><<<2048, 256, 0, stream>>>((const u32x4*)d_in[0], (const u32x4*)d_in[1], (const u32x4*)d_in[2], (u32x4*)d_ws, in_sizes[0] / 8, ts);
  spin_kernel<<<1, 256, 0, stream>>>(ts, 2048, 10);
  hipLaunchKernelGGL(attn_base, dim3(512), dim3(512), SHM, stream, (const unsigned short*)d_in[0], (const unsigned short*)d_in[1], (const unsigned short*)d_in[2], (unsigned short*)d_out);
}
```

```cpp
#include <hip/hip_runtime.h>
#include <hip/hip_bf16.h>

#define ATTN_STAMPS 0

namespace {
constexpr int kS = 512, kD = 192;
typedef unsigned u32x4 __attribute__((ext_vector_type(4)));

__device__ __forceinline__ float bf_lo(unsigned w) { return __uint_as_float(w << 16); }
__device__ __forceinline__ float bf_hi(unsigned w) { return __uint_as_float(w & 0xffff0000u); }

__device__ __forceinline__ void safe_rows(const unsigned short* __restrict__ Q, const unsigned short* __restrict__ K, const unsigned short* __restrict__ V,
                                          unsigned short* __restrict__ O, unsigned head, unsigned row0, unsigned lane) {
  const float c = 0.10411754995584488f;
  const unsigned short* kh = K + (size_t)head * kS * kD;
  const unsigned short* vh = V + (size_t)head * kS * kD;
#pragma unroll 1
  for (unsigned r = 0; r < 64; ++r) {
    const size_t row = (size_t)head * kS + row0 + r;
    const u32x4* q = (const u32x4*)(Q + row * kD);
    float s[8];
#pragma unroll
    for (int j = 0; j < 8; ++j) {
      const u32x4* k = (const u32x4*)(kh + (size_t)(lane + 64 * j) * kD);
      float a = 0.f;
#pragma unroll 1
      for (int ch = 0; ch < kD / 8; ++ch) {
        const u32x4 x = q[ch], y = k[ch];
#pragma unroll
        for (int e = 0; e < 4; ++e) { a = fmaf(bf_lo(x[e]), bf_lo(y[e]), a); a = fmaf(bf_hi(x[e]), bf_hi(y[e]), a); }
      }
      s[j] = a;
    }
    float m = s[0];
#pragma unroll
    for (int j = 1; j < 8; ++j) m = fmaxf(m, s[j]);
#pragma unroll
    for (int o = 32; o > 0; o >>= 1) m = fmaxf(m, __shfl_xor(m, o));
    float l = 0.f;
#pragma unroll
    for (int j = 0; j < 8; ++j) { s[j] = exp2f((s[j] - m) * c); l += s[j]; }
#pragma unroll
    for (int o = 32; o > 0; o >>= 1) l += __shfl_xor(l, o);
    float o0 = 0.f, o1 = 0.f, o2 = 0.f;
#pragma unroll
    for (int j = 0; j < 8; ++j) {
#pragma unroll 1
      for (int t = 0; t < 64; ++t) {
        const float p = __shfl(s[j], t);
        const unsigned short* v = vh + (size_t)(t + 64 * j) * kD;
        o0 = fmaf(p, __uint_as_float((unsigned)v[lane] << 16), o0);
        o1 = fmaf(p, __uint_as_float((unsigned)v[lane + 64] << 16), o1);
        o2 = fmaf(p, __uint_as_float((unsigned)v[lane + 128] << 16), o2);
      }
    }
    const float il = 1.0f / l;
    __hip_bfloat16* out = (__hip_bfloat16*)(O + row * kD);
    out[lane] = __float2bfloat16(o0 * il);
    out[lane + 64] = __float2bfloat16(o1 * il);
    out[lane + 128] = __float2bfloat16(o2 * il);
  }
}
}

__global__ __launch_bounds__(256, 1) void attn_fwd_kernel(const unsigned short* __restrict__ Q, const unsigned short* __restrict__ K,
                                                          const unsigned short* __restrict__ V, unsigned short* __restrict__ O, unsigned* __restrict__ dbg) {
  unsigned flag;
  const unsigned long long ka = (unsigned long long)__builtin_amdgcn_kernarg_segment_ptr();
  (void)dbg;
  asm volatile(
    "L_code_start:\n"
    "s_load_dwordx8 s[60:67], %1, 0x0\n"
    "s_getpc_b64 s[52:53]\n"
    "s_and_b32 s53, s53, 0xffff\n"
    "s_mov_b32 s54, L_code_end-L_code_start-4\n"
    "s_mov_b32 s55, 0x20000\n"
    "v_and_b32 v249, 63, v255\n"
    "v_lshlrev_b32 v249, 6, v249\n"
    "s_mov_b32 m0, 159744\n"
    "s_mov_b32 s51, 64\n"
    "s_nop 0\n"
    "buffer_load_dword v249, s[52:55], s51 offen lds\n"
    "s_mov_b32 s51, 4160\n"
    "s_nop 0\n"
    "buffer_load_dword v249, s[52:55], s51 offen lds\n"
    "s_mov_b32 s51, 8256\n"
    "s_nop 0\n"
    "buffer_load_dword v249, s[52:55], s51 offen lds\n"
    "v_lshrrev_b32 v4, 6, v255\n"
    "s_nop 0\n"
    "v_readfirstlane_b32 s58, v4\n"
    "v_and_b32 v0, 63, v255\n"
    "s_nop 3\n"
    "s_mul_i32 s37, s58, 3072\n"
    "s_mul_i32 s44, s58, 24576\n"
    "s_add_u32 s44, s44, 61440\n"
    "s_and_b32 s50, %2, 7\n"
    "s_lshl_b32 s50, s50, 5\n"
    "s_lshr_b32 s51, %2, 4\n"
    "s_add_u32 s50, s50, s51\n"
    "s_mul_i32 s50, s50, 196608\n"
    "s_add_u32 s38, s50, s37\n"
    "s_add_u32 s39, s38, 3145728\n"
    "s_lshr_b32 s51, %2, 3\n"
    "s_and_b32 s51, s51, 1\n"
    "s_mul_i32 s51, s51, 98304\n"
    "s_add_u32 s50, s50, s51\n"
    "s_mul_i32 s51, s58, 24576\n"
    "s_add_u32 s40, s50, s51\n"
    "s_add_u32 s41, s40, 3145728\n"
    "s_and_b32 s50, s58, 1\n"
    "s_lshl_b32 s50, s50, 2\n"
    "v_add_u32 v4, 0, v0\n"
    "v_mul_u32_u24 v5, 43691, v4\n"
    "v_lshrrev_b32 v5, 20, v5\n"
    "v_mul_u32_u24 v6, 24, v5\n"
    "v_sub_u32 v6, v4, v6\n"
    "v_lshrrev_b32 v3, 1, v5\n"
    "v_mul_u32_u24 v5, 384, v5\n"
    "v_subrev_u32 v5, 0, v5\n"
    "v_xor_b32 v2, v6, v3\n"
    "v_lshl_add_u32 v225, v2, 4, v5\n"
    "v_xor_b32 v4, 4, v2\n"
    "v_lshl_add_u32 v228, v4, 4, v5\n"
    "v_xor_b32 v4, s50, v2\n"
    "v_lshl_add_u32 v222, v4, 4, v5\n"
    "v_lshlrev_b32 v3, 1, v3\n"
    "v_xor_b32 v4, v6, v3\n"
    "v_lshl_add_u32 v231, v4, 4, v5\n"
    "v_add_u32 v4, 64, v0\n"
    "v_mul_u32_u24 v5, 43691, v4\n"
    "v_lshrrev_b32 v5, 20, v5\n"
    "v_mul_u32_u24 v6, 24, v5\n"
    "v_sub_u32 v6, v4, v6\n"
    "v_lshrrev_b32 v3, 1, v5\n"
    "v_mul_u32_u24 v5, 384, v5\n"
    "v_subrev_u32 v5, 1024, v5\n"
    "v_xor_b32 v2, v6, v3\n"
    "v_lshl_add_u32 v226, v2, 4, v5\n"
    "v_xor_b32 v4, 4, v2\n"
    "v_lshl_add_u32 v229, v4, 4, v5\n"
    "v_xor_b32 v4, s50, v2\n"
    "v_lshl_add_u32 v223, v4, 4, v5\n"
    "v_lshlrev_b32 v3, 1, v3\n"
    "v_xor_b32 v4, v6, v3\n"
    "v_lshl_add_u32 v232, v4, 4, v5\n"
    "v_add_u32 v4, 128, v0\n"
    "v_mul_u32_u24 v5, 43691, v4\n"
    "v_lshrrev_b32 v5, 20, v5\n"
    "v_mul_u32_u24 v6, 24, v5\n"
    "v_sub_u32 v6, v4, v6\n"
    "v_lshrrev_b32 v3, 1, v5\n"
    "v_mul_u32_u24 v5, 384, v5\n"
    "v_subrev_u32 v5, 2048, v5\n"
    "v_xor_b32 v2, v6, v3\n"
    "v_lshl_add_u32 v227, v2, 4, v5\n"
    "v_xor_b32 v4, 4, v2\n"
    "v_lshl_add_u32 v230, v4, 4, v5\n"
    "v_xor_b32 v4, s50, v2\n"
    "v_lshl_add_u32 v224, v4, 4, v5\n"
    "v_lshlrev_b32 v3, 1, v3\n"
    "v_xor_b32 v4, v6, v3\n"
    "v_lshl_add_u32 v233, v4, 4, v5\n"
    "s_waitcnt lgkmcnt(0)\n"
    "s_mov_b32 s20, s62\n"
    "s_and_b32 s21, s63, 0xffff\n"
    "s_mov_b32 s22, 50331648\n"
    "s_mov_b32 s23, 0x20000\n"
    "s_mov_b32 s16, s60\n"
    "s_and_b32 s17, s61, 0xffff\n"
    "s_mov_b32 s18, 50331648\n"
    "s_mov_b32 s19, 0x20000\n"
    "s_mov_b32 s24, s64\n"
    "s_and_b32 s25, s65, 0xffff\n"
    "s_mov_b32 s26, 50331648\n"
    "s_mov_b32 s27, 0x20000\n"
    "s_mov_b32 s28, s66\n"
    "s_and_b32 s29, s67, 0xffff\n"
    "s_mov_b32 s30, 50331648\n"
    "s_mov_b32 s31, 0x20000\n"
    "s_add_u32 m0, s37, 0\n"
    "s_add_u32 s42, s38, 0\n"
    "s_nop 0\n"
    "buffer_load_dwordx4 v222, s[20:23], s42 offen nt lds\n"
    "buffer_load_dwordx4 v223, s[20:23], s42 offen offset:1024 nt lds\n"
    "buffer_load_dwordx4 v224, s[20:23], s42 offen offset:2048 nt lds\n"
    "s_add_u32 m0, s37, 12288\n"
    "s_add_u32 s42, s38, 12288\n"
    "s_nop 0\n"
    "buffer_load_dwordx4 v222, s[20:23], s42 offen nt lds\n"
    "buffer_load_dwordx4 v223, s[20:23], s42 offen offset:1024 nt lds\n"
    "buffer_load_dwordx4 v224, s[20:23], s42 offen offset:2048 nt lds\n"
    "s_add_u32 m0, s44, 0\n"
    "s_add_u32 s43, s40, 0\n"
    "s_nop 0\n"
    "buffer_load_dwordx4 v225, s[16:19], s43 offen nt lds\n"
    "buffer_load_dwordx4 v226, s[16:19], s43 offen offset:1024 nt lds\n"
    "buffer_load_dwordx4 v227, s[16:19], s43 offen offset:2048 nt lds\n"
    "s_add_u32 m0, s44, 3072\n"
    "s_add_u32 s43, s40, 3072\n"
    "s_nop 0\n"
    "buffer_load_dwordx4 v228, s[16:19], s43 offen nt lds\n"
    "buffer_load_dwordx4 v229, s[16:19], s43 offen offset:1024 nt lds\n"
    "buffer_load_dwordx4 v230, s[16:19], s43 offen offset:2048 nt lds\n"
    "s_add_u32 m0, s44, 6144\n"
    "s_add_u32 s43, s40, 6144\n"
    "s_nop 0\n"
    "buffer_load_dwordx4 v225, s[16:19], s43 offen nt lds\n"
    "buffer_load_dwordx4 v226, s[16:19], s43 offen offset:1024 nt lds\n"
    "buffer_load_dwordx4 v227, s[16:19], s43 offen offset:2048 nt lds\n"
    "s_add_u32 m0, s44, 9216\n"
    "s_add_u32 s43, s40, 9216\n"
    "s_nop 0\n"
    "buffer_load_dwordx4 v228, s[16:19], s43 offen nt lds\n"
    "buffer_load_dwordx4 v229, s[16:19], s43 offen offset:1024 nt lds\n"
    "buffer_load_dwordx4 v230, s[16:19], s43 offen offset:2048 nt lds\n"
    "s_add_u32 m0, s44, 12288\n"
    "s_add_u32 s43, s40, 12288\n"
    "s_nop 0\n"
    "buffer_load_dwordx4 v225, s[16:19], s43 offen nt lds\n"
    "buffer_load_dwordx4 v226, s[16:19], s43 offen offset:1024 nt lds\n"
    "buffer_load_dwordx4 v227, s[16:19], s43 offen offset:2048 nt lds\n"
    "s_add_u32 m0, s44, 15360\n"
    "s_add_u32 s43, s40, 15360\n"
    "s_nop 0\n"
    "buffer_load_dwordx4 v228, s[16:19], s43 offen nt lds\n"
    "buffer_load_dwordx4 v229, s[16:19], s43 offen offset:1024 nt lds\n"
    "buffer_load_dwordx4 v230, s[16:19], s43 offen offset:2048 nt lds\n"
    "s_add_u32 m0, s37, 24576\n"
    "s_add_u32 s42, s38, 0\n"
    "s_nop 0\n"
    "buffer_load_dwordx4 v231, s[24:27], s42 offen nt lds\n"
    "buffer_load_dwordx4 v232, s[24:27], s42 offen offset:1024 nt lds\n"
    "buffer_load_dwordx4 v233, s[24:27], s42 offen offset:2048 nt lds\n"
    "s_add_u32 m0, s44, 18432\n"
    "s_add_u32 s43, s40, 18432\n"
    "s_nop 0\n"
    "buffer_load_dwordx4 v225, s[16:19], s43 offen nt lds\n"
    "buffer_load_dwordx4 v226, s[16:19], s43 offen offset:1024 nt lds\n"
    "buffer_load_dwordx4 v227, s[16:19], s43 offen offset:2048 nt lds\n"
    "s_add_u32 m0, s44, 21504\n"
    "s_add_u32 s43, s40, 21504\n"
    "s_nop 0\n"
    "buffer_load_dwordx4 v228, s[16:19], s43 offen nt lds\n"
    "buffer_load_dwordx4 v229, s[16:19], s43 offen offset:1024 nt lds\n"
    "buffer_load_dwordx4 v230, s[16:19], s43 offen offset:2048 nt lds\n"
    "v_and_b32 v1, 15, v0\n"
    "v_lshrrev_b32 v2, 4, v0\n"
    "v_lshrrev_b32 v3, 1, v1\n"
    "v_mul_u32_u24 v4, 384, v1\n"
    "v_xor_b32 v5, v2, v3\n"
    "v_lshl_add_u32 v216, v5, 4, v4\n"
    "v_xor_b32 v217, 64, v216\n"
    "v_add_u32 v234, s44, v216\n"
    "v_add_u32 v235, s44, v217\n"
    "v_lshrrev_b32 v5, 1, v2\n"
    "v_xor_b32 v5, v5, v3\n"
    "v_lshl_add_u32 v5, v5, 4, v4\n"
    "v_and_b32 v6, 1, v2\n"
    "v_lshl_add_u32 v5, v6, 3, v5\n"
    "v_add_u32 v237, s44, v5\n"
    "v_xor_b32 v238, 32, v237\n"
    "v_xor_b32 v239, 64, v237\n"
    "v_xor_b32 v240, 96, v237\n"
    "v_add_u32 v241, s44, v225\n"
    "v_add_u32 v242, s44, v226\n"
    "v_add_u32 v243, s44, v227\n"
    "v_add_u32 v244, s44, v228\n"
    "v_add_u32 v245, s44, v229\n"
    "v_add_u32 v246, s44, v230\n"
    "v_lshlrev_b32 v236, 4, v0\n"
    "v_lshrrev_b32 v4, 2, v1\n"
    "v_lshl_add_u32 v5, v2, 2, v4\n"
    "v_mul_u32_u24 v5, 384, v5\n"
    "v_lshrrev_b32 v4, 1, v4\n"
    "v_and_b32 v6, 1, v2\n"
    "v_lshl_add_u32 v4, v6, 1, v4\n"
    "v_lshl_add_u32 v5, v4, 5, v5\n"
    "v_and_b32 v6, 3, v0\n"
    "v_lshl_add_u32 v218, v6, 3, v5\n"
    "v_xor_b32 v219, 32, v218\n"
    "v_xor_b32 v220, 64, v218\n"
    "v_xor_b32 v221, 96, v218\n"
    "v_cmp_eq_u32 vcc, 0, v6\n"
    "v_mov_b32 v4, 0x3f803f80\n"
    "s_nop 1\n"
    "v_cndmask_b32 v208, 0, v4, vcc\n"
    "v_cndmask_b32 v209, 0, v4, vcc\n"
    "v_cndmask_b32 v210, 0, v4, vcc\n"
    "v_cndmask_b32 v211, 0, v4, vcc\n"
    "s_mov_b32 s36, 0x3dd53b95\n"
    "s_mov_b32 s49, 0xbdd53b95\n"
    "s_mov_b32 s48, 0x5f800000\n"
    "s_mov_b32 s45, 0\n"
    "s_mov_b64 s[46:47], 0\n"
    "s_waitcnt vmcnt(27)\n"
    "s_barrier\n"
    "ds_read_b128 a[0:3], v216 offset:0\n"
    "ds_read_b128 a[4:7], v217 offset:0\n"
    "ds_read_b128 a[8:11], v216 offset:128\n"
    "ds_read_b128 a[12:15], v217 offset:128\n"
    "ds_read_b128 a[16:19], v216 offset:256\n"
    "ds_read_b128 a[20:23], v217 offset:256\n"
    "ds_read_b128 a[24:27], v216 offset:6144\n"
    "ds_read_b128 a[28:31], v217 offset:6144\n"
    "ds_read_b128 a[32:35], v216 offset:6272\n"
    "ds_read_b128 a[36:39], v217 offset:6272\n"
    "ds_read_b128 a[40:43], v216 offset:6400\n"
    "ds_read_b128 a[44:47], v217 offset:6400\n"
    "ds_read_b128 a[48:51], v216 offset:12288\n"
    "ds_read_b128 a[52:55], v217 offset:12288\n"
    "ds_read_b128 a[56:59], v216 offset:12416\n"
    "ds_read_b128 a[60:63], v217 offset:12416\n"
    "ds_read_b128 a[64:67], v216 offset:12544\n"
    "ds_read_b128 a[68:71], v217 offset:12544\n"
    "ds_read_b128 a[72:75], v216 offset:18432\n"
    "ds_read_b128 a[76:79], v217 offset:18432\n"
    "ds_read_b128 a[80:83], v216 offset:18560\n"
    "ds_read_b128 a[84:87], v217 offset:18560\n"
    "ds_read_b128 a[88:91], v216 offset:18688\n"
    "ds_read_b128 a[92:95], v217 offset:18688\n"
    "L_ph0:\n"
    "s_waitcnt vmcnt(21)\n"
    "ds_read_b128 a[96:99], v234 offset:0\n"
    "ds_read_b128 a[100:103], v235 offset:0\n"
    "ds_read_b128 a[104:107], v234 offset:128\n"
    "ds_read_b128 a[108:111], v235 offset:128\n"
    "ds_read_b128 a[112:115], v234 offset:256\n"
    "ds_read_b128 a[116:119], v235 offset:256\n"
    "s_waitcnt lgkmcnt(0)\n"
    "v_mfma_f32_16x16x32_bf16 v[0:3], a[0:3], a[96:99], 0\n"
    "v_mfma_f32_16x16x32_bf16 v[0:3], a[4:7], a[100:103], v[0:3]\n"
    "v_mfma_f32_16x16x32_bf16 v[0:3], a[8:11], a[104:107], v[0:3]\n"
    "v_mfma_f32_16x16x32_bf16 v[0:3], a[12:15], a[108:111], v[0:3]\n"
    "v_mfma_f32_16x16x32_bf16 v[0:3], a[16:19], a[112:115], v[0:3]\n"
    "v_mfma_f32_16x16x32_bf16 v[0:3], a[20:23], a[116:119], v[0:3]\n"
    "v_mfma_f32_16x16x32_bf16 v[4:7], a[24:27], a[96:99], 0\n"
    "v_mfma_f32_16x16x32_bf16 v[4:7], a[28:31], a[100:103], v[4:7]\n"
    "v_mfma_f32_16x16x32_bf16 v[4:7], a[32:35], a[104:107], v[4:7]\n"
    "v_mfma_f32_16x16x32_bf16 v[4:7], a[36:39], a[108:111], v[4:7]\n"
    "v_mfma_f32_16x16x32_bf16 v[4:7], a[40:43], a[112:115], v[4:7]\n"
    "v_mfma_f32_16x16x32_bf16 v[4:7], a[44:47], a[116:119], v[4:7]\n"
    "v_mfma_f32_16x16x32_bf16 v[32:35], a[48:51], a[96:99], 0\n"
    "v_mfma_f32_16x16x32_bf16 v[32:35], a[52:55], a[100:103], v[32:35]\n"
    "v_max3_f32 v176, v0, v1, v2\n"
    "v_mfma_f32_16x16x32_bf16 v[32:35], a[56:59], a[104:107], v[32:35]\n"
    "v_mfma_f32_16x16x32_bf16 v[32:35], a[60:63], a[108:111], v[32:35]\n"
    "v_mfma_f32_16x16x32_bf16 v[32:35], a[64:67], a[112:115], v[32:35]\n"
    "v_mfma_f32_16x16x32_bf16 v[32:35], a[68:71], a[116:119], v[32:35]\n"
    "v_mfma_f32_16x16x32_bf16 v[36:39], a[72:75], a[96:99], 0\n"
    "v_mfma_f32_16x16x32_bf16 v[36:39], a[76:79], a[100:103], v[36:39]\n"
    "v_max3_f32 v176, v176, v3, v4\n"
    "v_mfma_f32_16x16x32_bf16 v[36:39], a[80:83], a[104:107], v[36:39]\n"
    "v_max3_f32 v176, v176, v5, v6\n"
    "v_mfma_f32_16x16x32_bf16 v[36:39], a[84:87], a[108:111], v[36:39]\n"
    "v_max_f32 v176, v176, v7\n"
    "v_mfma_f32_16x16x32_bf16 v[36:39], a[88:91], a[112:115], v[36:39]\n"
    "v_mov_b32 v177, v176\n"
    "v_mfma_f32_16x16x32_bf16 v[36:39], a[92:95], a[116:119], v[36:39]\n"
    "s_barrier\n"
    "s_add_u32 m0, s37, 36864\n"
    "s_add_u32 s42, s38, 24576\n"
    "s_nop 0\n"
    "buffer_load_dwordx4 v222, s[20:23], s42 offen nt lds\n"
    "buffer_load_dwordx4 v223, s[20:23], s42 offen offset:1024 nt lds\n"
    "buffer_load_dwordx4 v224, s[20:23], s42 offen offset:2048 nt lds\n"
    "s_waitcnt vmcnt(18)\n"
    "ds_read_b128 a[120:123], v234 offset:6144\n"
    "ds_read_b128 a[124:127], v235 offset:6144\n"
    "ds_read_b128 a[128:131], v234 offset:6272\n"
    "ds_read_b128 a[132:135], v235 offset:6272\n"
    "ds_read_b128 a[136:139], v234 offset:6400\n"
    "ds_read_b128 a[140:143], v235 offset:6400\n"
    "s_waitcnt lgkmcnt(0)\n"
    "v_mfma_f32_16x16x32_bf16 v[8:11], a[0:3], a[120:123], 0\n"
    "v_permlane16_swap_b32 v176, v177\n"
    "v_mfma_f32_16x16x32_bf16 v[8:11], a[4:7], a[124:127], v[8:11]\n"
    "v_max_f32 v176, v176, v177\n"
    "v_mfma_f32_16x16x32_bf16 v[8:11], a[8:11], a[128:131], v[8:11]\n"
    "v_mov_b32 v177, v176\n"
    "v_mfma_f32_16x16x32_bf16 v[8:11], a[12:15], a[132:135], v[8:11]\n"
    "v_mfma_f32_16x16x32_bf16 v[8:11], a[16:19], a[136:139], v[8:11]\n"
    "v_permlane32_swap_b32 v176, v177\n"
    "v_mfma_f32_16x16x32_bf16 v[8:11], a[20:23], a[140:143], v[8:11]\n"
    "v_max_f32 v176, v176, v177\n"
    "v_mfma_f32_16x16x32_bf16 v[12:15], a[24:27], a[120:123], 0\n"
    "v_mul_f32 v212, s49, v176\n"
    "v_mfma_f32_16x16x32_bf16 v[12:15], a[28:31], a[124:127], v[12:15]\n"
    "v_fma_f32 v0, v0, s36, v212\n"
    "v_fma_f32 v1, v1, s36, v212\n"
    "v_mfma_f32_16x16x32_bf16 v[12:15], a[32:35], a[128:131], v[12:15]\n"
    "v_exp_f32 v0, v0\n"
    "v_mfma_f32_16x16x32_bf16 v[12:15], a[36:39], a[132:135], v[12:15]\n"
    "v_exp_f32 v1, v1\n"
    "v_mfma_f32_16x16x32_bf16 v[12:15], a[40:43], a[136:139], v[12:15]\n"
    "v_fma_f32 v2, v2, s36, v212\n"
    "v_fma_f32 v3, v3, s36, v212\n"
    "v_mfma_f32_16x16x32_bf16 v[12:15], a[44:47], a[140:143], v[12:15]\n"
    "v_exp_f32 v2, v2\n"
    "v_mfma_f32_16x16x32_bf16 v[40:43], a[48:51], a[120:123], 0\n"
    "v_exp_f32 v3, v3\n"
    "v_mfma_f32_16x16x32_bf16 v[40:43], a[52:55], a[124:127], v[40:43]\n"
    "v_fma_f32 v4, v4, s36, v212\n"
    "v_fma_f32 v5, v5, s36, v212\n"
    "v_mfma_f32_16x16x32_bf16 v[40:43], a[56:59], a[128:131], v[40:43]\n"
    "v_exp_f32 v4, v4\n"
    "v_mfma_f32_16x16x32_bf16 v[40:43], a[60:63], a[132:135], v[40:43]\n"
    "v_exp_f32 v5, v5\n"
    "v_mfma_f32_16x16x32_bf16 v[40:43], a[64:67], a[136:139], v[40:43]\n"
    "v_fma_f32 v6, v6, s36, v212\n"
    "v_fma_f32 v7, v7, s36, v212\n"
    "v_mfma_f32_16x16x32_bf16 v[40:43], a[68:71], a[140:143], v[40:43]\n"
    "v_exp_f32 v6, v6\n"
    "v_mfma_f32_16x16x32_bf16 v[44:47], a[72:75], a[120:123], 0\n"
    "v_exp_f32 v7, v7\n"
    "v_mfma_f32_16x16x32_bf16 v[44:47], a[76:79], a[124:127], v[44:47]\n"
    "v_cvt_pk_bf16_f32 v0, v0, v1\n"
    "v_cvt_pk_bf16_f32 v1, v2, v3\n"
    "v_mfma_f32_16x16x32_bf16 v[44:47], a[80:83], a[128:131], v[44:47]\n"
    "v_cvt_pk_bf16_f32 v2, v4, v5\n"
    "v_cvt_pk_bf16_f32 v3, v6, v7\n"
    "v_mfma_f32_16x16x32_bf16 v[44:47], a[84:87], a[132:135], v[44:47]\n"
    "v_max3_f32 v180, v8, v9, v10\n"
    "v_fma_f32 v32, v32, s36, v212\n"
    "v_mfma_f32_16x16x32_bf16 v[44:47], a[88:91], a[136:139], v[44:47]\n"
    "v_max3_f32 v180, v180, v11, v12\n"
    "v_fma_f32 v33, v33, s36, v212\n"
    "v_mfma_f32_16x16x32_bf16 v[44:47], a[92:95], a[140:143], v[44:47]\n"
    "v_max3_f32 v180, v180, v13, v14\n"
    "v_fma_f32 v34, v34, s36, v212\n"
    "s_add_u32 m0, s37, 49152\n"
    "s_add_u32 s42, s38, 12288\n"
    "s_nop 0\n"
    "buffer_load_dwordx4 v231, s[24:27], s42 offen nt lds\n"
    "buffer_load_dwordx4 v232, s[24:27], s42 offen offset:1024 nt lds\n"
    "buffer_load_dwordx4 v233, s[24:27], s42 offen offset:2048 nt lds\n"
    "s_waitcnt vmcnt(15)\n"
    "ds_read_b128 a[144:147], v234 offset:12288\n"
    "ds_read_b128 a[148:151], v235 offset:12288\n"
    "ds_read_b128 a[152:155], v234 offset:12416\n"
    "ds_read_b128 a[156:159], v235 offset:12416\n"
    "ds_read_b128 a[160:163], v234 offset:12544\n"
    "ds_read_b128 a[164:167], v235 offset:12544\n"
    "s_waitcnt lgkmcnt(0)\n"
    "v_mfma_f32_16x16x32_bf16 v[16:19], a[0:3], a[144:147], 0\n"
    "v_max_f32 v180, v180, v15\n"
    "v_fma_f32 v35, v35, s36, v212\n"
    "v_mfma_f32_16x16x32_bf16 v[16:19], a[4:7], a[148:151], v[16:19]\n"
    "v_mov_b32 v181, v180\n"
    "v_fma_f32 v36, v36, s36, v212\n"
    "v_mfma_f32_16x16x32_bf16 v[16:19], a[8:11], a[152:155], v[16:19]\n"
    "v_exp_f32 v32, v32\n"
    "v_mfma_f32_16x16x32_bf16 v[16:19], a[12:15], a[156:159], v[16:19]\n"
    "v_permlane16_swap_b32 v180, v181\n"
    "v_fma_f32 v37, v37, s36, v212\n"
    "v_mfma_f32_16x16x32_bf16 v[16:19], a[16:19], a[160:163], v[16:19]\n"
    "v_max_f32 v180, v180, v181\n"
    "v_fma_f32 v38, v38, s36, v212\n"
    "v_mfma_f32_16x16x32_bf16 v[16:19], a[20:23], a[164:167], v[16:19]\n"
    "v_mov_b32 v181, v180\n"
    "v_fma_f32 v39, v39, s36, v212\n"
    "v_mfma_f32_16x16x32_bf16 v[20:23], a[24:27], a[144:147], 0\n"
    "v_exp_f32 v33, v33\n"
    "v_mfma_f32_16x16x32_bf16 v[20:23], a[28:31], a[148:151], v[20:23]\n"
    "v_permlane32_swap_b32 v180, v181\n"
    "v_cvt_pk_bf16_f32 v32, v32, v33\n"
    "v_mfma_f32_16x16x32_bf16 v[20:23], a[32:35], a[152:155], v[20:23]\n"
    "v_max_f32 v180, v180, v181\n"
    "v_mfma_f32_16x16x32_bf16 v[20:23], a[36:39], a[156:159], v[20:23]\n"
    "v_mul_f32 v213, s49, v180\n"
    "v_mfma_f32_16x16x32_bf16 v[20:23], a[40:43], a[160:163], v[20:23]\n"
    "v_fma_f32 v8, v8, s36, v213\n"
    "v_fma_f32 v9, v9, s36, v213\n"
    "v_mfma_f32_16x16x32_bf16 v[20:23], a[44:47], a[164:167], v[20:23]\n"
    "v_exp_f32 v8, v8\n"
    "v_mfma_f32_16x16x32_bf16 v[48:51], a[48:51], a[144:147], 0\n"
    "v_exp_f32 v9, v9\n"
    "v_mfma_f32_16x16x32_bf16 v[48:51], a[52:55], a[148:151], v[48:51]\n"
    "v_fma_f32 v10, v10, s36, v213\n"
    "v_fma_f32 v11, v11, s36, v213\n"
    "v_mfma_f32_16x16x32_bf16 v[48:51], a[56:59], a[152:155], v[48:51]\n"
    "v_exp_f32 v10, v10\n"
    "v_mfma_f32_16x16x32_bf16 v[48:51], a[60:63], a[156:159], v[48:51]\n"
    "v_exp_f32 v11, v11\n"
    "v_mfma_f32_16x16x32_bf16 v[48:51], a[64:67], a[160:163], v[48:51]\n"
    "v_fma_f32 v12, v12, s36, v213\n"
    "v_fma_f32 v13, v13, s36, v213\n"
    "v_mfma_f32_16x16x32_bf16 v[48:51], a[68:71], a[164:167], v[48:51]\n"
    "v_exp_f32 v12, v12\n"
    "v_mfma_f32_16x16x32_bf16 v[52:55], a[72:75], a[144:147], 0\n"
    "v_exp_f32 v13, v13\n"
    "v_mfma_f32_16x16x32_bf16 v[52:55], a[76:79], a[148:151], v[52:55]\n"
    "v_fma_f32 v14, v14, s36, v213\n"
    "v_fma_f32 v15, v15, s36, v213\n"
    "v_mfma_f32_16x16x32_bf16 v[52:55], a[80:83], a[152:155], v[52:55]\n"
    "v_exp_f32 v14, v14\n"
    "v_mfma_f32_16x16x32_bf16 v[52:55], a[84:87], a[156:159], v[52:55]\n"
    "v_exp_f32 v15, v15\n"
    "v_mfma_f32_16x16x32_bf16 v[52:55], a[88:91], a[160:163], v[52:55]\n"
    "v_cvt_pk_bf16_f32 v8, v8, v9\n"
    "v_cvt_pk_bf16_f32 v9, v10, v11\n"
    "v_mfma_f32_16x16x32_bf16 v[52:55], a[92:95], a[164:167], v[52:55]\n"
    "v_cvt_pk_bf16_f32 v10, v12, v13\n"
    "v_cvt_pk_bf16_f32 v11, v14, v15\n"
    "s_waitcnt vmcnt(6)\n"
    "ds_read_b128 a[168:171], v234 offset:18432\n"
    "ds_read_b128 a[172:175], v235 offset:18432\n"
    "ds_read_b128 a[176:179], v234 offset:18560\n"
    "ds_read_b128 a[180:183], v235 offset:18560\n"
    "ds_read_b128 a[184:187], v234 offset:18688\n"
    "ds_read_b128 a[188:191], v235 offset:18688\n"
    "s_barrier\n"
    "s_add_u32 m0, s37, 0\n"
    "s_add_u32 s42, s38, 36864\n"
    "s_nop 0\n"
    "buffer_load_dwordx4 v222, s[20:23], s42 offen nt lds\n"
    "buffer_load_dwordx4 v223, s[20:23], s42 offen offset:1024 nt lds\n"
    "buffer_load_dwordx4 v224, s[20:23], s42 offen offset:2048 nt lds\n"
    "s_add_u32 m0, s37, 12288\n"
    "s_add_u32 s42, s38, 24576\n"
    "s_nop 0\n"
    "buffer_load_dwordx4 v231, s[24:27], s42 offen nt lds\n"
    "buffer_load_dwordx4 v232, s[24:27], s42 offen offset:1024 nt lds\n"
    "buffer_load_dwordx4 v233, s[24:27], s42 offen offset:2048 nt lds\n"
    "s_waitcnt lgkmcnt(0)\n"
    "v_mfma_f32_16x16x32_bf16 v[24:27], a[0:3], a[168:171], 0\n"
    "v_max3_f32 v184, v16, v17, v18\n"
    "v_fma_f32 v40, v40, s36, v213\n"
    "v_mfma_f32_16x16x32_bf16 v[24:27], a[4:7], a[172:175], v[24:27]\n"
    "v_max3_f32 v184, v184, v19, v20\n"
    "v_fma_f32 v41, v41, s36, v213\n"
    "v_mfma_f32_16x16x32_bf16 v[24:27], a[8:11], a[176:179], v[24:27]\n"
    "v_max3_f32 v184, v184, v21, v22\n"
    "v_fma_f32 v42, v42, s36, v213\n"
    "v_mfma_f32_16x16x32_bf16 v[24:27], a[12:15], a[180:183], v[24:27]\n"
    "v_max_f32 v184, v184, v23\n"
    "v_fma_f32 v43, v43, s36, v213\n"
    "v_mfma_f32_16x16x32_bf16 v[24:27], a[16:19], a[184:187], v[24:27]\n"
    "v_mov_b32 v185, v184\n"
    "v_fma_f32 v44, v44, s36, v213\n"
    "v_mfma_f32_16x16x32_bf16 v[24:27], a[20:23], a[188:191], v[24:27]\n"
    "v_exp_f32 v34, v34\n"
    "v_mfma_f32_16x16x32_bf16 v[28:31], a[24:27], a[168:171], 0\n"
    "v_permlane16_swap_b32 v184, v185\n"
    "v_fma_f32 v45, v45, s36, v213\n"
    "v_mfma_f32_16x16x32_bf16 v[28:31], a[28:31], a[172:175], v[28:31]\n"
    "v_max_f32 v184, v184, v185\n"
    "v_fma_f32 v46, v46, s36, v213\n"
    "v_mfma_f32_16x16x32_bf16 v[28:31], a[32:35], a[176:179], v[28:31]\n"
    "v_mov_b32 v185, v184\n"
    "v_fma_f32 v47, v47, s36, v213\n"
    "v_mfma_f32_16x16x32_bf16 v[28:31], a[36:39], a[180:183], v[28:31]\n"
    "v_exp_f32 v35, v35\n"
    "v_mfma_f32_16x16x32_bf16 v[28:31], a[40:43], a[184:187], v[28:31]\n"
    "v_permlane32_swap_b32 v184, v185\n"
    "v_cvt_pk_bf16_f32 v33, v34, v35\n"
    "v_mfma_f32_16x16x32_bf16 v[28:31], a[44:47], a[188:191], v[28:31]\n"
    "v_max_f32 v184, v184, v185\n"
    "v_mfma_f32_16x16x32_bf16 v[56:59], a[48:51], a[168:171], 0\n"
    "ds_read_b64_tr_b16 a[0:1], v218 offset:24576\n"
    "v_mul_f32 v214, s49, v184\n"
    "v_mfma_f32_16x16x32_bf16 v[56:59], a[52:55], a[172:175], v[56:59]\n"
    "ds_read_b64_tr_b16 a[2:3], v218 offset:30720\n"
    "v_fma_f32 v16, v16, s36, v214\n"
    "v_mfma_f32_16x16x32_bf16 v[56:59], a[56:59], a[176:179], v[56:59]\n"
    "ds_read_b64_tr_b16 a[4:5], v219 offset:24576\n"
    "v_fma_f32 v17, v17, s36, v214\n"
    "v_mfma_f32_16x16x32_bf16 v[56:59], a[60:63], a[180:183], v[56:59]\n"
    "ds_read_b64_tr_b16 a[6:7], v219 offset:30720\n"
    "v_fma_f32 v18, v18, s36, v214\n"
    "v_mfma_f32_16x16x32_bf16 v[56:59], a[64:67], a[184:187], v[56:59]\n"
    "ds_read_b64_tr_b16 a[8:9], v220 offset:24576\n"
    "v_fma_f32 v19, v19, s36, v214\n"
    "v_mfma_f32_16x16x32_bf16 v[56:59], a[68:71], a[188:191], v[56:59]\n"
    "ds_read_b64_tr_b16 a[10:11], v220 offset:30720\n"
    "v_fma_f32 v20, v20, s36, v214\n"
    "v_mfma_f32_16x16x32_bf16 v[60:63], a[72:75], a[168:171], 0\n"
    "ds_read_b64_tr_b16 a[12:13], v221 offset:24576\n"
    "v_fma_f32 v21, v21, s36, v214\n"
    "v_mfma_f32_16x16x32_bf16 v[60:63], a[76:79], a[172:175], v[60:63]\n"
    "ds_read_b64_tr_b16 a[14:15], v221 offset:30720\n"
    "v_fma_f32 v22, v22, s36, v214\n"
    "v_mfma_f32_16x16x32_bf16 v[60:63], a[80:83], a[176:179], v[60:63]\n"
    "ds_read_b64_tr_b16 a[16:17], v218 offset:24704\n"
    "v_fma_f32 v23, v23, s36, v214\n"
    "v_mfma_f32_16x16x32_bf16 v[60:63], a[84:87], a[180:183], v[60:63]\n"
    "ds_read_b64_tr_b16 a[18:19], v218 offset:30848\n"
    "v_max3_f32 v188, v24, v25, v26\n"
    "v_mfma_f32_16x16x32_bf16 v[60:63], a[88:91], a[184:187], v[60:63]\n"
    "ds_read_b64_tr_b16 a[20:21], v219 offset:24704\n"
    "v_max3_f32 v188, v188, v27, v28\n"
    "v_mfma_f32_16x16x32_bf16 v[60:63], a[92:95], a[188:191], v[60:63]\n"
    "ds_read_b64_tr_b16 a[22:23], v219 offset:30848\n"
    "v_max3_f32 v188, v188, v29, v30\n"
    "ds_read_b64_tr_b16 a[24:25], v220 offset:24704\n"
    "v_max_f32 v188, v188, v31\n"
    "ds_read_b64_tr_b16 a[26:27], v220 offset:30848\n"
    "v_mov_b32 v189, v188\n"
    "ds_read_b64_tr_b16 a[28:29], v221 offset:24704\n"
    "v_fma_f32 v48, v48, s36, v214\n"
    "ds_read_b64_tr_b16 a[30:31], v221 offset:30848\n"
    "v_permlane16_swap_b32 v188, v189\n"
    "ds_read_b64_tr_b16 a[32:33], v218 offset:24832\n"
    "v_max_f32 v188, v188, v189\n"
    "ds_read_b64_tr_b16 a[34:35], v218 offset:30976\n"
    "v_mov_b32 v189, v188\n"
    "ds_read_b64_tr_b16 a[36:37], v219 offset:24832\n"
    "v_fma_f32 v49, v49, s36, v214\n"
    "ds_read_b64_tr_b16 a[38:39], v219 offset:30976\n"
    "v_permlane32_swap_b32 v188, v189\n"
    "ds_read_b64_tr_b16 a[40:41], v220 offset:24832\n"
    "v_max_f32 v188, v188, v189\n"
    "ds_read_b64_tr_b16 a[42:43], v220 offset:30976\n"
    "v_mul_f32 v215, s49, v188\n"
    "ds_read_b64_tr_b16 a[44:45], v221 offset:24832\n"
    "v_fma_f32 v24, v24, s36, v215\n"
    "ds_read_b64_tr_b16 a[46:47], v221 offset:30976\n"
    "v_fma_f32 v25, v25, s36, v215\n"
    "v_exp_f32 v16, v16\n"
    "v_exp_f32 v17, v17\n"
    "v_exp_f32 v18, v18\n"
    "s_waitcnt vmcnt(9) lgkmcnt(0)\n"
    "s_barrier\n"
    "s_add_u32 m0, s37, 24576\n"
    "s_add_u32 s42, s38, 49152\n"
    "L_ph3:\n"
    "v_mfma_f32_16x16x32_bf16 v[64:67], a[0:3], v[0:3], 0\n"
    "buffer_load_dwordx4 v222, s[20:23], s42 offen nt lds\n"
    "v_mfma_f32_16x16x32_bf16 v[80:83], a[4:7], v[0:3], 0\n"
    "ds_read_b128 a[48:51], v216 offset:36864\n"
    "v_cvt_pk_bf16_f32 v16, v16, v17\n"
    "v_mfma_f32_16x16x32_bf16 v[96:99], a[8:11], v[0:3], 0\n"
    "buffer_load_dwordx4 v223, s[20:23], s42 offen offset:1024 nt lds\n"
    "v_mfma_f32_16x16x32_bf16 v[112:115], a[12:15], v[0:3], 0\n"
    "ds_read_b128 a[52:55], v217 offset:36864\n"
    "v_fma_f32 v26, v26, s36, v215\n"
    "v_mfma_f32_16x16x32_bf16 v[128:131], a[16:19], v[0:3], 0\n"
    "buffer_load_dwordx4 v224, s[20:23], s42 offen offset:2048 nt lds\n"
    "v_mfma_f32_16x16x32_bf16 v[144:147], a[20:23], v[0:3], 0\n"
    "s_mov_b32 m0, 159744\n"
    "s_mov_b32 s51, L_ph7-L_code_start\n"
    "v_mfma_f32_16x16x32_bf16 v[160:163], a[24:27], v[0:3], 0\n"
    "buffer_load_dword v249, s[52:55], s51 offen lds\n"
    "v_mfma_f32_16x16x32_bf16 v[176:179], a[28:31], v[0:3], 0\n"
    "ds_read_b128 a[56:59], v216 offset:36992\n"
    "s_add_u32 m0, s37, 36864\n"
    "v_mfma_f32_16x16x32_bf16 a[192:195], a[32:35], v[0:3], 0\n"
    "ds_read_b128 a[60:63], v217 offset:36992\n"
    "s_add_u32 s42, s38, 36864\n"
    "v_mfma_f32_16x16x32_bf16 a[208:211], a[36:39], v[0:3], 0\n"
    "ds_read_b128 a[64:67], v216 offset:37120\n"
    "v_fma_f32 v27, v27, s36, v215\n"
    "v_mfma_f32_16x16x32_bf16 a[224:227], a[40:43], v[0:3], 0\n"
    "ds_read_b128 a[68:71], v217 offset:37120\n"
    "v_fma_f32 v28, v28, s36, v215\n"
    "v_mfma_f32_16x16x32_bf16 a[240:243], a[44:47], v[0:3], 0\n"
    "ds_read_b128 a[72:75], v216 offset:43008\n"
    "v_fma_f32 v29, v29, s36, v215\n"
    "v_mfma_f32_16x16x32_bf16 v[192:195], v[208:211], v[0:3], 0\n"
    "ds_read_b128 a[76:79], v217 offset:43008\n"
    "v_fma_f32 v30, v30, s36, v215\n"
    "v_mfma_f32_16x16x32_bf16 v[68:71], a[0:3], v[8:11], 0\n"
    "ds_read_b128 a[80:83], v216 offset:43136\n"
    "v_fma_f32 v31, v31, s36, v215\n"
    "v_mfma_f32_16x16x32_bf16 v[84:87], a[4:7], v[8:11], 0\n"
    "ds_read_b128 a[84:87], v217 offset:43136\n"
    "v_fma_f32 v50, v50, s36, v214\n"
    "v_mfma_f32_16x16x32_bf16 v[100:103], a[8:11], v[8:11], 0\n"
    "ds_read_b128 a[88:91], v216 offset:43264\n"
    "v_fma_f32 v51, v51, s36, v214\n"
    "v_mfma_f32_16x16x32_bf16 v[116:119], a[12:15], v[8:11], 0\n"
    "ds_read_b128 a[92:95], v217 offset:43264\n"
    "v_fma_f32 v52, v52, s36, v214\n"
    "v_mfma_f32_16x16x32_bf16 v[132:135], a[16:19], v[8:11], 0\n"
    "v_exp_f32 v19, v19\n"
    "v_mfma_f32_16x16x32_bf16 v[148:151], a[20:23], v[8:11], 0\n"
    "v_exp_f32 v20, v20\n"
    "v_mfma_f32_16x16x32_bf16 v[164:167], a[24:27], v[8:11], 0\n"
    "v_exp_f32 v21, v21\n"
    "v_mfma_f32_16x16x32_bf16 v[180:183], a[28:31], v[8:11], 0\n"
    "v_exp_f32 v22, v22\n"
    "v_mfma_f32_16x16x32_bf16 a[196:199], a[32:35], v[8:11], 0\n"
    "v_exp_f32 v23, v23\n"
    "v_mfma_f32_16x16x32_bf16 a[212:215], a[36:39], v[8:11], 0\n"
    "v_cvt_pk_bf16_f32 v17, v18, v19\n"
    "v_cvt_pk_bf16_f32 v18, v20, v21\n"
    "v_mfma_f32_16x16x32_bf16 a[228:231], a[40:43], v[8:11], 0\n"
    "v_cvt_pk_bf16_f32 v19, v22, v23\n"
    "v_fma_f32 v53, v53, s36, v214\n"
    "v_mfma_f32_16x16x32_bf16 a[244:247], a[44:47], v[8:11], 0\n"
    "v_exp_f32 v24, v24\n"
    "v_mfma_f32_16x16x32_bf16 v[196:199], v[208:211], v[8:11], 0\n"
    "v_exp_f32 v25, v25\n"
    "v_mfma_f32_16x16x32_bf16 v[72:75], a[0:3], v[16:19], 0\n"
    "v_exp_f32 v26, v26\n"
    "v_mfma_f32_16x16x32_bf16 v[88:91], a[4:7], v[16:19], 0\n"
    "v_exp_f32 v27, v27\n"
    "v_mfma_f32_16x16x32_bf16 v[104:107], a[8:11], v[16:19], 0\n"
    "v_exp_f32 v28, v28\n"
    "v_mfma_f32_16x16x32_bf16 v[120:123], a[12:15], v[16:19], 0\n"
    "v_exp_f32 v29, v29\n"
    "v_mfma_f32_16x16x32_bf16 v[136:139], a[16:19], v[16:19], 0\n"
    "v_exp_f32 v30, v30\n"
    "v_mfma_f32_16x16x32_bf16 v[152:155], a[20:23], v[16:19], 0\n"
    "v_exp_f32 v31, v31\n"
    "v_mfma_f32_16x16x32_bf16 v[168:171], a[24:27], v[16:19], 0\n"
    "v_cvt_pk_bf16_f32 v24, v24, v25\n"
    "v_cvt_pk_bf16_f32 v25, v26, v27\n"
    "v_mfma_f32_16x16x32_bf16 v[184:187], a[28:31], v[16:19], 0\n"
    "v_cvt_pk_bf16_f32 v26, v28, v29\n"
    "v_cvt_pk_bf16_f32 v27, v30, v31\n"
    "v_mfma_f32_16x16x32_bf16 a[200:203], a[32:35], v[16:19], 0\n"
    "v_exp_f32 v36, v36\n"
    "v_mfma_f32_16x16x32_bf16 a[216:219], a[36:39], v[16:19], 0\n"
    "v_exp_f32 v37, v37\n"
    "v_mfma_f32_16x16x32_bf16 a[232:235], a[40:43], v[16:19], 0\n"
    "v_exp_f32 v38, v38\n"
    "v_mfma_f32_16x16x32_bf16 a[248:251], a[44:47], v[16:19], 0\n"
    "v_exp_f32 v39, v39\n"
    "v_mfma_f32_16x16x32_bf16 v[200:203], v[208:211], v[16:19], 0\n"
    "v_cvt_pk_bf16_f32 v34, v36, v37\n"
    "v_cvt_pk_bf16_f32 v35, v38, v39\n"
    "v_mfma_f32_16x16x32_bf16 v[76:79], a[0:3], v[24:27], 0\n"
    "v_exp_f32 v40, v40\n"
    "v_mfma_f32_16x16x32_bf16 v[92:95], a[4:7], v[24:27], 0\n"
    "v_exp_f32 v41, v41\n"
    "v_mfma_f32_16x16x32_bf16 v[108:111], a[8:11], v[24:27], 0\n"
    "v_exp_f32 v42, v42\n"
    "v_mfma_f32_16x16x32_bf16 v[124:127], a[12:15], v[24:27], 0\n"
    "v_exp_f32 v43, v43\n"
    "v_mfma_f32_16x16x32_bf16 v[140:143], a[16:19], v[24:27], 0\n"
    "v_exp_f32 v44, v44\n"
    "v_mfma_f32_16x16x32_bf16 v[156:159], a[20:23], v[24:27], 0\n"
    "v_exp_f32 v45, v45\n"
    "v_mfma_f32_16x16x32_bf16 v[172:175], a[24:27], v[24:27], 0\n"
    "v_exp_f32 v46, v46\n"
    "v_mfma_f32_16x16x32_bf16 v[188:191], a[28:31], v[24:27], 0\n"
    "v_exp_f32 v47, v47\n"
    "v_mfma_f32_16x16x32_bf16 a[204:207], a[32:35], v[24:27], 0\n"
    "v_cvt_pk_bf16_f32 v40, v40, v41\n"
    "v_cvt_pk_bf16_f32 v41, v42, v43\n"
    "v_mfma_f32_16x16x32_bf16 a[220:223], a[36:39], v[24:27], 0\n"
    "v_cvt_pk_bf16_f32 v42, v44, v45\n"
    "v_cvt_pk_bf16_f32 v43, v46, v47\n"
    "v_mfma_f32_16x16x32_bf16 a[236:239], a[40:43], v[24:27], 0\n"
    "v_exp_f32 v48, v48\n"
    "v_mfma_f32_16x16x32_bf16 a[252:255], a[44:47], v[24:27], 0\n"
    "v_exp_f32 v49, v49\n"
    "v_mfma_f32_16x16x32_bf16 v[204:207], v[208:211], v[24:27], 0\n"
    "s_waitcnt vmcnt(10) lgkmcnt(0)\n"
    "s_barrier\n"
    "L_ph4:\n"
    "v_mfma_f32_16x16x32_bf16 v[0:3], a[48:51], a[96:99], 0\n"
    "buffer_load_dwordx4 v231, s[24:27], s42 offen nt lds\n"
    "v_mfma_f32_16x16x32_bf16 v[0:3], a[52:55], a[100:103], v[0:3]\n"
    "ds_read_b64_tr_b16 a[0:1], v218 offset:49152\n"
    "v_fma_f32 v54, v54, s36, v214\n"
    "v_mfma_f32_16x16x32_bf16 v[0:3], a[56:59], a[104:107], v[0:3]\n"
    "buffer_load_dwordx4 v232, s[24:27], s42 offen offset:1024 nt lds\n"
    "v_mfma_f32_16x16x32_bf16 v[0:3], a[60:63], a[108:111], v[0:3]\n"
    "ds_read_b64_tr_b16 a[2:3], v218 offset:55296\n"
    "v_fma_f32 v55, v55, s36, v214\n"
    "v_mfma_f32_16x16x32_bf16 v[0:3], a[64:67], a[112:115], v[0:3]\n"
    "buffer_load_dwordx4 v233, s[24:27], s42 offen offset:2048 nt lds\n"
    "v_mfma_f32_16x16x32_bf16 v[0:3], a[68:71], a[116:119], v[0:3]\n"
    "ds_read_b64_tr_b16 a[4:5], v219 offset:49152\n"
    "s_add_u32 m0, s37, 49152\n"
    "v_mfma_f32_16x16x32_bf16 v[4:7], a[72:75], a[96:99], 0\n"
    "ds_read_b64_tr_b16 a[6:7], v219 offset:55296\n"
    "s_add_u32 s42, s38, 61440\n"
    "v_mfma_f32_16x16x32_bf16 v[4:7], a[76:79], a[100:103], v[4:7]\n"
    "ds_read_b64_tr_b16 a[8:9], v220 offset:49152\n"
    "v_cvt_pk_bf16_f32 v48, v48, v49\n"
    "v_mfma_f32_16x16x32_bf16 v[4:7], a[80:83], a[104:107], v[4:7]\n"
    "ds_read_b64_tr_b16 a[10:11], v220 offset:55296\n"
    "v_fma_f32 v56, v56, s36, v215\n"
    "v_mfma_f32_16x16x32_bf16 v[4:7], a[84:87], a[108:111], v[4:7]\n"
    "ds_read_b64_tr_b16 a[12:13], v221 offset:49152\n"
    "v_fma_f32 v57, v57, s36, v215\n"
    "v_mfma_f32_16x16x32_bf16 v[4:7], a[88:91], a[112:115], v[4:7]\n"
    "ds_read_b64_tr_b16 a[14:15], v221 offset:55296\n"
    "v_fma_f32 v58, v58, s36, v215\n"
    "v_mfma_f32_16x16x32_bf16 v[4:7], a[92:95], a[116:119], v[4:7]\n"
    "ds_read_b64_tr_b16 a[16:17], v218 offset:49280\n"
    "v_fma_f32 v59, v59, s36, v215\n"
    "v_mfma_f32_16x16x32_bf16 v[8:11], a[48:51], a[120:123], 0\n"
    "ds_read_b64_tr_b16 a[18:19], v218 offset:55424\n"
    "v_fma_f32 v60, v60, s36, v215\n"
    "v_mfma_f32_16x16x32_bf16 v[8:11], a[52:55], a[124:127], v[8:11]\n"
    "ds_read_b64_tr_b16 a[20:21], v219 offset:49280\n"
    "v_fma_f32 v61, v61, s36, v215\n"
    "v_mfma_f32_16x16x32_bf16 v[8:11], a[56:59], a[128:131], v[8:11]\n"
    "ds_read_b64_tr_b16 a[22:23], v219 offset:55424\n"
    "v_fma_f32 v62, v62, s36, v215\n"
    "v_mfma_f32_16x16x32_bf16 v[8:11], a[60:63], a[132:135], v[8:11]\n"
    "ds_read_b64_tr_b16 a[24:25], v220 offset:49280\n"
    "v_fma_f32 v63, v63, s36, v215\n"
    "v_mfma_f32_16x16x32_bf16 v[8:11], a[64:67], a[136:139], v[8:11]\n"
    "ds_read_b64_tr_b16 a[26:27], v220 offset:55424\n"
    "v_fma_f32 v0, v0, s36, v212\n"
    "v_mfma_f32_16x16x32_bf16 v[8:11], a[68:71], a[140:143], v[8:11]\n"
    "ds_read_b64_tr_b16 a[28:29], v221 offset:49280\n"
    "v_fma_f32 v1, v1, s36, v212\n"
    "v_mfma_f32_16x16x32_bf16 v[12:15], a[72:75], a[120:123], 0\n"
    "ds_read_b64_tr_b16 a[30:31], v221 offset:55424\n"
    "v_fma_f32 v2, v2, s36, v212\n"
    "v_mfma_f32_16x16x32_bf16 v[12:15], a[76:79], a[124:127], v[12:15]\n"
    "ds_read_b64_tr_b16 a[32:33], v218 offset:49408\n"
    "v_fma_f32 v3, v3, s36, v212\n"
    "v_mfma_f32_16x16x32_bf16 v[12:15], a[80:83], a[128:131], v[12:15]\n"
    "ds_read_b64_tr_b16 a[34:35], v218 offset:55552\n"
    "v_fma_f32 v4, v4, s36, v212\n"
    "v_mfma_f32_16x16x32_bf16 v[12:15], a[84:87], a[132:135], v[12:15]\n"
    "ds_read_b64_tr_b16 a[36:37], v219 offset:49408\n"
    "v_fma_f32 v5, v5, s36, v212\n"
    "v_mfma_f32_16x16x32_bf16 v[12:15], a[88:91], a[136:139], v[12:15]\n"
    "ds_read_b64_tr_b16 a[38:39], v219 offset:55552\n"
    "v_fma_f32 v6, v6, s36, v212\n"
    "v_mfma_f32_16x16x32_bf16 v[12:15], a[92:95], a[140:143], v[12:15]\n"
    "ds_read_b64_tr_b16 a[40:41], v220 offset:49408\n"
    "v_fma_f32 v7, v7, s36, v212\n"
    "v_mfma_f32_16x16x32_bf16 v[16:19], a[48:51], a[144:147], 0\n"
    "ds_read_b64_tr_b16 a[42:43], v220 offset:55552\n"
    "v_mfma_f32_16x16x32_bf16 v[16:19], a[52:55], a[148:151], v[16:19]\n"
    "ds_read_b64_tr_b16 a[44:45], v221 offset:49408\n"
    "v_fma_f32 v8, v8, s36, v213\n"
    "v_mfma_f32_16x16x32_bf16 v[16:19], a[56:59], a[152:155], v[16:19]\n"
    "ds_read_b64_tr_b16 a[46:47], v221 offset:55552\n"
    "v_fma_f32 v9, v9, s36, v213\n"
    "v_mfma_f32_16x16x32_bf16 v[16:19], a[60:63], a[156:159], v[16:19]\n"
    "v_exp_f32 v50, v50\n"
    "v_mfma_f32_16x16x32_bf16 v[16:19], a[64:67], a[160:163], v[16:19]\n"
    "v_exp_f32 v51, v51\n"
    "v_mfma_f32_16x16x32_bf16 v[16:19], a[68:71], a[164:167], v[16:19]\n"
    "v_exp_f32 v52, v52\n"
    "v_mfma_f32_16x16x32_bf16 v[20:23], a[72:75], a[144:147], 0\n"
    "v_exp_f32 v53, v53\n"
    "v_mfma_f32_16x16x32_bf16 v[20:23], a[76:79], a[148:151], v[20:23]\n"
    "v_exp_f32 v54, v54\n"
    "v_mfma_f32_16x16x32_bf16 v[20:23], a[80:83], a[152:155], v[20:23]\n"
    "v_exp_f32 v55, v55\n"
    "v_mfma_f32_16x16x32_bf16 v[20:23], a[84:87], a[156:159], v[20:23]\n"
    "v_cvt_pk_bf16_f32 v49, v50, v51\n"
    "v_cvt_pk_bf16_f32 v50, v52, v53\n"
    "v_mfma_f32_16x16x32_bf16 v[20:23], a[88:91], a[160:163], v[20:23]\n"
    "v_cvt_pk_bf16_f32 v51, v54, v55\n"
    "v_fma_f32 v10, v10, s36, v213\n"
    "v_mfma_f32_16x16x32_bf16 v[20:23], a[92:95], a[164:167], v[20:23]\n"
    "v_exp_f32 v56, v56\n"
    "v_mfma_f32_16x16x32_bf16 v[24:27], a[48:51], a[168:171], 0\n"
    "v_exp_f32 v57, v57\n"
    "v_mfma_f32_16x16x32_bf16 v[24:27], a[52:55], a[172:175], v[24:27]\n"
    "v_exp_f32 v58, v58\n"
    "v_mfma_f32_16x16x32_bf16 v[24:27], a[56:59], a[176:179], v[24:27]\n"
    "v_exp_f32 v59, v59\n"
    "v_mfma_f32_16x16x32_bf16 v[24:27], a[60:63], a[180:183], v[24:27]\n"
    "v_exp_f32 v60, v60\n"
    "v_mfma_f32_16x16x32_bf16 v[24:27], a[64:67], a[184:187], v[24:27]\n"
    "v_exp_f32 v61, v61\n"
    "v_mfma_f32_16x16x32_bf16 v[24:27], a[68:71], a[188:191], v[24:27]\n"
    "v_exp_f32 v62, v62\n"
    "v_mfma_f32_16x16x32_bf16 v[28:31], a[72:75], a[168:171], 0\n"
    "v_exp_f32 v63, v63\n"
    "v_mfma_f32_16x16x32_bf16 v[28:31], a[76:79], a[172:175], v[28:31]\n"
    "v_cvt_pk_bf16_f32 v56, v56, v57\n"
    "v_cvt_pk_bf16_f32 v57, v58, v59\n"
    "v_mfma_f32_16x16x32_bf16 v[28:31], a[80:83], a[176:179], v[28:31]\n"
    "v_cvt_pk_bf16_f32 v58, v60, v61\n"
    "v_cvt_pk_bf16_f32 v59, v62, v63\n"
    "v_mfma_f32_16x16x32_bf16 v[28:31], a[84:87], a[180:183], v[28:31]\n"
    "v_exp_f32 v0, v0\n"
    "v_mfma_f32_16x16x32_bf16 v[28:31], a[88:91], a[184:187], v[28:31]\n"
    "v_exp_f32 v1, v1\n"
    "v_mfma_f32_16x16x32_bf16 v[28:31], a[92:95], a[188:191], v[28:31]\n"
    "s_waitcnt vmcnt(10) lgkmcnt(0)\n"
    "s_barrier\n"
    "L_ph5:\n"
    "v_mfma_f32_16x16x32_bf16 v[64:67], a[0:3], v[32:35], v[64:67]\n"
    "buffer_load_dwordx4 v222, s[20:23], s42 offen nt lds\n"
    "v_mfma_f32_16x16x32_bf16 v[80:83], a[4:7], v[32:35], v[80:83]\n"
    "ds_read_b128 a[48:51], v216 offset:0\n"
    "v_cvt_pk_bf16_f32 v0, v0, v1\n"
    "v_mfma_f32_16x16x32_bf16 v[96:99], a[8:11], v[32:35], v[96:99]\n"
    "buffer_load_dwordx4 v223, s[20:23], s42 offen offset:1024 nt lds\n"
    "v_mfma_f32_16x16x32_bf16 v[112:115], a[12:15], v[32:35], v[112:115]\n"
    "ds_read_b128 a[52:55], v217 offset:0\n"
    "v_fma_f32 v11, v11, s36, v213\n"
    "v_mfma_f32_16x16x32_bf16 v[128:131], a[16:19], v[32:35], v[128:131]\n"
    "buffer_load_dwordx4 v224, s[20:23], s42 offen offset:2048 nt lds\n"
    "v_mfma_f32_16x16x32_bf16 v[144:147], a[20:23], v[32:35], v[144:147]\n"
    "s_add_u32 m0, s44, 0\n"
    "s_add_u32 s43, s41, 0\n"
    "v_mfma_f32_16x16x32_bf16 v[160:163], a[24:27], v[32:35], v[160:163]\n"
    "buffer_load_dwordx4 v225, s[16:19], s43 offen nt lds\n"
    "v_mfma_f32_16x16x32_bf16 v[176:179], a[28:31], v[32:35], v[176:179]\n"
    "ds_read_b128 a[56:59], v216 offset:128\n"
    "s_add_u32 m0, s37, 0\n"
    "v_mfma_f32_16x16x32_bf16 a[192:195], a[32:35], v[32:35], a[192:195]\n"
    "ds_read_b128 a[60:63], v217 offset:128\n"
    "s_add_u32 s42, s38, 49152\n"
    "v_mfma_f32_16x16x32_bf16 a[208:211], a[36:39], v[32:35], a[208:211]\n"
    "ds_read_b128 a[64:67], v216 offset:256\n"
    "v_fma_f32 v12, v12, s36, v213\n"
    "v_mfma_f32_16x16x32_bf16 a[224:227], a[40:43], v[32:35], a[224:227]\n"
    "ds_read_b128 a[68:71], v217 offset:256\n"
    "v_fma_f32 v13, v13, s36, v213\n"
    "v_mfma_f32_16x16x32_bf16 a[240:243], a[44:47], v[32:35], a[240:243]\n"
    "ds_read_b128 a[72:75], v216 offset:6144\n"
    "v_fma_f32 v14, v14, s36, v213\n"
    "v_mfma_f32_16x16x32_bf16 v[192:195], v[208:211], v[32:35], v[192:195]\n"
    "ds_read_b128 a[76:79], v217 offset:6144\n"
    "v_fma_f32 v15, v15, s36, v213\n"
    "v_mfma_f32_16x16x32_bf16 v[68:71], a[0:3], v[40:43], v[68:71]\n"
    "ds_read_b128 a[80:83], v216 offset:6272\n"
    "v_fma_f32 v16, v16, s36, v214\n"
    "v_mfma_f32_16x16x32_bf16 v[84:87], a[4:7], v[40:43], v[84:87]\n"
    "ds_read_b128 a[84:87], v217 offset:6272\n"
    "v_fma_f32 v17, v17, s36, v214\n"
    "v_mfma_f32_16x16x32_bf16 v[100:103], a[8:11], v[40:43], v[100:103]\n"
    "ds_read_b128 a[88:91], v216 offset:6400\n"
    "v_fma_f32 v18, v18, s36, v214\n"
    "v_mfma_f32_16x16x32_bf16 v[116:119], a[12:15], v[40:43], v[116:119]\n"
    "ds_read_b128 a[92:95], v217 offset:6400\n"
    "v_fma_f32 v19, v19, s36, v214\n"
    "v_mfma_f32_16x16x32_bf16 v[132:135], a[16:19], v[40:43], v[132:135]\n"
    "v_exp_f32 v2, v2\n"
    "v_mfma_f32_16x16x32_bf16 v[148:151], a[20:23], v[40:43], v[148:151]\n"
    "v_exp_f32 v3, v3\n"
    "v_mfma_f32_16x16x32_bf16 v[164:167], a[24:27], v[40:43], v[164:167]\n"
    "v_exp_f32 v4, v4\n"
    "v_mfma_f32_16x16x32_bf16 v[180:183], a[28:31], v[40:43], v[180:183]\n"
    "v_exp_f32 v5, v5\n"
    "v_mfma_f32_16x16x32_bf16 a[196:199], a[32:35], v[40:43], a[196:199]\n"
    "v_exp_f32 v6, v6\n"
    "v_mfma_f32_16x16x32_bf16 a[212:215], a[36:39], v[40:43], a[212:215]\n"
    "v_exp_f32 v7, v7\n"
    "v_mfma_f32_16x16x32_bf16 a[228:231], a[40:43], v[40:43], a[228:231]\n"
    "v_cvt_pk_bf16_f32 v1, v2, v3\n"
    "v_cvt_pk_bf16_f32 v2, v4, v5\n"
    "v_mfma_f32_16x16x32_bf16 a[244:247], a[44:47], v[40:43], a[244:247]\n"
    "v_cvt_pk_bf16_f32 v3, v6, v7\n"
    "v_fma_f32 v20, v20, s36, v214\n"
    "v_mfma_f32_16x16x32_bf16 v[196:199], v[208:211], v[40:43], v[196:199]\n"
    "v_exp_f32 v8, v8\n"
    "v_mfma_f32_16x16x32_bf16 v[72:75], a[0:3], v[48:51], v[72:75]\n"
    "v_exp_f32 v9, v9\n"
    "v_mfma_f32_16x16x32_bf16 v[88:91], a[4:7], v[48:51], v[88:91]\n"
    "v_exp_f32 v10, v10\n"
    "v_mfma_f32_16x16x32_bf16 v[104:107], a[8:11], v[48:51], v[104:107]\n"
    "v_exp_f32 v11, v11\n"
    "v_mfma_f32_16x16x32_bf16 v[120:123], a[12:15], v[48:51], v[120:123]\n"
    "v_exp_f32 v12, v12\n"
    "v_mfma_f32_16x16x32_bf16 v[136:139], a[16:19], v[48:51], v[136:139]\n"
    "v_exp_f32 v13, v13\n"
    "v_mfma_f32_16x16x32_bf16 v[152:155], a[20:23], v[48:51], v[152:155]\n"
    "v_exp_f32 v14, v14\n"
    "v_mfma_f32_16x16x32_bf16 v[168:171], a[24:27], v[48:51], v[168:171]\n"
    "v_exp_f32 v15, v15\n"
    "v_mfma_f32_16x16x32_bf16 v[184:187], a[28:31], v[48:51], v[184:187]\n"
    "v_cvt_pk_bf16_f32 v8, v8, v9\n"
    "v_cvt_pk_bf16_f32 v9, v10, v11\n"
    "v_mfma_f32_16x16x32_bf16 a[200:203], a[32:35], v[48:51], a[200:203]\n"
    "v_cvt_pk_bf16_f32 v10, v12, v13\n"
    "v_cvt_pk_bf16_f32 v11, v14, v15\n"
    "v_mfma_f32_16x16x32_bf16 a[216:219], a[36:39], v[48:51], a[216:219]\n"
    "v_exp_f32 v16, v16\n"
    "v_mfma_f32_16x16x32_bf16 a[232:235], a[40:43], v[48:51], a[232:235]\n"
    "v_exp_f32 v17, v17\n"
    "v_mfma_f32_16x16x32_bf16 a[248:251], a[44:47], v[48:51], a[248:251]\n"
    "v_exp_f32 v18, v18\n"
    "v_mfma_f32_16x16x32_bf16 v[200:203], v[208:211], v[48:51], v[200:203]\n"
    "v_exp_f32 v19, v19\n"
    "v_mfma_f32_16x16x32_bf16 v[76:79], a[0:3], v[56:59], v[76:79]\n"
    "v_exp_f32 v20, v20\n"
    "v_mfma_f32_16x16x32_bf16 v[92:95], a[4:7], v[56:59], v[92:95]\n"
    "v_fma_f32 v21, v21, s36, v214\n"
    "v_fma_f32 v22, v22, s36, v214\n"
    "v_mfma_f32_16x16x32_bf16 v[108:111], a[8:11], v[56:59], v[108:111]\n"
    "v_exp_f32 v21, v21\n"
    "v_mfma_f32_16x16x32_bf16 v[124:127], a[12:15], v[56:59], v[124:127]\n"
    "v_exp_f32 v22, v22\n"
    "v_mfma_f32_16x16x32_bf16 v[140:143], a[16:19], v[56:59], v[140:143]\n"
    "v_fma_f32 v23, v23, s36, v214\n"
    "v_cvt_pk_bf16_f32 v16, v16, v17\n"
    "v_mfma_f32_16x16x32_bf16 v[156:159], a[20:23], v[56:59], v[156:159]\n"
    "v_exp_f32 v23, v23\n"
    "v_mfma_f32_16x16x32_bf16 v[172:175], a[24:27], v[56:59], v[172:175]\n"
    "v_cvt_pk_bf16_f32 v17, v18, v19\n"
    "v_cvt_pk_bf16_f32 v18, v20, v21\n"
    "v_mfma_f32_16x16x32_bf16 v[188:191], a[28:31], v[56:59], v[188:191]\n"
    "v_cvt_pk_bf16_f32 v19, v22, v23\n"
    "v_fma_f32 v24, v24, s36, v215\n"
    "v_mfma_f32_16x16x32_bf16 a[204:207], a[32:35], v[56:59], a[204:207]\n"
    "v_exp_f32 v24, v24\n"
    "v_mfma_f32_16x16x32_bf16 a[220:223], a[36:39], v[56:59], a[220:223]\n"
    "v_fma_f32 v25, v25, s36, v215\n"
    "v_fma_f32 v26, v26, s36, v215\n"
    "v_mfma_f32_16x16x32_bf16 a[236:239], a[40:43], v[56:59], a[236:239]\n"
    "v_exp_f32 v25, v25\n"
    "v_mfma_f32_16x16x32_bf16 a[252:255], a[44:47], v[56:59], a[252:255]\n"
    "v_exp_f32 v26, v26\n"
    "v_mfma_f32_16x16x32_bf16 v[204:207], v[208:211], v[56:59], v[204:207]\n"
    "s_waitcnt vmcnt(11) lgkmcnt(0)\n"
    "s_barrier\n"
    "L_ph6:\n"
    "v_mfma_f32_16x16x32_bf16 v[32:35], a[48:51], a[96:99], 0\n"
    "buffer_load_dwordx4 v231, s[24:27], s42 offen nt lds\n"
    "v_mfma_f32_16x16x32_bf16 v[32:35], a[52:55], a[100:103], v[32:35]\n"
    "ds_read_b64_tr_b16 a[0:1], v218 offset:12288\n"
    "v_fma_f32 v27, v27, s36, v215\n"
    "v_mfma_f32_16x16x32_bf16 v[32:35], a[56:59], a[104:107], v[32:35]\n"
    "buffer_load_dwordx4 v232, s[24:27], s42 offen offset:1024 nt lds\n"
    "v_mfma_f32_16x16x32_bf16 v[32:35], a[60:63], a[108:111], v[32:35]\n"
    "ds_read_b64_tr_b16 a[2:3], v218 offset:18432\n"
    "v_fma_f32 v28, v28, s36, v215\n"
    "v_mfma_f32_16x16x32_bf16 v[32:35], a[64:67], a[112:115], v[32:35]\n"
    "buffer_load_dwordx4 v233, s[24:27], s42 offen offset:2048 nt lds\n"
    "v_mfma_f32_16x16x32_bf16 v[32:35], a[68:71], a[116:119], v[32:35]\n"
    "s_add_u32 m0, s44, 0\n"
    "s_add_u32 s43, s41, 0\n"
    "v_mfma_f32_16x16x32_bf16 v[36:39], a[72:75], a[96:99], 0\n"
    "buffer_load_dwordx4 v226, s[16:19], s43 offen offset:1024 nt lds\n"
    "v_mfma_f32_16x16x32_bf16 v[36:39], a[76:79], a[100:103], v[36:39]\n"
    "ds_read_b64_tr_b16 a[4:5], v219 offset:12288\n"
    "s_add_u32 m0, s37, 12288\n"
    "v_mfma_f32_16x16x32_bf16 v[36:39], a[80:83], a[104:107], v[36:39]\n"
    "ds_read_b64_tr_b16 a[6:7], v219 offset:18432\n"
    "s_add_u32 s42, s38, 73728\n"
    "v_mfma_f32_16x16x32_bf16 v[36:39], a[84:87], a[108:111], v[36:39]\n"
    "ds_read_b64_tr_b16 a[8:9], v220 offset:12288\n"
    "v_fma_f32 v29, v29, s36, v215\n"
    "v_mfma_f32_16x16x32_bf16 v[36:39], a[88:91], a[112:115], v[36:39]\n"
    "ds_read_b64_tr_b16 a[10:11], v220 offset:18432\n"
    "v_fma_f32 v30, v30, s36, v215\n"
    "v_mfma_f32_16x16x32_bf16 v[36:39], a[92:95], a[116:119], v[36:39]\n"
    "ds_read_b64_tr_b16 a[12:13], v221 offset:12288\n"
    "v_fma_f32 v31, v31, s36, v215\n"
    "v_mfma_f32_16x16x32_bf16 v[40:43], a[48:51], a[120:123], 0\n"
    "ds_read_b64_tr_b16 a[14:15], v221 offset:18432\n"
    "v_cvt_pk_bf16_f32 v24, v24, v25\n"
    "v_mfma_f32_16x16x32_bf16 v[40:43], a[52:55], a[124:127], v[40:43]\n"
    "ds_read_b64_tr_b16 a[16:17], v218 offset:12416\n"
    "v_fma_f32 v32, v32, s36, v212\n"
    "v_mfma_f32_16x16x32_bf16 v[40:43], a[56:59], a[128:131], v[40:43]\n"
    "ds_read_b64_tr_b16 a[18:19], v218 offset:18560\n"
    "v_fma_f32 v33, v33, s36, v212\n"
    "v_mfma_f32_16x16x32_bf16 v[40:43], a[60:63], a[132:135], v[40:43]\n"
    "ds_read_b64_tr_b16 a[20:21], v219 offset:12416\n"
    "v_fma_f32 v34, v34, s36, v212\n"
    "v_mfma_f32_16x16x32_bf16 v[40:43], a[64:67], a[136:139], v[40:43]\n"
    "ds_read_b64_tr_b16 a[22:23], v219 offset:18560\n"
    "v_fma_f32 v35, v35, s36, v212\n"
    "v_mfma_f32_16x16x32_bf16 v[40:43], a[68:71], a[140:143], v[40:43]\n"
    "ds_read_b64_tr_b16 a[24:25], v220 offset:12416\n"
    "v_mfma_f32_16x16x32_bf16 v[44:47], a[72:75], a[120:123], 0\n"
    "ds_read_b64_tr_b16 a[26:27], v220 offset:18560\n"
    "v_mfma_f32_16x16x32_bf16 v[44:47], a[76:79], a[124:127], v[44:47]\n"
    "ds_read_b64_tr_b16 a[28:29], v221 offset:12416\n"
    "v_fma_f32 v36, v36, s36, v212\n"
    "v_mfma_f32_16x16x32_bf16 v[44:47], a[80:83], a[128:131], v[44:47]\n"
    "ds_read_b64_tr_b16 a[30:31], v221 offset:18560\n"
    "v_fma_f32 v37, v37, s36, v212\n"
    "v_mfma_f32_16x16x32_bf16 v[44:47], a[84:87], a[132:135], v[44:47]\n"
    "ds_read_b64_tr_b16 a[32:33], v218 offset:12544\n"
    "v_fma_f32 v38, v38, s36, v212\n"
    "v_mfma_f32_16x16x32_bf16 v[44:47], a[88:91], a[136:139], v[44:47]\n"
    "ds_read_b64_tr_b16 a[34:35], v218 offset:18688\n"
    "v_fma_f32 v39, v39, s36, v212\n"
    "v_mfma_f32_16x16x32_bf16 v[44:47], a[92:95], a[140:143], v[44:47]\n"
    "ds_read_b64_tr_b16 a[36:37], v219 offset:12544\n"
    "v_mfma_f32_16x16x32_bf16 v[48:51], a[48:51], a[144:147], 0\n"
    "ds_read_b64_tr_b16 a[38:39], v219 offset:18688\n"
    "v_mfma_f32_16x16x32_bf16 v[48:51], a[52:55], a[148:151], v[48:51]\n"
    "ds_read_b64_tr_b16 a[40:41], v220 offset:12544\n"
    "v_fma_f32 v40, v40, s36, v213\n"
    "v_mfma_f32_16x16x32_bf16 v[48:51], a[56:59], a[152:155], v[48:51]\n"
    "ds_read_b64_tr_b16 a[42:43], v220 offset:18688\n"
    "v_fma_f32 v41, v41, s36, v213\n"
    "v_mfma_f32_16x16x32_bf16 v[48:51], a[60:63], a[156:159], v[48:51]\n"
    "ds_read_b64_tr_b16 a[44:45], v221 offset:12544\n"
    "v_fma_f32 v42, v42, s36, v213\n"
    "v_mfma_f32_16x16x32_bf16 v[48:51], a[64:67], a[160:163], v[48:51]\n"
    "ds_read_b64_tr_b16 a[46:47], v221 offset:18688\n"
    "v_fma_f32 v43, v43, s36, v213\n"
    "v_mfma_f32_16x16x32_bf16 v[48:51], a[68:71], a[164:167], v[48:51]\n"
    "v_exp_f32 v27, v27\n"
    "v_mfma_f32_16x16x32_bf16 v[52:55], a[72:75], a[144:147], 0\n"
    "v_exp_f32 v28, v28\n"
    "v_mfma_f32_16x16x32_bf16 v[52:55], a[76:79], a[148:151], v[52:55]\n"
    "v_exp_f32 v29, v29\n"
    "v_mfma_f32_16x16x32_bf16 v[52:55], a[80:83], a[152:155], v[52:55]\n"
    "v_exp_f32 v30, v30\n"
    "v_mfma_f32_16x16x32_bf16 v[52:55], a[84:87], a[156:159], v[52:55]\n"
    "v_exp_f32 v31, v31\n"
    "v_mfma_f32_16x16x32_bf16 v[52:55], a[88:91], a[160:163], v[52:55]\n"
    "v_cvt_pk_bf16_f32 v25, v26, v27\n"
    "v_cvt_pk_bf16_f32 v26, v28, v29\n"
    "v_mfma_f32_16x16x32_bf16 v[52:55], a[92:95], a[164:167], v[52:55]\n"
    "v_cvt_pk_bf16_f32 v27, v30, v31\n"
    "v_fma_f32 v44, v44, s36, v213\n"
    "v_mfma_f32_16x16x32_bf16 v[56:59], a[48:51], a[168:171], 0\n"
    "v_exp_f32 v32, v32\n"
    "v_mfma_f32_16x16x32_bf16 v[56:59], a[52:55], a[172:175], v[56:59]\n"
    "v_exp_f32 v33, v33\n"
    "v_mfma_f32_16x16x32_bf16 v[56:59], a[56:59], a[176:179], v[56:59]\n"
    "v_exp_f32 v34, v34\n"
    "v_mfma_f32_16x16x32_bf16 v[56:59], a[60:63], a[180:183], v[56:59]\n"
    "v_exp_f32 v35, v35\n"
    "v_mfma_f32_16x16x32_bf16 v[56:59], a[64:67], a[184:187], v[56:59]\n"
    "v_exp_f32 v36, v36\n"
    "v_mfma_f32_16x16x32_bf16 v[56:59], a[68:71], a[188:191], v[56:59]\n"
    "v_exp_f32 v37, v37\n"
    "v_mfma_f32_16x16x32_bf16 v[60:63], a[72:75], a[168:171], 0\n"
    "v_exp_f32 v38, v38\n"
    "v_mfma_f32_16x16x32_bf16 v[60:63], a[76:79], a[172:175], v[60:63]\n"
    "v_exp_f32 v39, v39\n"
    "v_mfma_f32_16x16x32_bf16 v[60:63], a[80:83], a[176:179], v[60:63]\n"
    "v_cvt_pk_bf16_f32 v32, v32, v33\n"
    "v_cvt_pk_bf16_f32 v33, v34, v35\n"
    "v_mfma_f32_16x16x32_bf16 v[60:63], a[84:87], a[180:183], v[60:63]\n"
    "v_cvt_pk_bf16_f32 v34, v36, v37\n"
    "v_cvt_pk_bf16_f32 v35, v38, v39\n"
    "v_mfma_f32_16x16x32_bf16 v[60:63], a[88:91], a[184:187], v[60:63]\n"
    "v_exp_f32 v40, v40\n"
    "v_mfma_f32_16x16x32_bf16 v[60:63], a[92:95], a[188:191], v[60:63]\n"
    "s_waitcnt vmcnt(12) lgkmcnt(0)\n"
    "s_barrier\n"
    "L_ph7:\n"
    "v_mfma_f32_16x16x32_bf16 v[64:67], a[0:3], v[0:3], v[64:67]\n"
    "buffer_load_dwordx4 v222, s[20:23], s42 offen nt lds\n"
    "v_mfma_f32_16x16x32_bf16 v[80:83], a[4:7], v[0:3], v[80:83]\n"
    "ds_read_b128 a[48:51], v216 offset:24576\n"
    "v_fma_f32 v45, v45, s36, v213\n"
    "v_mfma_f32_16x16x32_bf16 v[96:99], a[8:11], v[0:3], v[96:99]\n"
    "buffer_load_dwordx4 v223, s[20:23], s42 offen offset:1024 nt lds\n"
    "v_mfma_f32_16x16x32_bf16 v[112:115], a[12:15], v[0:3], v[112:115]\n"
    "ds_read_b128 a[52:55], v217 offset:24576\n"
    "v_fma_f32 v46, v46, s36, v213\n"
    "v_mfma_f32_16x16x32_bf16 v[128:131], a[16:19], v[0:3], v[128:131]\n"
    "buffer_load_dwordx4 v224, s[20:23], s42 offen offset:2048 nt lds\n"
    "v_mfma_f32_16x16x32_bf16 v[144:147], a[20:23], v[0:3], v[144:147]\n"
    "s_add_u32 m0, s44, 0\n"
    "s_add_u32 s43, s41, 0\n"
    "v_mfma_f32_16x16x32_bf16 v[160:163], a[24:27], v[0:3], v[160:163]\n"
    "buffer_load_dwordx4 v227, s[16:19], s43 offen offset:2048 nt lds\n"
    "v_mfma_f32_16x16x32_bf16 v[176:179], a[28:31], v[0:3], v[176:179]\n"
    "s_mov_b32 m0, 159744\n"
    "s_mov_b32 s51, L_ph11-L_code_start\n"
    "v_mfma_f32_16x16x32_bf16 a[192:195], a[32:35], v[0:3], a[192:195]\n"
    "buffer_load_dword v249, s[52:55], s51 offen lds\n"
    "v_mfma_f32_16x16x32_bf16 a[208:211], a[36:39], v[0:3], a[208:211]\n"
    "ds_read_b128 a[56:59], v216 offset:24704\n"
    "s_add_u32 m0, s37, 24576\n"
    "v_mfma_f32_16x16x32_bf16 a[224:227], a[40:43], v[0:3], a[224:227]\n"
    "ds_read_b128 a[60:63], v217 offset:24704\n"
    "s_add_u32 s42, s38, 61440\n"
    "v_mfma_f32_16x16x32_bf16 a[240:243], a[44:47], v[0:3], a[240:243]\n"
    "ds_read_b128 a[64:67], v216 offset:24832\n"
    "v_fma_f32 v47, v47, s36, v213\n"
    "v_mfma_f32_16x16x32_bf16 v[192:195], v[208:211], v[0:3], v[192:195]\n"
    "ds_read_b128 a[68:71], v217 offset:24832\n"
    "v_fma_f32 v48, v48, s36, v214\n"
    "v_mfma_f32_16x16x32_bf16 v[68:71], a[0:3], v[8:11], v[68:71]\n"
    "ds_read_b128 a[72:75], v216 offset:30720\n"
    "v_fma_f32 v49, v49, s36, v214\n"
    "v_mfma_f32_16x16x32_bf16 v[84:87], a[4:7], v[8:11], v[84:87]\n"
    "ds_read_b128 a[76:79], v217 offset:30720\n"
    "v_fma_f32 v50, v50, s36, v214\n"
    "v_mfma_f32_16x16x32_bf16 v[100:103], a[8:11], v[8:11], v[100:103]\n"
    "ds_read_b128 a[80:83], v216 offset:30848\n"
    "v_fma_f32 v51, v51, s36, v214\n"
    "v_mfma_f32_16x16x32_bf16 v[116:119], a[12:15], v[8:11], v[116:119]\n"
    "ds_read_b128 a[84:87], v217 offset:30848\n"
    "v_fma_f32 v52, v52, s36, v214\n"
    "v_mfma_f32_16x16x32_bf16 v[132:135], a[16:19], v[8:11], v[132:135]\n"
    "ds_read_b128 a[88:91], v216 offset:30976\n"
    "v_fma_f32 v53, v53, s36, v214\n"
    "v_mfma_f32_16x16x32_bf16 v[148:151], a[20:23], v[8:11], v[148:151]\n"
    "ds_read_b128 a[92:95], v217 offset:30976\n"
    "v_fma_f32 v54, v54, s36, v214\n"
    "v_mfma_f32_16x16x32_bf16 v[164:167], a[24:27], v[8:11], v[164:167]\n"
    "v_exp_f32 v41, v41\n"
    "v_mfma_f32_16x16x32_bf16 v[180:183], a[28:31], v[8:11], v[180:183]\n"
    "v_exp_f32 v42, v42\n"
    "v_mfma_f32_16x16x32_bf16 a[196:199], a[32:35], v[8:11], a[196:199]\n"
    "v_exp_f32 v43, v43\n"
    "v_mfma_f32_16x16x32_bf16 a[212:215], a[36:39], v[8:11], a[212:215]\n"
    "v_exp_f32 v44, v44\n"
    "v_mfma_f32_16x16x32_bf16 a[228:231], a[40:43], v[8:11], a[228:231]\n"
    "v_exp_f32 v45, v45\n"
    "v_mfma_f32_16x16x32_bf16 a[244:247], a[44:47], v[8:11], a[244:247]\n"
    "v_exp_f32 v46, v46\n"
    "v_mfma_f32_16x16x32_bf16 v[196:199], v[208:211], v[8:11], v[196:199]\n"
    "v_exp_f32 v47, v47\n"
    "v_mfma_f32_16x16x32_bf16 v[72:75], a[0:3], v[16:19], v[72:75]\n"
    "v_cvt_pk_bf16_f32 v40, v40, v41\n"
    "v_cvt_pk_bf16_f32 v41, v42, v43\n"
    "v_mfma_f32_16x16x32_bf16 v[88:91], a[4:7], v[16:19], v[88:91]\n"
    "v_cvt_pk_bf16_f32 v42, v44, v45\n"
    "v_cvt_pk_bf16_f32 v43, v46, v47\n"
    "v_mfma_f32_16x16x32_bf16 v[104:107], a[8:11], v[16:19], v[104:107]\n"
    "v_exp_f32 v48, v48\n"
    "v_mfma_f32_16x16x32_bf16 v[120:123], a[12:15], v[16:19], v[120:123]\n"
    "v_exp_f32 v49, v49\n"
    "v_mfma_f32_16x16x32_bf16 v[136:139], a[16:19], v[16:19], v[136:139]\n"
    "v_exp_f32 v50, v50\n"
    "v_mfma_f32_16x16x32_bf16 v[152:155], a[20:23], v[16:19], v[152:155]\n"
    "v_exp_f32 v51, v51\n"
    "v_mfma_f32_16x16x32_bf16 v[168:171], a[24:27], v[16:19], v[168:171]\n"
    "v_exp_f32 v52, v52\n"
    "v_mfma_f32_16x16x32_bf16 v[184:187], a[28:31], v[16:19], v[184:187]\n"
    "v_exp_f32 v53, v53\n"
    "v_mfma_f32_16x16x32_bf16 a[200:203], a[32:35], v[16:19], a[200:203]\n"
    "v_exp_f32 v54, v54\n"
    "v_mfma_f32_16x16x32_bf16 a[216:219], a[36:39], v[16:19], a[216:219]\n"
    "v_fma_f32 v55, v55, s36, v214\n"
    "v_cvt_pk_bf16_f32 v48, v48, v49\n"
    "v_mfma_f32_16x16x32_bf16 a[232:235], a[40:43], v[16:19], a[232:235]\n"
    "v_exp_f32 v55, v55\n"
    "v_mfma_f32_16x16x32_bf16 a[248:251], a[44:47], v[16:19], a[248:251]\n"
    "v_cvt_pk_bf16_f32 v49, v50, v51\n"
    "v_cvt_pk_bf16_f32 v50, v52, v53\n"
    "v_mfma_f32_16x16x32_bf16 v[200:203], v[208:211], v[16:19], v[200:203]\n"
    "v_cvt_pk_bf16_f32 v51, v54, v55\n"
    "v_fma_f32 v56, v56, s36, v215\n"
    "v_mfma_f32_16x16x32_bf16 v[76:79], a[0:3], v[24:27], v[76:79]\n"
    "v_exp_f32 v56, v56\n"
    "v_mfma_f32_16x16x32_bf16 v[92:95], a[4:7], v[24:27], v[92:95]\n"
    "v_fma_f32 v57, v57, s36, v215\n"
    "v_fma_f32 v58, v58, s36, v215\n"
    "v_mfma_f32_16x16x32_bf16 v[108:111], a[8:11], v[24:27], v[108:111]\n"
    "v_exp_f32 v57, v57\n"
    "v_mfma_f32_16x16x32_bf16 v[124:127], a[12:15], v[24:27], v[124:127]\n"
    "v_exp_f32 v58, v58\n"
    "v_mfma_f32_16x16x32_bf16 v[140:143], a[16:19], v[24:27], v[140:143]\n"
    "v_fma_f32 v59, v59, s36, v215\n"
    "v_fma_f32 v60, v60, s36, v215\n"
    "v_mfma_f32_16x16x32_bf16 v[156:159], a[20:23], v[24:27], v[156:159]\n"
    "v_exp_f32 v59, v59\n"
    "v_mfma_f32_16x16x32_bf16 v[172:175], a[24:27], v[24:27], v[172:175]\n"
    "v_exp_f32 v60, v60\n"
    "v_mfma_f32_16x16x32_bf16 v[188:191], a[28:31], v[24:27], v[188:191]\n"
    "v_fma_f32 v61, v61, s36, v215\n"
    "v_fma_f32 v62, v62, s36, v215\n"
    "v_mfma_f32_16x16x32_bf16 a[204:207], a[32:35], v[24:27], a[204:207]\n"
    "v_exp_f32 v61, v61\n"
    "v_mfma_f32_16x16x32_bf16 a[220:223], a[36:39], v[24:27], a[220:223]\n"
    "v_exp_f32 v62, v62\n"
    "v_mfma_f32_16x16x32_bf16 a[236:239], a[40:43], v[24:27], a[236:239]\n"
    "v_fma_f32 v63, v63, s36, v215\n"
    "v_cvt_pk_bf16_f32 v56, v56, v57\n"
    "v_mfma_f32_16x16x32_bf16 a[252:255], a[44:47], v[24:27], a[252:255]\n"
    "v_exp_f32 v63, v63\n"
    "v_mfma_f32_16x16x32_bf16 v[204:207], v[208:211], v[24:27], v[204:207]\n"
    "s_waitcnt vmcnt(13) lgkmcnt(0)\n"
    "s_barrier\n"
    "L_ph8:\n"
    "v_mfma_f32_16x16x32_bf16 v[0:3], a[48:51], a[96:99], 0\n"
    "buffer_load_dwordx4 v231, s[24:27], s42 offen nt lds\n"
    "v_mfma_f32_16x16x32_bf16 v[0:3], a[52:55], a[100:103], v[0:3]\n"
    "ds_read_b64_tr_b16 a[0:1], v218 offset:36864\n"
    "v_cvt_pk_bf16_f32 v57, v58, v59\n"
    "v_mfma_f32_16x16x32_bf16 v[0:3], a[56:59], a[104:107], v[0:3]\n"
    "buffer_load_dwordx4 v232, s[24:27], s42 offen offset:1024 nt lds\n"
    "v_mfma_f32_16x16x32_bf16 v[0:3], a[60:63], a[108:111], v[0:3]\n"
    "ds_read_b64_tr_b16 a[2:3], v218 offset:43008\n"
    "v_cvt_pk_bf16_f32 v58, v60, v61\n"
    "v_mfma_f32_16x16x32_bf16 v[0:3], a[64:67], a[112:115], v[0:3]\n"
    "buffer_load_dwordx4 v233, s[24:27], s42 offen offset:2048 nt lds\n"
    "v_mfma_f32_16x16x32_bf16 v[0:3], a[68:71], a[116:119], v[0:3]\n"
    "s_add_u32 m0, s44, 3072\n"
    "s_add_u32 s43, s41, 3072\n"
    "v_mfma_f32_16x16x32_bf16 v[4:7], a[72:75], a[96:99], 0\n"
    "buffer_load_dwordx4 v228, s[16:19], s43 offen nt lds\n"
    "v_mfma_f32_16x16x32_bf16 v[4:7], a[76:79], a[100:103], v[4:7]\n"
    "ds_read_b64_tr_b16 a[4:5], v219 offset:36864\n"
    "s_add_u32 m0, s37, 36864\n"
    "v_mfma_f32_16x16x32_bf16 v[4:7], a[80:83], a[104:107], v[4:7]\n"
    "ds_read_b64_tr_b16 a[6:7], v219 offset:43008\n"
    "s_add_u32 s42, s38, 86016\n"
    "v_mfma_f32_16x16x32_bf16 v[4:7], a[84:87], a[108:111], v[4:7]\n"
    "ds_read_b64_tr_b16 a[8:9], v220 offset:36864\n"
    "v_cvt_pk_bf16_f32 v59, v62, v63\n"
    "v_mfma_f32_16x16x32_bf16 v[4:7], a[88:91], a[112:115], v[4:7]\n"
    "ds_read_b64_tr_b16 a[10:11], v220 offset:43008\n"
    "v_mfma_f32_16x16x32_bf16 v[4:7], a[92:95], a[116:119], v[4:7]\n"
    "ds_read_b64_tr_b16 a[12:13], v221 offset:36864\n"
    "v_mfma_f32_16x16x32_bf16 v[8:11], a[48:51], a[120:123], 0\n"
    "ds_read_b64_tr_b16 a[14:15], v221 offset:43008\n"
    "v_mfma_f32_16x16x32_bf16 v[8:11], a[52:55], a[124:127], v[8:11]\n"
    "ds_read_b64_tr_b16 a[16:17], v218 offset:36992\n"
    "v_fma_f32 v0, v0, s36, v212\n"
    "v_mfma_f32_16x16x32_bf16 v[8:11], a[56:59], a[128:131], v[8:11]\n"
    "ds_read_b64_tr_b16 a[18:19], v218 offset:43136\n"
    "v_fma_f32 v1, v1, s36, v212\n"
    "v_mfma_f32_16x16x32_bf16 v[8:11], a[60:63], a[132:135], v[8:11]\n"
    "ds_read_b64_tr_b16 a[20:21], v219 offset:36992\n"
    "v_fma_f32 v2, v2, s36, v212\n"
    "v_mfma_f32_16x16x32_bf16 v[8:11], a[64:67], a[136:139], v[8:11]\n"
    "ds_read_b64_tr_b16 a[22:23], v219 offset:43136\n"
    "v_fma_f32 v3, v3, s36, v212\n"
    "v_mfma_f32_16x16x32_bf16 v[8:11], a[68:71], a[140:143], v[8:11]\n"
    "ds_read_b64_tr_b16 a[24:25], v220 offset:36992\n"
    "v_mfma_f32_16x16x32_bf16 v[12:15], a[72:75], a[120:123], 0\n"
    "ds_read_b64_tr_b16 a[26:27], v220 offset:43136\n"
    "v_mfma_f32_16x16x32_bf16 v[12:15], a[76:79], a[124:127], v[12:15]\n"
    "ds_read_b64_tr_b16 a[28:29], v221 offset:36992\n"
    "v_fma_f32 v4, v4, s36, v212\n"
    "v_mfma_f32_16x16x32_bf16 v[12:15], a[80:83], a[128:131], v[12:15]\n"
    "ds_read_b64_tr_b16 a[30:31], v221 offset:43136\n"
    "v_fma_f32 v5, v5, s36, v212\n"
    "v_mfma_f32_16x16x32_bf16 v[12:15], a[84:87], a[132:135], v[12:15]\n"
    "ds_read_b64_tr_b16 a[32:33], v218 offset:37120\n"
    "v_fma_f32 v6, v6, s36, v212\n"
    "v_mfma_f32_16x16x32_bf16 v[12:15], a[88:91], a[136:139], v[12:15]\n"
    "ds_read_b64_tr_b16 a[34:35], v218 offset:43264\n"
    "v_fma_f32 v7, v7, s36, v212\n"
    "v_mfma_f32_16x16x32_bf16 v[12:15], a[92:95], a[140:143], v[12:15]\n"
    "ds_read_b64_tr_b16 a[36:37], v219 offset:37120\n"
    "v_mfma_f32_16x16x32_bf16 v[16:19], a[48:51], a[144:147], 0\n"
    "ds_read_b64_tr_b16 a[38:39], v219 offset:43264\n"
    "v_mfma_f32_16x16x32_bf16 v[16:19], a[52:55], a[148:151], v[16:19]\n"
    "ds_read_b64_tr_b16 a[40:41], v220 offset:37120\n"
    "v_fma_f32 v8, v8, s36, v213\n"
    "v_mfma_f32_16x16x32_bf16 v[16:19], a[56:59], a[152:155], v[16:19]\n"
    "ds_read_b64_tr_b16 a[42:43], v220 offset:43264\n"
    "v_fma_f32 v9, v9, s36, v213\n"
    "v_mfma_f32_16x16x32_bf16 v[16:19], a[60:63], a[156:159], v[16:19]\n"
    "ds_read_b64_tr_b16 a[44:45], v221 offset:37120\n"
    "v_fma_f32 v10, v10, s36, v213\n"
    "v_mfma_f32_16x16x32_bf16 v[16:19], a[64:67], a[160:163], v[16:19]\n"
    "ds_read_b64_tr_b16 a[46:47], v221 offset:43264\n"
    "v_fma_f32 v11, v11, s36, v213\n"
    "v_mfma_f32_16x16x32_bf16 v[16:19], a[68:71], a[164:167], v[16:19]\n"
    "v_exp_f32 v0, v0\n"
    "v_mfma_f32_16x16x32_bf16 v[20:23], a[72:75], a[144:147], 0\n"
    "v_exp_f32 v1, v1\n"
    "v_mfma_f32_16x16x32_bf16 v[20:23], a[76:79], a[148:151], v[20:23]\n"
    "v_exp_f32 v2, v2\n"
    "v_mfma_f32_16x16x32_bf16 v[20:23], a[80:83], a[152:155], v[20:23]\n"
    "v_exp_f32 v3, v3\n"
    "v_mfma_f32_16x16x32_bf16 v[20:23], a[84:87], a[156:159], v[20:23]\n"
    "v_exp_f32 v4, v4\n"
    "v_mfma_f32_16x16x32_bf16 v[20:23], a[88:91], a[160:163], v[20:23]\n"
    "v_exp_f32 v5, v5\n"
    "v_mfma_f32_16x16x32_bf16 v[20:23], a[92:95], a[164:167], v[20:23]\n"
    "v_exp_f32 v6, v6\n"
    "v_mfma_f32_16x16x32_bf16 v[24:27], a[48:51], a[168:171], 0\n"
    "v_exp_f32 v7, v7\n"
    "v_mfma_f32_16x16x32_bf16 v[24:27], a[52:55], a[172:175], v[24:27]\n"
    "v_cvt_pk_bf16_f32 v0, v0, v1\n"
    "v_cvt_pk_bf16_f32 v1, v2, v3\n"
    "v_mfma_f32_16x16x32_bf16 v[24:27], a[56:59], a[176:179], v[24:27]\n"
    "v_cvt_pk_bf16_f32 v2, v4, v5\n"
    "v_cvt_pk_bf16_f32 v3, v6, v7\n"
    "v_mfma_f32_16x16x32_bf16 v[24:27], a[60:63], a[180:183], v[24:27]\n"
    "v_exp_f32 v8, v8\n"
    "v_mfma_f32_16x16x32_bf16 v[24:27], a[64:67], a[184:187], v[24:27]\n"
    "v_exp_f32 v9, v9\n"
    "v_mfma_f32_16x16x32_bf16 v[24:27], a[68:71], a[188:191], v[24:27]\n"
    "v_exp_f32 v10, v10\n"
    "v_mfma_f32_16x16x32_bf16 v[28:31], a[72:75], a[168:171], 0\n"
    "v_exp_f32 v11, v11\n"
    "v_mfma_f32_16x16x32_bf16 v[28:31], a[76:79], a[172:175], v[28:31]\n"
    "v_fma_f32 v12, v12, s36, v213\n"
    "v_fma_f32 v13, v13, s36, v213\n"
    "v_mfma_f32_16x16x32_bf16 v[28:31], a[80:83], a[176:179], v[28:31]\n"
    "v_exp_f32 v12, v12\n"
    "v_mfma_f32_16x16x32_bf16 v[28:31], a[84:87], a[180:183], v[28:31]\n"
    "v_exp_f32 v13, v13\n"
    "v_mfma_f32_16x16x32_bf16 v[28:31], a[88:91], a[184:187], v[28:31]\n"
    "v_fma_f32 v14, v14, s36, v213\n"
    "v_fma_f32 v15, v15, s36, v213\n"
    "v_mfma_f32_16x16x32_bf16 v[28:31], a[92:95], a[188:191], v[28:31]\n"
    "s_waitcnt vmcnt(14) lgkmcnt(0)\n"
    "s_barrier\n"
    "L_ph9:\n"
    "v_mfma_f32_16x16x32_bf16 v[64:67], a[0:3], v[32:35], v[64:67]\n"
    "buffer_load_dwordx4 v222, s[20:23], s42 offen nt lds\n"
    "v_mfma_f32_16x16x32_bf16 v[80:83], a[4:7], v[32:35], v[80:83]\n"
    "ds_read_b128 a[48:51], v216 offset:49152\n"
    "v_cvt_pk_bf16_f32 v8, v8, v9\n"
    "v_mfma_f32_16x16x32_bf16 v[96:99], a[8:11], v[32:35], v[96:99]\n"
    "buffer_load_dwordx4 v223, s[20:23], s42 offen offset:1024 nt lds\n"
    "v_mfma_f32_16x16x32_bf16 v[112:115], a[12:15], v[32:35], v[112:115]\n"
    "ds_read_b128 a[52:55], v217 offset:49152\n"
    "v_cvt_pk_bf16_f32 v9, v10, v11\n"
    "v_mfma_f32_16x16x32_bf16 v[128:131], a[16:19], v[32:35], v[128:131]\n"
    "buffer_load_dwordx4 v224, s[20:23], s42 offen offset:2048 nt lds\n"
    "v_mfma_f32_16x16x32_bf16 v[144:147], a[20:23], v[32:35], v[144:147]\n"
    "s_add_u32 m0, s44, 3072\n"
    "s_add_u32 s43, s41, 3072\n"
    "v_mfma_f32_16x16x32_bf16 v[160:163], a[24:27], v[32:35], v[160:163]\n"
    "buffer_load_dwordx4 v229, s[16:19], s43 offen offset:1024 nt lds\n"
    "v_mfma_f32_16x16x32_bf16 v[176:179], a[28:31], v[32:35], v[176:179]\n"
    "ds_read_b128 a[56:59], v216 offset:49280\n"
    "s_add_u32 m0, s37, 49152\n"
    "v_mfma_f32_16x16x32_bf16 a[192:195], a[32:35], v[32:35], a[192:195]\n"
    "ds_read_b128 a[60:63], v217 offset:49280\n"
    "s_add_u32 s42, s38, 73728\n"
    "v_mfma_f32_16x16x32_bf16 a[208:211], a[36:39], v[32:35], a[208:211]\n"
    "ds_read_b128 a[64:67], v216 offset:49408\n"
    "v_cvt_pk_bf16_f32 v10, v12, v13\n"
    "v_mfma_f32_16x16x32_bf16 a[224:227], a[40:43], v[32:35], a[224:227]\n"
    "ds_read_b128 a[68:71], v217 offset:49408\n"
    "v_fma_f32 v16, v16, s36, v214\n"
    "v_mfma_f32_16x16x32_bf16 a[240:243], a[44:47], v[32:35], a[240:243]\n"
    "ds_read_b128 a[72:75], v216 offset:55296\n"
    "v_fma_f32 v17, v17, s36, v214\n"
    "v_mfma_f32_16x16x32_bf16 v[192:195], v[208:211], v[32:35], v[192:195]\n"
    "ds_read_b128 a[76:79], v217 offset:55296\n"
    "v_fma_f32 v18, v18, s36, v214\n"
    "v_mfma_f32_16x16x32_bf16 v[68:71], a[0:3], v[40:43], v[68:71]\n"
    "ds_read_b128 a[80:83], v216 offset:55424\n"
    "v_fma_f32 v19, v19, s36, v214\n"
    "v_mfma_f32_16x16x32_bf16 v[84:87], a[4:7], v[40:43], v[84:87]\n"
    "ds_read_b128 a[84:87], v217 offset:55424\n"
    "v_fma_f32 v20, v20, s36, v214\n"
    "v_mfma_f32_16x16x32_bf16 v[100:103], a[8:11], v[40:43], v[100:103]\n"
    "ds_read_b128 a[88:91], v216 offset:55552\n"
    "v_fma_f32 v21, v21, s36, v214\n"
    "v_mfma_f32_16x16x32_bf16 v[116:119], a[12:15], v[40:43], v[116:119]\n"
    "ds_read_b128 a[92:95], v217 offset:55552\n"
    "v_fma_f32 v22, v22, s36, v214\n"
    "v_mfma_f32_16x16x32_bf16 v[132:135], a[16:19], v[40:43], v[132:135]\n"
    "v_exp_f32 v14, v14\n"
    "v_mfma_f32_16x16x32_bf16 v[148:151], a[20:23], v[40:43], v[148:151]\n"
    "v_exp_f32 v15, v15\n"
    "v_mfma_f32_16x16x32_bf16 v[164:167], a[24:27], v[40:43], v[164:167]\n"
    "v_cvt_pk_bf16_f32 v11, v14, v15\n"
    "v_fma_f32 v23, v23, s36, v214\n"
    "v_mfma_f32_16x16x32_bf16 v[180:183], a[28:31], v[40:43], v[180:183]\n"
    "v_exp_f32 v16, v16\n"
    "v_mfma_f32_16x16x32_bf16 a[196:199], a[32:35], v[40:43], a[196:199]\n"
    "v_exp_f32 v17, v17\n"
    "v_mfma_f32_16x16x32_bf16 a[212:215], a[36:39], v[40:43], a[212:215]\n"
    "v_exp_f32 v18, v18\n"
    "v_mfma_f32_16x16x32_bf16 a[228:231], a[40:43], v[40:43], a[228:231]\n"
    "v_exp_f32 v19, v19\n"
    "v_mfma_f32_16x16x32_bf16 a[244:247], a[44:47], v[40:43], a[244:247]\n"
    "v_exp_f32 v20, v20\n"
    "v_mfma_f32_16x16x32_bf16 v[196:199], v[208:211], v[40:43], v[196:199]\n"
    "v_exp_f32 v21, v21\n"
    "v_mfma_f32_16x16x32_bf16 v[72:75], a[0:3], v[48:51], v[72:75]\n"
    "v_exp_f32 v22, v22\n"
    "v_mfma_f32_16x16x32_bf16 v[88:91], a[4:7], v[48:51], v[88:91]\n"
    "v_exp_f32 v23, v23\n"
    "v_mfma_f32_16x16x32_bf16 v[104:107], a[8:11], v[48:51], v[104:107]\n"
    "v_cvt_pk_bf16_f32 v16, v16, v17\n"
    "v_cvt_pk_bf16_f32 v17, v18, v19\n"
    "v_mfma_f32_16x16x32_bf16 v[120:123], a[12:15], v[48:51], v[120:123]\n"
    "v_cvt_pk_bf16_f32 v18, v20, v21\n"
    "v_cvt_pk_bf16_f32 v19, v22, v23\n"
    "v_mfma_f32_16x16x32_bf16 v[136:139], a[16:19], v[48:51], v[136:139]\n"
    "v_fma_f32 v24, v24, s36, v215\n"
    "v_fma_f32 v25, v25, s36, v215\n"
    "v_mfma_f32_16x16x32_bf16 v[152:155], a[20:23], v[48:51], v[152:155]\n"
    "v_exp_f32 v24, v24\n"
    "v_mfma_f32_16x16x32_bf16 v[168:171], a[24:27], v[48:51], v[168:171]\n"
    "v_exp_f32 v25, v25\n"
    "v_mfma_f32_16x16x32_bf16 v[184:187], a[28:31], v[48:51], v[184:187]\n"
    "v_fma_f32 v26, v26, s36, v215\n"
    "v_fma_f32 v27, v27, s36, v215\n"
    "v_mfma_f32_16x16x32_bf16 a[200:203], a[32:35], v[48:51], a[200:203]\n"
    "v_exp_f32 v26, v26\n"
    "v_mfma_f32_16x16x32_bf16 a[216:219], a[36:39], v[48:51], a[216:219]\n"
    "v_exp_f32 v27, v27\n"
    "v_mfma_f32_16x16x32_bf16 a[232:235], a[40:43], v[48:51], a[232:235]\n"
    "v_fma_f32 v28, v28, s36, v215\n"
    "v_fma_f32 v29, v29, s36, v215\n"
    "v_mfma_f32_16x16x32_bf16 a[248:251], a[44:47], v[48:51], a[248:251]\n"
    "v_exp_f32 v28, v28\n"
    "v_mfma_f32_16x16x32_bf16 v[200:203], v[208:211], v[48:51], v[200:203]\n"
    "v_exp_f32 v29, v29\n"
    "v_mfma_f32_16x16x32_bf16 v[76:79], a[0:3], v[56:59], v[76:79]\n"
    "v_fma_f32 v30, v30, s36, v215\n"
    "v_fma_f32 v31, v31, s36, v215\n"
    "v_mfma_f32_16x16x32_bf16 v[92:95], a[4:7], v[56:59], v[92:95]\n"
    "v_exp_f32 v30, v30\n"
    "v_mfma_f32_16x16x32_bf16 v[108:111], a[8:11], v[56:59], v[108:111]\n"
    "v_exp_f32 v31, v31\n"
    "v_mfma_f32_16x16x32_bf16 v[124:127], a[12:15], v[56:59], v[124:127]\n"
    "v_cvt_pk_bf16_f32 v24, v24, v25\n"
    "v_cvt_pk_bf16_f32 v25, v26, v27\n"
    "v_mfma_f32_16x16x32_bf16 v[140:143], a[16:19], v[56:59], v[140:143]\n"
    "v_cvt_pk_bf16_f32 v26, v28, v29\n"
    "v_cvt_pk_bf16_f32 v27, v30, v31\n"
    "v_mfma_f32_16x16x32_bf16 v[156:159], a[20:23], v[56:59], v[156:159]\n"
    "v_mfma_f32_16x16x32_bf16 v[172:175], a[24:27], v[56:59], v[172:175]\n"
    "v_mfma_f32_16x16x32_bf16 v[188:191], a[28:31], v[56:59], v[188:191]\n"
    "v_mfma_f32_16x16x32_bf16 a[204:207], a[32:35], v[56:59], a[204:207]\n"
    "v_mfma_f32_16x16x32_bf16 a[220:223], a[36:39], v[56:59], a[220:223]\n"
    "v_mfma_f32_16x16x32_bf16 a[236:239], a[40:43], v[56:59], a[236:239]\n"
    "v_mfma_f32_16x16x32_bf16 a[252:255], a[44:47], v[56:59], a[252:255]\n"
    "v_mfma_f32_16x16x32_bf16 v[204:207], v[208:211], v[56:59], v[204:207]\n"
    "s_waitcnt vmcnt(14) lgkmcnt(0)\n"
    "s_barrier\n"
    "L_ph10:\n"
    "v_mfma_f32_16x16x32_bf16 v[32:35], a[48:51], a[96:99], 0\n"
    "buffer_load_dwordx4 v231, s[24:27], s42 offen nt lds\n"
    "v_mfma_f32_16x16x32_bf16 v[32:35], a[52:55], a[100:103], v[32:35]\n"
    "ds_read_b64_tr_b16 a[0:1], v218 offset:0\n"
    "v_mfma_f32_16x16x32_bf16 v[32:35], a[56:59], a[104:107], v[32:35]\n"
    "buffer_load_dwordx4 v232, s[24:27], s42 offen offset:1024 nt lds\n"
    "v_mfma_f32_16x16x32_bf16 v[32:35], a[60:63], a[108:111], v[32:35]\n"
    "ds_read_b64_tr_b16 a[2:3], v218 offset:6144\n"
    "v_mfma_f32_16x16x32_bf16 v[32:35], a[64:67], a[112:115], v[32:35]\n"
    "buffer_load_dwordx4 v233, s[24:27], s42 offen offset:2048 nt lds\n"
    "v_mfma_f32_16x16x32_bf16 v[32:35], a[68:71], a[116:119], v[32:35]\n"
    "s_add_u32 m0, s44, 3072\n"
    "s_add_u32 s43, s41, 3072\n"
    "v_mfma_f32_16x16x32_bf16 v[36:39], a[72:75], a[96:99], 0\n"
    "buffer_load_dwordx4 v230, s[16:19], s43 offen offset:2048 nt lds\n"
    "v_mfma_f32_16x16x32_bf16 v[36:39], a[76:79], a[100:103], v[36:39]\n"
    "ds_read_b64_tr_b16 a[4:5], v219 offset:0\n"
    "s_add_u32 m0, s37, 0\n"
    "v_mfma_f32_16x16x32_bf16 v[36:39], a[80:83], a[104:107], v[36:39]\n"
    "ds_read_b64_tr_b16 a[6:7], v219 offset:6144\n"
    "s_add_u32 s42, s38, 98304\n"
    "v_mfma_f32_16x16x32_bf16 v[36:39], a[84:87], a[108:111], v[36:39]\n"
    "ds_read_b64_tr_b16 a[8:9], v220 offset:0\n"
    "v_mfma_f32_16x16x32_bf16 v[36:39], a[88:91], a[112:115], v[36:39]\n"
    "ds_read_b64_tr_b16 a[10:11], v220 offset:6144\n"
    "v_mfma_f32_16x16x32_bf16 v[36:39], a[92:95], a[116:119], v[36:39]\n"
    "ds_read_b64_tr_b16 a[12:13], v221 offset:0\n"
    "v_mfma_f32_16x16x32_bf16 v[40:43], a[48:51], a[120:123], 0\n"
    "ds_read_b64_tr_b16 a[14:15], v221 offset:6144\n"
    "v_mfma_f32_16x16x32_bf16 v[40:43], a[52:55], a[124:127], v[40:43]\n"
    "ds_read_b64_tr_b16 a[16:17], v218 offset:128\n"
    "v_fma_f32 v32, v32, s36, v212\n"
    "v_mfma_f32_16x16x32_bf16 v[40:43], a[56:59], a[128:131], v[40:43]\n"
    "ds_read_b64_tr_b16 a[18:19], v218 offset:6272\n"
    "v_fma_f32 v33, v33, s36, v212\n"
    "v_mfma_f32_16x16x32_bf16 v[40:43], a[60:63], a[132:135], v[40:43]\n"
    "ds_read_b64_tr_b16 a[20:21], v219 offset:128\n"
    "v_fma_f32 v34, v34, s36, v212\n"
    "v_mfma_f32_16x16x32_bf16 v[40:43], a[64:67], a[136:139], v[40:43]\n"
    "ds_read_b64_tr_b16 a[22:23], v219 offset:6272\n"
    "v_fma_f32 v35, v35, s36, v212\n"
    "v_mfma_f32_16x16x32_bf16 v[40:43], a[68:71], a[140:143], v[40:43]\n"
    "ds_read_b64_tr_b16 a[24:25], v220 offset:128\n"
    "v_mfma_f32_16x16x32_bf16 v[44:47], a[72:75], a[120:123], 0\n"
    "ds_read_b64_tr_b16 a[26:27], v220 offset:6272\n"
    "v_mfma_f32_16x16x32_bf16 v[44:47], a[76:79], a[124:127], v[44:47]\n"
    "ds_read_b64_tr_b16 a[28:29], v221 offset:128\n"
    "v_fma_f32 v36, v36, s36, v212\n"
    "v_mfma_f32_16x16x32_bf16 v[44:47], a[80:83], a[128:131], v[44:47]\n"
    "ds_read_b64_tr_b16 a[30:31], v221 offset:6272\n"
    "v_fma_f32 v37, v37, s36, v212\n"
    "v_mfma_f32_16x16x32_bf16 v[44:47], a[84:87], a[132:135], v[44:47]\n"
    "ds_read_b64_tr_b16 a[32:33], v218 offset:256\n"
    "v_fma_f32 v38, v38, s36, v212\n"
    "v_mfma_f32_16x16x32_bf16 v[44:47], a[88:91], a[136:139], v[44:47]\n"
    "ds_read_b64_tr_b16 a[34:35], v218 offset:6400\n"
    "v_fma_f32 v39, v39, s36, v212\n"
    "v_mfma_f32_16x16x32_bf16 v[44:47], a[92:95], a[140:143], v[44:47]\n"
    "ds_read_b64_tr_b16 a[36:37], v219 offset:256\n"
    "v_mfma_f32_16x16x32_bf16 v[48:51], a[48:51], a[144:147], 0\n"
    "ds_read_b64_tr_b16 a[38:39], v219 offset:6400\n"
    "v_mfma_f32_16x16x32_bf16 v[48:51], a[52:55], a[148:151], v[48:51]\n"
    "ds_read_b64_tr_b16 a[40:41], v220 offset:256\n"
    "v_fma_f32 v40, v40, s36, v213\n"
    "v_mfma_f32_16x16x32_bf16 v[48:51], a[56:59], a[152:155], v[48:51]\n"
    "ds_read_b64_tr_b16 a[42:43], v220 offset:6400\n"
    "v_fma_f32 v41, v41, s36, v213\n"
    "v_mfma_f32_16x16x32_bf16 v[48:51], a[60:63], a[156:159], v[48:51]\n"
    "ds_read_b64_tr_b16 a[44:45], v221 offset:256\n"
    "v_fma_f32 v42, v42, s36, v213\n"
    "v_mfma_f32_16x16x32_bf16 v[48:51], a[64:67], a[160:163], v[48:51]\n"
    "ds_read_b64_tr_b16 a[46:47], v221 offset:6400\n"
    "v_fma_f32 v43, v43, s36, v213\n"
    "v_mfma_f32_16x16x32_bf16 v[48:51], a[68:71], a[164:167], v[48:51]\n"
    "v_exp_f32 v32, v32\n"
    "v_mfma_f32_16x16x32_bf16 v[52:55], a[72:75], a[144:147], 0\n"
    "v_exp_f32 v33, v33\n"
    "v_mfma_f32_16x16x32_bf16 v[52:55], a[76:79], a[148:151], v[52:55]\n"
    "v_exp_f32 v34, v34\n"
    "v_mfma_f32_16x16x32_bf16 v[52:55], a[80:83], a[152:155], v[52:55]\n"
    "v_exp_f32 v35, v35\n"
    "v_mfma_f32_16x16x32_bf16 v[52:55], a[84:87], a[156:159], v[52:55]\n"
    "v_exp_f32 v36, v36\n"
    "v_mfma_f32_16x16x32_bf16 v[52:55], a[88:91], a[160:163], v[52:55]\n"
    "v_exp_f32 v37, v37\n"
    "v_mfma_f32_16x16x32_bf16 v[52:55], a[92:95], a[164:167], v[52:55]\n"
    "v_exp_f32 v38, v38\n"
    "v_mfma_f32_16x16x32_bf16 v[56:59], a[48:51], a[168:171], 0\n"
    "v_exp_f32 v39, v39\n"
    "v_mfma_f32_16x16x32_bf16 v[56:59], a[52:55], a[172:175], v[56:59]\n"
    "v_cvt_pk_bf16_f32 v32, v32, v33\n"
    "v_cvt_pk_bf16_f32 v33, v34, v35\n"
    "v_mfma_f32_16x16x32_bf16 v[56:59], a[56:59], a[176:179], v[56:59]\n"
    "v_cvt_pk_bf16_f32 v34, v36, v37\n"
    "v_cvt_pk_bf16_f32 v35, v38, v39\n"
    "v_mfma_f32_16x16x32_bf16 v[56:59], a[60:63], a[180:183], v[56:59]\n"
    "v_exp_f32 v40, v40\n"
    "v_mfma_f32_16x16x32_bf16 v[56:59], a[64:67], a[184:187], v[56:59]\n"
    "v_exp_f32 v41, v41\n"
    "v_mfma_f32_16x16x32_bf16 v[56:59], a[68:71], a[188:191], v[56:59]\n"
    "v_exp_f32 v42, v42\n"
    "v_mfma_f32_16x16x32_bf16 v[60:63], a[72:75], a[168:171], 0\n"
    "v_exp_f32 v43, v43\n"
    "v_mfma_f32_16x16x32_bf16 v[60:63], a[76:79], a[172:175], v[60:63]\n"
    "v_fma_f32 v44, v44, s36, v213\n"
    "v_fma_f32 v45, v45, s36, v213\n"
    "v_mfma_f32_16x16x32_bf16 v[60:63], a[80:83], a[176:179], v[60:63]\n"
    "v_exp_f32 v44, v44\n"
    "v_mfma_f32_16x16x32_bf16 v[60:63], a[84:87], a[180:183], v[60:63]\n"
    "v_exp_f32 v45, v45\n"
    "v_mfma_f32_16x16x32_bf16 v[60:63], a[88:91], a[184:187], v[60:63]\n"
    "v_fma_f32 v46, v46, s36, v213\n"
    "v_fma_f32 v47, v47, s36, v213\n"
    "v_mfma_f32_16x16x32_bf16 v[60:63], a[92:95], a[188:191], v[60:63]\n"
    "s_waitcnt vmcnt(14) lgkmcnt(0)\n"
    "s_barrier\n"
    "L_ph11:\n"
    "v_mfma_f32_16x16x32_bf16 v[64:67], a[0:3], v[0:3], v[64:67]\n"
    "buffer_load_dwordx4 v222, s[20:23], s42 offen nt lds\n"
    "v_mfma_f32_16x16x32_bf16 v[80:83], a[4:7], v[0:3], v[80:83]\n"
    "ds_read_b128 a[48:51], v216 offset:12288\n"
    "v_cvt_pk_bf16_f32 v40, v40, v41\n"
    "v_mfma_f32_16x16x32_bf16 v[96:99], a[8:11], v[0:3], v[96:99]\n"
    "buffer_load_dwordx4 v223, s[20:23], s42 offen offset:1024 nt lds\n"
    "v_mfma_f32_16x16x32_bf16 v[112:115], a[12:15], v[0:3], v[112:115]\n"
    "ds_read_b128 a[52:55], v217 offset:12288\n"
    "v_cvt_pk_bf16_f32 v41, v42, v43\n"
    "v_mfma_f32_16x16x32_bf16 v[128:131], a[16:19], v[0:3], v[128:131]\n"
    "buffer_load_dwordx4 v224, s[20:23], s42 offen offset:2048 nt lds\n"
    "v_mfma_f32_16x16x32_bf16 v[144:147], a[20:23], v[0:3], v[144:147]\n"
    "s_add_u32 m0, s44, 6144\n"
    "s_add_u32 s43, s41, 6144\n"
    "v_mfma_f32_16x16x32_bf16 v[160:163], a[24:27], v[0:3], v[160:163]\n"
    "buffer_load_dwordx4 v225, s[16:19], s43 offen nt lds\n"
    "v_mfma_f32_16x16x32_bf16 v[176:179], a[28:31], v[0:3], v[176:179]\n"
    "s_mov_b32 m0, 159744\n"
    "s_mov_b32 s51, L_ph15-L_code_start\n"
    "v_mfma_f32_16x16x32_bf16 a[192:195], a[32:35], v[0:3], a[192:195]\n"
    "buffer_load_dword v249, s[52:55], s51 offen lds\n"
    "v_mfma_f32_16x16x32_bf16 a[208:211], a[36:39], v[0:3], a[208:211]\n"
    "ds_read_b128 a[56:59], v216 offset:12416\n"
    "s_add_u32 m0, s37, 12288\n"
    "v_mfma_f32_16x16x32_bf16 a[224:227], a[40:43], v[0:3], a[224:227]\n"
    "ds_read_b128 a[60:63], v217 offset:12416\n"
    "s_add_u32 s42, s38, 86016\n"
    "v_mfma_f32_16x16x32_bf16 a[240:243], a[44:47], v[0:3], a[240:243]\n"
    "ds_read_b128 a[64:67], v216 offset:12544\n"
    "v_cvt_pk_bf16_f32 v42, v44, v45\n"
    "v_mfma_f32_16x16x32_bf16 v[192:195], v[208:211], v[0:3], v[192:195]\n"
    "ds_read_b128 a[68:71], v217 offset:12544\n"
    "v_fma_f32 v48, v48, s36, v214\n"
    "v_mfma_f32_16x16x32_bf16 v[68:71], a[0:3], v[8:11], v[68:71]\n"
    "ds_read_b128 a[72:75], v216 offset:18432\n"
    "v_fma_f32 v49, v49, s36, v214\n"
    "v_mfma_f32_16x16x32_bf16 v[84:87], a[4:7], v[8:11], v[84:87]\n"
    "ds_read_b128 a[76:79], v217 offset:18432\n"
    "v_fma_f32 v50, v50, s36, v214\n"
    "v_mfma_f32_16x16x32_bf16 v[100:103], a[8:11], v[8:11], v[100:103]\n"
    "ds_read_b128 a[80:83], v216 offset:18560\n"
    "v_fma_f32 v51, v51, s36, v214\n"
    "v_mfma_f32_16x16x32_bf16 v[116:119], a[12:15], v[8:11], v[116:119]\n"
    "ds_read_b128 a[84:87], v217 offset:18560\n"
    "v_fma_f32 v52, v52, s36, v214\n"
    "v_mfma_f32_16x16x32_bf16 v[132:135], a[16:19], v[8:11], v[132:135]\n"
    "ds_read_b128 a[88:91], v216 offset:18688\n"
    "v_fma_f32 v53, v53, s36, v214\n"
    "v_mfma_f32_16x16x32_bf16 v[148:151], a[20:23], v[8:11], v[148:151]\n"
    "ds_read_b128 a[92:95], v217 offset:18688\n"
    "v_fma_f32 v54, v54, s36, v214\n"
    "v_mfma_f32_16x16x32_bf16 v[164:167], a[24:27], v[8:11], v[164:167]\n"
    "v_exp_f32 v46, v46\n"
    "v_mfma_f32_16x16x32_bf16 v[180:183], a[28:31], v[8:11], v[180:183]\n"
    "v_exp_f32 v47, v47\n"
    "v_mfma_f32_16x16x32_bf16 a[196:199], a[32:35], v[8:11], a[196:199]\n"
    "v_cvt_pk_bf16_f32 v43, v46, v47\n"
    "v_fma_f32 v55, v55, s36, v214\n"
    "v_mfma_f32_16x16x32_bf16 a[212:215], a[36:39], v[8:11], a[212:215]\n"
    "v_exp_f32 v48, v48\n"
    "v_mfma_f32_16x16x32_bf16 a[228:231], a[40:43], v[8:11], a[228:231]\n"
    "v_exp_f32 v49, v49\n"
    "v_mfma_f32_16x16x32_bf16 a[244:247], a[44:47], v[8:11], a[244:247]\n"
    "v_exp_f32 v50, v50\n"
    "v_mfma_f32_16x16x32_bf16 v[196:199], v[208:211], v[8:11], v[196:199]\n"
    "v_exp_f32 v51, v51\n"
    "v_mfma_f32_16x16x32_bf16 v[72:75], a[0:3], v[16:19], v[72:75]\n"
    "v_exp_f32 v52, v52\n"
    "v_mfma_f32_16x16x32_bf16 v[88:91], a[4:7], v[16:19], v[88:91]\n"
    "v_exp_f32 v53, v53\n"
    "v_mfma_f32_16x16x32_bf16 v[104:107], a[8:11], v[16:19], v[104:107]\n"
    "v_exp_f32 v54, v54\n"
    "v_mfma_f32_16x16x32_bf16 v[120:123], a[12:15], v[16:19], v[120:123]\n"
    "v_exp_f32 v55, v55\n"
    "v_mfma_f32_16x16x32_bf16 v[136:139], a[16:19], v[16:19], v[136:139]\n"
    "v_cvt_pk_bf16_f32 v48, v48, v49\n"
    "v_cvt_pk_bf16_f32 v49, v50, v51\n"
    "v_mfma_f32_16x16x32_bf16 v[152:155], a[20:23], v[16:19], v[152:155]\n"
    "v_cvt_pk_bf16_f32 v50, v52, v53\n"
    "v_cvt_pk_bf16_f32 v51, v54, v55\n"
    "v_mfma_f32_16x16x32_bf16 v[168:171], a[24:27], v[16:19], v[168:171]\n"
    "v_fma_f32 v56, v56, s36, v215\n"
    "v_fma_f32 v57, v57, s36, v215\n"
    "v_mfma_f32_16x16x32_bf16 v[184:187], a[28:31], v[16:19], v[184:187]\n"
    "v_exp_f32 v56, v56\n"
    "v_mfma_f32_16x16x32_bf16 a[200:203], a[32:35], v[16:19], a[200:203]\n"
    "v_exp_f32 v57, v57\n"
    "v_mfma_f32_16x16x32_bf16 a[216:219], a[36:39], v[16:19], a[216:219]\n"
    "v_fma_f32 v58, v58, s36, v215\n"
    "v_fma_f32 v59, v59, s36, v215\n"
    "v_mfma_f32_16x16x32_bf16 a[232:235], a[40:43], v[16:19], a[232:235]\n"
    "v_exp_f32 v58, v58\n"
    "v_mfma_f32_16x16x32_bf16 a[248:251], a[44:47], v[16:19], a[248:251]\n"
    "v_exp_f32 v59, v59\n"
    "v_mfma_f32_16x16x32_bf16 v[200:203], v[208:211], v[16:19], v[200:203]\n"
    "v_fma_f32 v60, v60, s36, v215\n"
    "v_fma_f32 v61, v61, s36, v215\n"
    "v_mfma_f32_16x16x32_bf16 v[76:79], a[0:3], v[24:27], v[76:79]\n"
    "v_exp_f32 v60, v60\n"
    "v_mfma_f32_16x16x32_bf16 v[92:95], a[4:7], v[24:27], v[92:95]\n"
    "v_exp_f32 v61, v61\n"
    "v_mfma_f32_16x16x32_bf16 v[108:111], a[8:11], v[24:27], v[108:111]\n"
    "v_fma_f32 v62, v62, s36, v215\n"
    "v_fma_f32 v63, v63, s36, v215\n"
    "v_mfma_f32_16x16x32_bf16 v[124:127], a[12:15], v[24:27], v[124:127]\n"
    "v_exp_f32 v62, v62\n"
    "v_mfma_f32_16x16x32_bf16 v[140:143], a[16:19], v[24:27], v[140:143]\n"
    "v_exp_f32 v63, v63\n"
    "v_mfma_f32_16x16x32_bf16 v[156:159], a[20:23], v[24:27], v[156:159]\n"
    "v_cvt_pk_bf16_f32 v56, v56, v57\n"
    "v_cvt_pk_bf16_f32 v57, v58, v59\n"
    "v_mfma_f32_16x16x32_bf16 v[172:175], a[24:27], v[24:27], v[172:175]\n"
    "v_cvt_pk_bf16_f32 v58, v60, v61\n"
    "v_cvt_pk_bf16_f32 v59, v62, v63\n"
    "v_mfma_f32_16x16x32_bf16 v[188:191], a[28:31], v[24:27], v[188:191]\n"
    "v_mfma_f32_16x16x32_bf16 a[204:207], a[32:35], v[24:27], a[204:207]\n"
    "v_mfma_f32_16x16x32_bf16 a[220:223], a[36:39], v[24:27], a[220:223]\n"
    "v_mfma_f32_16x16x32_bf16 a[236:239], a[40:43], v[24:27], a[236:239]\n"
    "v_mfma_f32_16x16x32_bf16 a[252:255], a[44:47], v[24:27], a[252:255]\n"
    "v_mfma_f32_16x16x32_bf16 v[204:207], v[208:211], v[24:27], v[204:207]\n"
    "s_waitcnt vmcnt(14) lgkmcnt(0)\n"
    "s_barrier\n"
    "L_ph12:\n"
    "v_mfma_f32_16x16x32_bf16 v[0:3], a[48:51], a[96:99], 0\n"
    "buffer_load_dwordx4 v231, s[24:27], s42 offen nt lds\n"
    "v_mfma_f32_16x16x32_bf16 v[0:3], a[52:55], a[100:103], v[0:3]\n"
    "ds_read_b64_tr_b16 a[0:1], v218 offset:24576\n"
    "v_mfma_f32_16x16x32_bf16 v[0:3], a[56:59], a[104:107], v[0:3]\n"
    "buffer_load_dwordx4 v232, s[24:27], s42 offen offset:1024 nt lds\n"
    "v_mfma_f32_16x16x32_bf16 v[0:3], a[60:63], a[108:111], v[0:3]\n"
    "ds_read_b64_tr_b16 a[2:3], v218 offset:30720\n"
    "v_mfma_f32_16x16x32_bf16 v[0:3], a[64:67], a[112:115], v[0:3]\n"
    "buffer_load_dwordx4 v233, s[24:27], s42 offen offset:2048 nt lds\n"
    "v_mfma_f32_16x16x32_bf16 v[0:3], a[68:71], a[116:119], v[0:3]\n"
    "s_add_u32 m0, s44, 6144\n"
    "s_add_u32 s43, s41, 6144\n"
    "v_mfma_f32_16x16x32_bf16 v[4:7], a[72:75], a[96:99], 0\n"
    "buffer_load_dwordx4 v226, s[16:19], s43 offen offset:1024 nt lds\n"
    "v_mfma_f32_16x16x32_bf16 v[4:7], a[76:79], a[100:103], v[4:7]\n"
    "ds_read_b64_tr_b16 a[4:5], v219 offset:24576\n"
    "s_add_u32 m0, s37, 24576\n"
    "v_mfma_f32_16x16x32_bf16 v[4:7], a[80:83], a[104:107], v[4:7]\n"
    "ds_read_b64_tr_b16 a[6:7], v219 offset:30720\n"
    "s_add_u32 s42, s38, 110592\n"
    "v_mfma_f32_16x16x32_bf16 v[4:7], a[84:87], a[108:111], v[4:7]\n"
    "ds_read_b64_tr_b16 a[8:9], v220 offset:24576\n"
    "v_mfma_f32_16x16x32_bf16 v[4:7], a[88:91], a[112:115], v[4:7]\n"
    "ds_read_b64_tr_b16 a[10:11], v220 offset:30720\n"
    "v_mfma_f32_16x16x32_bf16 v[4:7], a[92:95], a[116:119], v[4:7]\n"
    "ds_read_b64_tr_b16 a[12:13], v221 offset:24576\n"
    "v_mfma_f32_16x16x32_bf16 v[8:11], a[48:51], a[120:123], 0\n"
    "ds_read_b64_tr_b16 a[14:15], v221 offset:30720\n"
    "v_mfma_f32_16x16x32_bf16 v[8:11], a[52:55], a[124:127], v[8:11]\n"
    "ds_read_b64_tr_b16 a[16:17], v218 offset:24704\n"
    "v_fma_f32 v0, v0, s36, v212\n"
    "v_mfma_f32_16x16x32_bf16 v[8:11], a[56:59], a[128:131], v[8:11]\n"
    "ds_read_b64_tr_b16 a[18:19], v218 offset:30848\n"
    "v_fma_f32 v1, v1, s36, v212\n"
    "v_mfma_f32_16x16x32_bf16 v[8:11], a[60:63], a[132:135], v[8:11]\n"
    "ds_read_b64_tr_b16 a[20:21], v219 offset:24704\n"
    "v_fma_f32 v2, v2, s36, v212\n"
    "v_mfma_f32_16x16x32_bf16 v[8:11], a[64:67], a[136:139], v[8:11]\n"
    "ds_read_b64_tr_b16 a[22:23], v219 offset:30848\n"
    "v_fma_f32 v3, v3, s36, v212\n"
    "v_mfma_f32_16x16x32_bf16 v[8:11], a[68:71], a[140:143], v[8:11]\n"
    "ds_read_b64_tr_b16 a[24:25], v220 offset:24704\n"
    "v_mfma_f32_16x16x32_bf16 v[12:15], a[72:75], a[120:123], 0\n"
    "ds_read_b64_tr_b16 a[26:27], v220 offset:30848\n"
    "v_mfma_f32_16x16x32_bf16 v[12:15], a[76:79], a[124:127], v[12:15]\n"
    "ds_read_b64_tr_b16 a[28:29], v221 offset:24704\n"
    "v_fma_f32 v4, v4, s36, v212\n"
    "v_mfma_f32_16x16x32_bf16 v[12:15], a[80:83], a[128:131], v[12:15]\n"
    "ds_read_b64_tr_b16 a[30:31], v221 offset:30848\n"
    "v_fma_f32 v5, v5, s36, v212\n"
    "v_mfma_f32_16x16x32_bf16 v[12:15], a[84:87], a[132:135], v[12:15]\n"
    "ds_read_b64_tr_b16 a[32:33], v218 offset:24832\n"
    "v_fma_f32 v6, v6, s36, v212\n"
    "v_mfma_f32_16x16x32_bf16 v[12:15], a[88:91], a[136:139], v[12:15]\n"
    "ds_read_b64_tr_b16 a[34:35], v218 offset:30976\n"
    "v_fma_f32 v7, v7, s36, v212\n"
    "v_mfma_f32_16x16x32_bf16 v[12:15], a[92:95], a[140:143], v[12:15]\n"
    "ds_read_b64_tr_b16 a[36:37], v219 offset:24832\n"
    "v_mfma_f32_16x16x32_bf16 v[16:19], a[48:51], a[144:147], 0\n"
    "ds_read_b64_tr_b16 a[38:39], v219 offset:30976\n"
    "v_mfma_f32_16x16x32_bf16 v[16:19], a[52:55], a[148:151], v[16:19]\n"
    "ds_read_b64_tr_b16 a[40:41], v220 offset:24832\n"
    "v_fma_f32 v8, v8, s36, v213\n"
    "v_mfma_f32_16x16x32_bf16 v[16:19], a[56:59], a[152:155], v[16:19]\n"
    "ds_read_b64_tr_b16 a[42:43], v220 offset:30976\n"
    "v_fma_f32 v9, v9, s36, v213\n"
    "v_mfma_f32_16x16x32_bf16 v[16:19], a[60:63], a[156:159], v[16:19]\n"
    "ds_read_b64_tr_b16 a[44:45], v221 offset:24832\n"
    "v_fma_f32 v10, v10, s36, v213\n"
    "v_mfma_f32_16x16x32_bf16 v[16:19], a[64:67], a[160:163], v[16:19]\n"
    "ds_read_b64_tr_b16 a[46:47], v221 offset:30976\n"
    "v_fma_f32 v11, v11, s36, v213\n"
    "v_mfma_f32_16x16x32_bf16 v[16:19], a[68:71], a[164:167], v[16:19]\n"
    "v_exp_f32 v0, v0\n"
    "v_mfma_f32_16x16x32_bf16 v[20:23], a[72:75], a[144:147], 0\n"
    "v_exp_f32 v1, v1\n"
    "v_mfma_f32_16x16x32_bf16 v[20:23], a[76:79], a[148:151], v[20:23]\n"
    "v_exp_f32 v2, v2\n"
    "v_mfma_f32_16x16x32_bf16 v[20:23], a[80:83], a[152:155], v[20:23]\n"
    "v_exp_f32 v3, v3\n"
    "v_mfma_f32_16x16x32_bf16 v[20:23], a[84:87], a[156:159], v[20:23]\n"
    "v_exp_f32 v4, v4\n"
    "v_mfma_f32_16x16x32_bf16 v[20:23], a[88:91], a[160:163], v[20:23]\n"
    "v_exp_f32 v5, v5\n"
    "v_mfma_f32_16x16x32_bf16 v[20:23], a[92:95], a[164:167], v[20:23]\n"
    "v_exp_f32 v6, v6\n"
    "v_mfma_f32_16x16x32_bf16 v[24:27], a[48:51], a[168:171], 0\n"
    "v_exp_f32 v7, v7\n"
    "v_mfma_f32_16x16x32_bf16 v[24:27], a[52:55], a[172:175], v[24:27]\n"
    "v_cvt_pk_bf16_f32 v0, v0, v1\n"
    "v_cvt_pk_bf16_f32 v1, v2, v3\n"
    "v_mfma_f32_16x16x32_bf16 v[24:27], a[56:59], a[176:179], v[24:27]\n"
    "v_cvt_pk_bf16_f32 v2, v4, v5\n"
    "v_cvt_pk_bf16_f32 v3, v6, v7\n"
    "v_mfma_f32_16x16x32_bf16 v[24:27], a[60:63], a[180:183], v[24:27]\n"
    "v_exp_f32 v8, v8\n"
    "v_mfma_f32_16x16x32_bf16 v[24:27], a[64:67], a[184:187], v[24:27]\n"
    "v_exp_f32 v9, v9\n"
    "v_mfma_f32_16x16x32_bf16 v[24:27], a[68:71], a[188:191], v[24:27]\n"
    "v_exp_f32 v10, v10\n"
    "v_mfma_f32_16x16x32_bf16 v[28:31], a[72:75], a[168:171], 0\n"
    "v_exp_f32 v11, v11\n"
    "v_mfma_f32_16x16x32_bf16 v[28:31], a[76:79], a[172:175], v[28:31]\n"
    "v_fma_f32 v12, v12, s36, v213\n"
    "v_fma_f32 v13, v13, s36, v213\n"
    "v_mfma_f32_16x16x32_bf16 v[28:31], a[80:83], a[176:179], v[28:31]\n"
    "v_exp_f32 v12, v12\n"
    "v_mfma_f32_16x16x32_bf16 v[28:31], a[84:87], a[180:183], v[28:31]\n"
    "v_exp_f32 v13, v13\n"
    "v_mfma_f32_16x16x32_bf16 v[28:31], a[88:91], a[184:187], v[28:31]\n"
    "v_fma_f32 v14, v14, s36, v213\n"
    "v_fma_f32 v15, v15, s36, v213\n"
    "v_mfma_f32_16x16x32_bf16 v[28:31], a[92:95], a[188:191], v[28:31]\n"
    "s_waitcnt vmcnt(14) lgkmcnt(0)\n"
    "s_barrier\n"
    "L_ph13:\n"
    "v_mfma_f32_16x16x32_bf16 v[64:67], a[0:3], v[32:35], v[64:67]\n"
    "buffer_load_dwordx4 v222, s[20:23], s42 offen nt lds\n"
    "v_mfma_f32_16x16x32_bf16 v[80:83], a[4:7], v[32:35], v[80:83]\n"
    "ds_read_b128 a[48:51], v216 offset:36864\n"
    "v_cvt_pk_bf16_f32 v8, v8, v9\n"
    "v_mfma_f32_16x16x32_bf16 v[96:99], a[8:11], v[32:35], v[96:99]\n"
    "buffer_load_dwordx4 v223, s[20:23], s42 offen offset:1024 nt lds\n"
    "v_mfma_f32_16x16x32_bf16 v[112:115], a[12:15], v[32:35], v[112:115]\n"
    "ds_read_b128 a[52:55], v217 offset:36864\n"
    "v_cvt_pk_bf16_f32 v9, v10, v11\n"
    "v_mfma_f32_16x16x32_bf16 v[128:131], a[16:19], v[32:35], v[128:131]\n"
    "buffer_load_dwordx4 v224, s[20:23], s42 offen offset:2048 nt lds\n"
    "v_mfma_f32_16x16x32_bf16 v[144:147], a[20:23], v[32:35], v[144:147]\n"
    "s_add_u32 m0, s44, 6144\n"
    "s_add_u32 s43, s41, 6144\n"
    "v_mfma_f32_16x16x32_bf16 v[160:163], a[24:27], v[32:35], v[160:163]\n"
    "buffer_load_dwordx4 v227, s[16:19], s43 offen offset:2048 nt lds\n"
    "v_mfma_f32_16x16x32_bf16 v[176:179], a[28:31], v[32:35], v[176:179]\n"
    "ds_read_b128 a[56:59], v216 offset:36992\n"
    "s_add_u32 m0, s37, 36864\n"
    "v_mfma_f32_16x16x32_bf16 a[192:195], a[32:35], v[32:35], a[192:195]\n"
    "ds_read_b128 a[60:63], v217 offset:36992\n"
    "s_add_u32 s42, s38, 98304\n"
    "v_mfma_f32_16x16x32_bf16 a[208:211], a[36:39], v[32:35], a[208:211]\n"
    "ds_read_b128 a[64:67], v216 offset:37120\n"
    "v_cvt_pk_bf16_f32 v10, v12, v13\n"
    "v_mfma_f32_16x16x32_bf16 a[224:227], a[40:43], v[32:35], a[224:227]\n"
    "ds_read_b128 a[68:71], v217 offset:37120\n"
    "v_fma_f32 v16, v16, s36, v214\n"
    "v_mfma_f32_16x16x32_bf16 a[240:243], a[44:47], v[32:35], a[240:243]\n"
    "ds_read_b128 a[72:75], v216 offset:43008\n"
    "v_fma_f32 v17, v17, s36, v214\n"
    "v_mfma_f32_16x16x32_bf16 v[192:195], v[208:211], v[32:35], v[192:195]\n"
    "ds_read_b128 a[76:79], v217 offset:43008\n"
    "v_fma_f32 v18, v18, s36, v214\n"
    "v_mfma_f32_16x16x32_bf16 v[68:71], a[0:3], v[40:43], v[68:71]\n"
    "ds_read_b128 a[80:83], v216 offset:43136\n"
    "v_fma_f32 v19, v19, s36, v214\n"
    "v_mfma_f32_16x16x32_bf16 v[84:87], a[4:7], v[40:43], v[84:87]\n"
    "ds_read_b128 a[84:87], v217 offset:43136\n"
    "v_fma_f32 v20, v20, s36, v214\n"
    "v_mfma_f32_16x16x32_bf16 v[100:103], a[8:11], v[40:43], v[100:103]\n"
    "ds_read_b128 a[88:91], v216 offset:43264\n"
    "v_fma_f32 v21, v21, s36, v214\n"
    "v_mfma_f32_16x16x32_bf16 v[116:119], a[12:15], v[40:43], v[116:119]\n"
    "ds_read_b128 a[92:95], v217 offset:43264\n"
    "v_fma_f32 v22, v22, s36, v214\n"
    "v_mfma_f32_16x16x32_bf16 v[132:135], a[16:19], v[40:43], v[132:135]\n"
    "v_exp_f32 v14, v14\n"
    "v_mfma_f32_16x16x32_bf16 v[148:151], a[20:23], v[40:43], v[148:151]\n"
    "v_exp_f32 v15, v15\n"
    "v_mfma_f32_16x16x32_bf16 v[164:167], a[24:27], v[40:43], v[164:167]\n"
    "v_cvt_pk_bf16_f32 v11, v14, v15\n"
    "v_fma_f32 v23, v23, s36, v214\n"
    "v_mfma_f32_16x16x32_bf16 v[180:183], a[28:31], v[40:43], v[180:183]\n"
    "v_exp_f32 v16, v16\n"
    "v_mfma_f32_16x16x32_bf16 a[196:199], a[32:35], v[40:43], a[196:199]\n"
    "v_exp_f32 v17, v17\n"
    "v_mfma_f32_16x16x32_bf16 a[212:215], a[36:39], v[40:43], a[212:215]\n"
    "v_exp_f32 v18, v18\n"
    "v_mfma_f32_16x16x32_bf16 a[228:231], a[40:43], v[40:43], a[228:231]\n"
    "v_exp_f32 v19, v19\n"
    "v_mfma_f32_16x16x32_bf16 a[244:247], a[44:47], v[40:43], a[244:247]\n"
    "v_exp_f32 v20, v20\n"
    "v_mfma_f32_16x16x32_bf16 v[196:199], v[208:211], v[40:43], v[196:199]\n"
    "v_exp_f32 v21, v21\n"
    "v_mfma_f32_16x16x32_bf16 v[72:75], a[0:3], v[48:51], v[72:75]\n"
    "v_exp_f32 v22, v22\n"
    "v_mfma_f32_16x16x32_bf16 v[88:91], a[4:7], v[48:51], v[88:91]\n"
    "v_exp_f32 v23, v23\n"
    "v_mfma_f32_16x16x32_bf16 v[104:107], a[8:11], v[48:51], v[104:107]\n"
    "v_cvt_pk_bf16_f32 v16, v16, v17\n"
    "v_cvt_pk_bf16_f32 v17, v18, v19\n"
    "v_mfma_f32_16x16x32_bf16 v[120:123], a[12:15], v[48:51], v[120:123]\n"
    "v_cvt_pk_bf16_f32 v18, v20, v21\n"
    "v_cvt_pk_bf16_f32 v19, v22, v23\n"
    "v_mfma_f32_16x16x32_bf16 v[136:139], a[16:19], v[48:51], v[136:139]\n"
    "v_fma_f32 v24, v24, s36, v215\n"
    "v_fma_f32 v25, v25, s36, v215\n"
    "v_mfma_f32_16x16x32_bf16 v[152:155], a[20:23], v[48:51], v[152:155]\n"
    "v_exp_f32 v24, v24\n"
    "v_mfma_f32_16x16x32_bf16 v[168:171], a[24:27], v[48:51], v[168:171]\n"
    "v_exp_f32 v25, v25\n"
    "v_mfma_f32_16x16x32_bf16 v[184:187], a[28:31], v[48:51], v[184:187]\n"
    "v_fma_f32 v26, v26, s36, v215\n"
    "v_fma_f32 v27, v27, s36, v215\n"
    "v_mfma_f32_16x16x32_bf16 a[200:203], a[32:35], v[48:51], a[200:203]\n"
    "v_exp_f32 v26, v26\n"
    "v_mfma_f32_16x16x32_bf16 a[216:219], a[36:39], v[48:51], a[216:219]\n"
    "v_exp_f32 v27, v27\n"
    "v_mfma_f32_16x16x32_bf16 a[232:235], a[40:43], v[48:51], a[232:235]\n"
    "v_fma_f32 v28, v28, s36, v215\n"
    "v_fma_f32 v29, v29, s36, v215\n"
    "v_mfma_f32_16x16x32_bf16 a[248:251], a[44:47], v[48:51], a[248:251]\n"
    "v_exp_f32 v28, v28\n"
    "v_mfma_f32_16x16x32_bf16 v[200:203], v[208:211], v[48:51], v[200:203]\n"
    "v_exp_f32 v29, v29\n"
    "v_mfma_f32_16x16x32_bf16 v[76:79], a[0:3], v[56:59], v[76:79]\n"
    "v_fma_f32 v30, v30, s36, v215\n"
    "v_fma_f32 v31, v31, s36, v215\n"
    "v_mfma_f32_16x16x32_bf16 v[92:95], a[4:7], v[56:59], v[92:95]\n"
    "v_exp_f32 v30, v30\n"
    "v_mfma_f32_16x16x32_bf16 v[108:111], a[8:11], v[56:59], v[108:111]\n"
    "v_exp_f32 v31, v31\n"
    "v_mfma_f32_16x16x32_bf16 v[124:127], a[12:15], v[56:59], v[124:127]\n"
    "v_cvt_pk_bf16_f32 v24, v24, v25\n"
    "v_cvt_pk_bf16_f32 v25, v26, v27\n"
    "v_mfma_f32_16x16x32_bf16 v[140:143], a[16:19], v[56:59], v[140:143]\n"
    "v_cvt_pk_bf16_f32 v26, v28, v29\n"
    "v_cvt_pk_bf16_f32 v27, v30, v31\n"
    "v_mfma_f32_16x16x32_bf16 v[156:159], a[20:23], v[56:59], v[156:159]\n"
    "v_mfma_f32_16x16x32_bf16 v[172:175], a[24:27], v[56:59], v[172:175]\n"
    "v_mfma_f32_16x16x32_bf16 v[188:191], a[28:31], v[56:59], v[188:191]\n"
    "v_mfma_f32_16x16x32_bf16 a[204:207], a[32:35], v[56:59], a[204:207]\n"
    "v_mfma_f32_16x16x32_bf16 a[220:223], a[36:39], v[56:59], a[220:223]\n"
    "v_mfma_f32_16x16x32_bf16 a[236:239], a[40:43], v[56:59], a[236:239]\n"
    "v_mfma_f32_16x16x32_bf16 a[252:255], a[44:47], v[56:59], a[252:255]\n"
    "v_mfma_f32_16x16x32_bf16 v[204:207], v[208:211], v[56:59], v[204:207]\n"
    "s_waitcnt vmcnt(14) lgkmcnt(0)\n"
    "s_barrier\n"
    "L_ph14:\n"
    "v_mfma_f32_16x16x32_bf16 v[32:35], a[48:51], a[96:99], 0\n"
    "buffer_load_dwordx4 v231, s[24:27], s42 offen nt lds\n"
    "v_mfma_f32_16x16x32_bf16 v[32:35], a[52:55], a[100:103], v[32:35]\n"
    "ds_read_b64_tr_b16 a[0:1], v218 offset:49152\n"
    "v_mfma_f32_16x16x32_bf16 v[32:35], a[56:59], a[104:107], v[32:35]\n"
    "buffer_load_dwordx4 v232, s[24:27], s42 offen offset:1024 nt lds\n"
    "v_mfma_f32_16x16x32_bf16 v[32:35], a[60:63], a[108:111], v[32:35]\n"
    "ds_read_b64_tr_b16 a[2:3], v218 offset:55296\n"
    "v_mfma_f32_16x16x32_bf16 v[32:35], a[64:67], a[112:115], v[32:35]\n"
    "buffer_load_dwordx4 v233, s[24:27], s42 offen offset:2048 nt lds\n"
    "v_mfma_f32_16x16x32_bf16 v[32:35], a[68:71], a[116:119], v[32:35]\n"
    "s_add_u32 m0, s44, 9216\n"
    "s_add_u32 s43, s41, 9216\n"
    "v_mfma_f32_16x16x32_bf16 v[36:39], a[72:75], a[96:99], 0\n"
    "buffer_load_dwordx4 v228, s[16:19], s43 offen nt lds\n"
    "v_mfma_f32_16x16x32_bf16 v[36:39], a[76:79], a[100:103], v[36:39]\n"
    "ds_read_b64_tr_b16 a[4:5], v219 offset:49152\n"
    "s_add_u32 m0, s37, 49152\n"
    "v_mfma_f32_16x16x32_bf16 v[36:39], a[80:83], a[104:107], v[36:39]\n"
    "ds_read_b64_tr_b16 a[6:7], v219 offset:55296\n"
    "s_add_u32 s42, s38, 122880\n"
    "v_mfma_f32_16x16x32_bf16 v[36:39], a[84:87], a[108:111], v[36:39]\n"
    "ds_read_b64_tr_b16 a[8:9], v220 offset:49152\n"
    "v_mfma_f32_16x16x32_bf16 v[36:39], a[88:91], a[112:115], v[36:39]\n"
    "ds_read_b64_tr_b16 a[10:11], v220 offset:55296\n"
    "v_mfma_f32_16x16x32_bf16 v[36:39], a[92:95], a[116:119], v[36:39]\n"
    "ds_read_b64_tr_b16 a[12:13], v221 offset:49152\n"
    "v_mfma_f32_16x16x32_bf16 v[40:43], a[48:51], a[120:123], 0\n"
    "ds_read_b64_tr_b16 a[14:15], v221 offset:55296\n"
    "v_mfma_f32_16x16x32_bf16 v[40:43], a[52:55], a[124:127], v[40:43]\n"
    "ds_read_b64_tr_b16 a[16:17], v218 offset:49280\n"
    "v_fma_f32 v32, v32, s36, v212\n"
    "v_mfma_f32_16x16x32_bf16 v[40:43], a[56:59], a[128:131], v[40:43]\n"
    "ds_read_b64_tr_b16 a[18:19], v218 offset:55424\n"
    "v_fma_f32 v33, v33, s36, v212\n"
    "v_mfma_f32_16x16x32_bf16 v[40:43], a[60:63], a[132:135], v[40:43]\n"
    "ds_read_b64_tr_b16 a[20:21], v219 offset:49280\n"
    "v_fma_f32 v34, v34, s36, v212\n"
    "v_mfma_f32_16x16x32_bf16 v[40:43], a[64:67], a[136:139], v[40:43]\n"
    "ds_read_b64_tr_b16 a[22:23], v219 offset:55424\n"
    "v_fma_f32 v35, v35, s36, v212\n"
    "v_mfma_f32_16x16x32_bf16 v[40:43], a[68:71], a[140:143], v[40:43]\n"
    "ds_read_b64_tr_b16 a[24:25], v220 offset:49280\n"
    "v_mfma_f32_16x16x32_bf16 v[44:47], a[72:75], a[120:123], 0\n"
    "ds_read_b64_tr_b16 a[26:27], v220 offset:55424\n"
    "v_mfma_f32_16x16x32_bf16 v[44:47], a[76:79], a[124:127], v[44:47]\n"
    "ds_read_b64_tr_b16 a[28:29], v221 offset:49280\n"
    "v_fma_f32 v36, v36, s36, v212\n"
    "v_mfma_f32_16x16x32_bf16 v[44:47], a[80:83], a[128:131], v[44:47]\n"
    "ds_read_b64_tr_b16 a[30:31], v221 offset:55424\n"
    "v_fma_f32 v37, v37, s36, v212\n"
    "v_mfma_f32_16x16x32_bf16 v[44:47], a[84:87], a[132:135], v[44:47]\n"
    "ds_read_b64_tr_b16 a[32:33], v218 offset:49408\n"
    "v_fma_f32 v38, v38, s36, v212\n"
    "v_mfma_f32_16x16x32_bf16 v[44:47], a[88:91], a[136:139], v[44:47]\n"
    "ds_read_b64_tr_b16 a[34:35], v218 offset:55552\n"
    "v_fma_f32 v39, v39, s36, v212\n"
    "v_mfma_f32_16x16x32_bf16 v[44:47], a[92:95], a[140:143], v[44:47]\n"
    "ds_read_b64_tr_b16 a[36:37], v219 offset:49408\n"
    "v_mfma_f32_16x16x32_bf16 v[48:51], a[48:51], a[144:147], 0\n"
    "ds_read_b64_tr_b16 a[38:39], v219 offset:55552\n"
    "v_mfma_f32_16x16x32_bf16 v[48:51], a[52:55], a[148:151], v[48:51]\n"
    "ds_read_b64_tr_b16 a[40:41], v220 offset:49408\n"
    "v_fma_f32 v40, v40, s36, v213\n"
    "v_mfma_f32_16x16x32_bf16 v[48:51], a[56:59], a[152:155], v[48:51]\n"
    "ds_read_b64_tr_b16 a[42:43], v220 offset:55552\n"
    "v_fma_f32 v41, v41, s36, v213\n"
    "v_mfma_f32_16x16x32_bf16 v[48:51], a[60:63], a[156:159], v[48:51]\n"
    "ds_read_b64_tr_b16 a[44:45], v221 offset:49408\n"
    "v_fma_f32 v42, v42, s36, v213\n"
    "v_mfma_f32_16x16x32_bf16 v[48:51], a[64:67], a[160:163], v[48:51]\n"
    "ds_read_b64_tr_b16 a[46:47], v221 offset:55552\n"
    "v_fma_f32 v43, v43, s36, v213\n"
    "v_mfma_f32_16x16x32_bf16 v[48:51], a[68:71], a[164:167], v[48:51]\n"
    "v_exp_f32 v32, v32\n"
    "v_mfma_f32_16x16x32_bf16 v[52:55], a[72:75], a[144:147], 0\n"
    "v_exp_f32 v33, v33\n"
    "v_mfma_f32_16x16x32_bf16 v[52:55], a[76:79], a[148:151], v[52:55]\n"
    "v_exp_f32 v34, v34\n"
    "v_mfma_f32_16x16x32_bf16 v[52:55], a[80:83], a[152:155], v[52:55]\n"
    "v_exp_f32 v35, v35\n"
    "v_mfma_f32_16x16x32_bf16 v[52:55], a[84:87], a[156:159], v[52:55]\n"
    "v_exp_f32 v36, v36\n"
    "v_mfma_f32_16x16x32_bf16 v[52:55], a[88:91], a[160:163], v[52:55]\n"
    "v_exp_f32 v37, v37\n"
    "v_mfma_f32_16x16x32_bf16 v[52:55], a[92:95], a[164:167], v[52:55]\n"
    "v_exp_f32 v38, v38\n"
    "v_mfma_f32_16x16x32_bf16 v[56:59], a[48:51], a[168:171], 0\n"
    "v_exp_f32 v39, v39\n"
    "v_mfma_f32_16x16x32_bf16 v[56:59], a[52:55], a[172:175], v[56:59]\n"
    "v_cvt_pk_bf16_f32 v32, v32, v33\n"
    "v_cvt_pk_bf16_f32 v33, v34, v35\n"
    "v_mfma_f32_16x16x32_bf16 v[56:59], a[56:59], a[176:179], v[56:59]\n"
    "v_cvt_pk_bf16_f32 v34, v36, v37\n"
    "v_cvt_pk_bf16_f32 v35, v38, v39\n"
    "v_mfma_f32_16x16x32_bf16 v[56:59], a[60:63], a[180:183], v[56:59]\n"
    "v_exp_f32 v40, v40\n"
    "v_mfma_f32_16x16x32_bf16 v[56:59], a[64:67], a[184:187], v[56:59]\n"
    "v_exp_f32 v41, v41\n"
    "v_mfma_f32_16x16x32_bf16 v[56:59], a[68:71], a[188:191], v[56:59]\n"
    "v_exp_f32 v42, v42\n"
    "v_mfma_f32_16x16x32_bf16 v[60:63], a[72:75], a[168:171], 0\n"
    "v_exp_f32 v43, v43\n"
    "v_mfma_f32_16x16x32_bf16 v[60:63], a[76:79], a[172:175], v[60:63]\n"
    "v_fma_f32 v44, v44, s36, v213\n"
    "v_fma_f32 v45, v45, s36, v213\n"
    "v_mfma_f32_16x16x32_bf16 v[60:63], a[80:83], a[176:179], v[60:63]\n"
    "v_exp_f32 v44, v44\n"
    "v_mfma_f32_16x16x32_bf16 v[60:63], a[84:87], a[180:183], v[60:63]\n"
    "v_exp_f32 v45, v45\n"
    "v_mfma_f32_16x16x32_bf16 v[60:63], a[88:91], a[184:187], v[60:63]\n"
    "v_fma_f32 v46, v46, s36, v213\n"
    "v_fma_f32 v47, v47, s36, v213\n"
    "v_mfma_f32_16x16x32_bf16 v[60:63], a[92:95], a[188:191], v[60:63]\n"
    "s_waitcnt vmcnt(14) lgkmcnt(0)\n"
    "s_barrier\n"
    "L_ph15:\n"
    "v_mfma_f32_16x16x32_bf16 v[64:67], a[0:3], v[0:3], v[64:67]\n"
    "buffer_load_dwordx4 v222, s[20:23], s42 offen nt lds\n"
    "v_mfma_f32_16x16x32_bf16 v[80:83], a[4:7], v[0:3], v[80:83]\n"
    "ds_read_b128 a[48:51], v216 offset:0\n"
    "v_cvt_pk_bf16_f32 v40, v40, v41\n"
    "v_mfma_f32_16x16x32_bf16 v[96:99], a[8:11], v[0:3], v[96:99]\n"
    "buffer_load_dwordx4 v223, s[20:23], s42 offen offset:1024 nt lds\n"
    "v_mfma_f32_16x16x32_bf16 v[112:115], a[12:15], v[0:3], v[112:115]\n"
    "ds_read_b128 a[52:55], v217 offset:0\n"
    "v_cvt_pk_bf16_f32 v41, v42, v43\n"
    "v_mfma_f32_16x16x32_bf16 v[128:131], a[16:19], v[0:3], v[128:131]\n"
    "buffer_load_dwordx4 v224, s[20:23], s42 offen offset:2048 nt lds\n"
    "v_mfma_f32_16x16x32_bf16 v[144:147], a[20:23], v[0:3], v[144:147]\n"
    "s_add_u32 m0, s44, 9216\n"
    "s_add_u32 s43, s41, 9216\n"
    "v_mfma_f32_16x16x32_bf16 v[160:163], a[24:27], v[0:3], v[160:163]\n"
    "buffer_load_dwordx4 v229, s[16:19], s43 offen offset:1024 nt lds\n"
    "v_mfma_f32_16x16x32_bf16 v[176:179], a[28:31], v[0:3], v[176:179]\n"
    "s_mov_b32 m0, 159744\n"
    "s_mov_b32 s51, L_ph19-L_code_start\n"
    "v_mfma_f32_16x16x32_bf16 a[192:195], a[32:35], v[0:3], a[192:195]\n"
    "buffer_load_dword v249, s[52:55], s51 offen lds\n"
    "v_mfma_f32_16x16x32_bf16 a[208:211], a[36:39], v[0:3], a[208:211]\n"
    "ds_read_b128 a[56:59], v216 offset:128\n"
    "s_add_u32 m0, s37, 0\n"
    "v_mfma_f32_16x16x32_bf16 a[224:227], a[40:43], v[0:3], a[224:227]\n"
    "ds_read_b128 a[60:63], v217 offset:128\n"
    "s_add_u32 s42, s38, 110592\n"
    "v_mfma_f32_16x16x32_bf16 a[240:243], a[44:47], v[0:3], a[240:243]\n"
    "ds_read_b128 a[64:67], v216 offset:256\n"
    "v_cvt_pk_bf16_f32 v42, v44, v45\n"
    "v_mfma_f32_16x16x32_bf16 v[192:195], v[208:211], v[0:3], v[192:195]\n"
    "ds_read_b128 a[68:71], v217 offset:256\n"
    "v_fma_f32 v48, v48, s36, v214\n"
    "v_mfma_f32_16x16x32_bf16 v[68:71], a[0:3], v[8:11], v[68:71]\n"
    "ds_read_b128 a[72:75], v216 offset:6144\n"
    "v_fma_f32 v49, v49, s36, v214\n"
    "v_mfma_f32_16x16x32_bf16 v[84:87], a[4:7], v[8:11], v[84:87]\n"
    "ds_read_b128 a[76:79], v217 offset:6144\n"
    "v_fma_f32 v50, v50, s36, v214\n"
    "v_mfma_f32_16x16x32_bf16 v[100:103], a[8:11], v[8:11], v[100:103]\n"
    "ds_read_b128 a[80:83], v216 offset:6272\n"
    "v_fma_f32 v51, v51, s36, v214\n"
    "v_mfma_f32_16x16x32_bf16 v[116:119], a[12:15], v[8:11], v[116:119]\n"
    "ds_read_b128 a[84:87], v217 offset:6272\n"
    "v_fma_f32 v52, v52, s36, v214\n"
    "v_mfma_f32_16x16x32_bf16 v[132:135], a[16:19], v[8:11], v[132:135]\n"
    "ds_read_b128 a[88:91], v216 offset:6400\n"
    "v_fma_f32 v53, v53, s36, v214\n"
    "v_mfma_f32_16x16x32_bf16 v[148:151], a[20:23], v[8:11], v[148:151]\n"
    "ds_read_b128 a[92:95], v217 offset:6400\n"
    "v_fma_f32 v54, v54, s36, v214\n"
    "v_mfma_f32_16x16x32_bf16 v[164:167], a[24:27], v[8:11], v[164:167]\n"
    "v_exp_f32 v46, v46\n"
    "v_mfma_f32_16x16x32_bf16 v[180:183], a[28:31], v[8:11], v[180:183]\n"
    "v_exp_f32 v47, v47\n"
    "v_mfma_f32_16x16x32_bf16 a[196:199], a[32:35], v[8:11], a[196:199]\n"
    "v_cvt_pk_bf16_f32 v43, v46, v47\n"
    "v_fma_f32 v55, v55, s36, v214\n"
    "v_mfma_f32_16x16x32_bf16 a[212:215], a[36:39], v[8:11], a[212:215]\n"
    "v_exp_f32 v48, v48\n"
    "v_mfma_f32_16x16x32_bf16 a[228:231], a[40:43], v[8:11], a[228:231]\n"
    "v_exp_f32 v49, v49\n"
    "v_mfma_f32_16x16x32_bf16 a[244:247], a[44:47], v[8:11], a[244:247]\n"
    "v_exp_f32 v50, v50\n"
    "v_mfma_f32_16x16x32_bf16 v[196:199], v[208:211], v[8:11], v[196:199]\n"
    "v_exp_f32 v51, v51\n"
    "v_mfma_f32_16x16x32_bf16 v[72:75], a[0:3], v[16:19], v[72:75]\n"
    "v_exp_f32 v52, v52\n"
    "v_mfma_f32_16x16x32_bf16 v[88:91], a[4:7], v[16:19], v[88:91]\n"
    "v_exp_f32 v53, v53\n"
    "v_mfma_f32_16x16x32_bf16 v[104:107], a[8:11], v[16:19], v[104:107]\n"
    "v_exp_f32 v54, v54\n"
    "v_mfma_f32_16x16x32_bf16 v[120:123], a[12:15], v[16:19], v[120:123]\n"
    "v_exp_f32 v55, v55\n"
    "v_mfma_f32_16x16x32_bf16 v[136:139], a[16:19], v[16:19], v[136:139]\n"
    "v_cvt_pk_bf16_f32 v48, v48, v49\n"
    "v_cvt_pk_bf16_f32 v49, v50, v51\n"
    "v_mfma_f32_16x16x32_bf16 v[152:155], a[20:23], v[16:19], v[152:155]\n"
    "v_cvt_pk_bf16_f32 v50, v52, v53\n"
    "v_cvt_pk_bf16_f32 v51, v54, v55\n"
    "v_mfma_f32_16x16x32_bf16 v[168:171], a[24:27], v[16:19], v[168:171]\n"
    "v_fma_f32 v56, v56, s36, v215\n"
    "v_fma_f32 v57, v57, s36, v215\n"
    "v_mfma_f32_16x16x32_bf16 v[184:187], a[28:31], v[16:19], v[184:187]\n"
    "v_exp_f32 v56, v56\n"
    "v_mfma_f32_16x16x32_bf16 a[200:203], a[32:35], v[16:19], a[200:203]\n"
    "v_exp_f32 v57, v57\n"
    "v_mfma_f32_16x16x32_bf16 a[216:219], a[36:39], v[16:19], a[216:219]\n"
    "v_fma_f32 v58, v58, s36, v215\n"
    "v_fma_f32 v59, v59, s36, v215\n"
    "v_mfma_f32_16x16x32_bf16 a[232:235], a[40:43], v[16:19], a[232:235]\n"
    "v_exp_f32 v58, v58\n"
    "v_mfma_f32_16x16x32_bf16 a[248:251], a[44:47], v[16:19], a[248:251]\n"
    "v_exp_f32 v59, v59\n"
    "v_mfma_f32_16x16x32_bf16 v[200:203], v[208:211], v[16:19], v[200:203]\n"
    "v_fma_f32 v60, v60, s36, v215\n"
    "v_fma_f32 v61, v61, s36, v215\n"
    "v_mfma_f32_16x16x32_bf16 v[76:79], a[0:3], v[24:27], v[76:79]\n"
    "v_exp_f32 v60, v60\n"
    "v_mfma_f32_16x16x32_bf16 v[92:95], a[4:7], v[24:27], v[92:95]\n"
    "v_exp_f32 v61, v61\n"
    "v_mfma_f32_16x16x32_bf16 v[108:111], a[8:11], v[24:27], v[108:111]\n"
    "v_fma_f32 v62, v62, s36, v215\n"
    "v_fma_f32 v63, v63, s36, v215\n"
    "v_mfma_f32_16x16x32_bf16 v[124:127], a[12:15], v[24:27], v[124:127]\n"
    "v_exp_f32 v62, v62\n"
    "v_mfma_f32_16x16x32_bf16 v[140:143], a[16:19], v[24:27], v[140:143]\n"
    "v_exp_f32 v63, v63\n"
    "v_mfma_f32_16x16x32_bf16 v[156:159], a[20:23], v[24:27], v[156:159]\n"
    "v_cvt_pk_bf16_f32 v56, v56, v57\n"
    "v_cvt_pk_bf16_f32 v57, v58, v59\n"
    "v_mfma_f32_16x16x32_bf16 v[172:175], a[24:27], v[24:27], v[172:175]\n"
    "v_cvt_pk_bf16_f32 v58, v60, v61\n"
    "v_cvt_pk_bf16_f32 v59, v62, v63\n"
    "v_mfma_f32_16x16x32_bf16 v[188:191], a[28:31], v[24:27], v[188:191]\n"
    "v_mfma_f32_16x16x32_bf16 a[204:207], a[32:35], v[24:27], a[204:207]\n"
    "v_mfma_f32_16x16x32_bf16 a[220:223], a[36:39], v[24:27], a[220:223]\n"
    "v_mfma_f32_16x16x32_bf16 a[236:239], a[40:43], v[24:27], a[236:239]\n"
    "v_mfma_f32_16x16x32_bf16 a[252:255], a[44:47], v[24:27], a[252:255]\n"
    "v_mfma_f32_16x16x32_bf16 v[204:207], v[208:211], v[24:27], v[204:207]\n"
    "s_waitcnt vmcnt(14) lgkmcnt(0)\n"
    "s_barrier\n"
    "L_ph16:\n"
    "v_mfma_f32_16x16x32_bf16 v[0:3], a[48:51], a[96:99], 0\n"
    "buffer_load_dwordx4 v231, s[24:27], s42 offen nt lds\n"
    "v_mfma_f32_16x16x32_bf16 v[0:3], a[52:55], a[100:103], v[0:3]\n"
    "ds_read_b64_tr_b16 a[0:1], v218 offset:12288\n"
    "v_mfma_f32_16x16x32_bf16 v[0:3], a[56:59], a[104:107], v[0:3]\n"
    "buffer_load_dwordx4 v232, s[24:27], s42 offen offset:1024 nt lds\n"
    "v_mfma_f32_16x16x32_bf16 v[0:3], a[60:63], a[108:111], v[0:3]\n"
    "ds_read_b64_tr_b16 a[2:3], v218 offset:18432\n"
    "v_mfma_f32_16x16x32_bf16 v[0:3], a[64:67], a[112:115], v[0:3]\n"
    "buffer_load_dwordx4 v233, s[24:27], s42 offen offset:2048 nt lds\n"
    "v_mfma_f32_16x16x32_bf16 v[0:3], a[68:71], a[116:119], v[0:3]\n"
    "s_add_u32 m0, s44, 9216\n"
    "s_add_u32 s43, s41, 9216\n"
    "v_mfma_f32_16x16x32_bf16 v[4:7], a[72:75], a[96:99], 0\n"
    "buffer_load_dwordx4 v230, s[16:19], s43 offen offset:2048 nt lds\n"
    "v_mfma_f32_16x16x32_bf16 v[4:7], a[76:79], a[100:103], v[4:7]\n"
    "ds_read_b64_tr_b16 a[4:5], v219 offset:12288\n"
    "s_add_u32 m0, s37, 12288\n"
    "v_mfma_f32_16x16x32_bf16 v[4:7], a[80:83], a[104:107], v[4:7]\n"
    "ds_read_b64_tr_b16 a[6:7], v219 offset:18432\n"
    "s_add_u32 s42, s38, 135168\n"
    "v_mfma_f32_16x16x32_bf16 v[4:7], a[84:87], a[108:111], v[4:7]\n"
    "ds_read_b64_tr_b16 a[8:9], v220 offset:12288\n"
    "v_mfma_f32_16x16x32_bf16 v[4:7], a[88:91], a[112:115], v[4:7]\n"
    "ds_read_b64_tr_b16 a[10:11], v220 offset:18432\n"
    "v_mfma_f32_16x16x32_bf16 v[4:7], a[92:95], a[116:119], v[4:7]\n"
    "ds_read_b64_tr_b16 a[12:13], v221 offset:12288\n"
    "v_mfma_f32_16x16x32_bf16 v[8:11], a[48:51], a[120:123], 0\n"
    "ds_read_b64_tr_b16 a[14:15], v221 offset:18432\n"
    "v_mfma_f32_16x16x32_bf16 v[8:11], a[52:55], a[124:127], v[8:11]\n"
    "ds_read_b64_tr_b16 a[16:17], v218 offset:12416\n"
    "v_fma_f32 v0, v0, s36, v212\n"
    "v_mfma_f32_16x16x32_bf16 v[8:11], a[56:59], a[128:131], v[8:11]\n"
    "ds_read_b64_tr_b16 a[18:19], v218 offset:18560\n"
    "v_fma_f32 v1, v1, s36, v212\n"
    "v_mfma_f32_16x16x32_bf16 v[8:11], a[60:63], a[132:135], v[8:11]\n"
    "ds_read_b64_tr_b16 a[20:21], v219 offset:12416\n"
    "v_fma_f32 v2, v2, s36, v212\n"
    "v_mfma_f32_16x16x32_bf16 v[8:11], a[64:67], a[136:139], v[8:11]\n"
    "ds_read_b64_tr_b16 a[22:23], v219 offset:18560\n"
    "v_fma_f32 v3, v3, s36, v212\n"
    "v_mfma_f32_16x16x32_bf16 v[8:11], a[68:71], a[140:143], v[8:11]\n"
    "ds_read_b64_tr_b16 a[24:25], v220 offset:12416\n"
    "v_mfma_f32_16x16x32_bf16 v[12:15], a[72:75], a[120:123], 0\n"
    "ds_read_b64_tr_b16 a[26:27], v220 offset:18560\n"
    "v_mfma_f32_16x16x32_bf16 v[12:15], a[76:79], a[124:127], v[12:15]\n"
    "ds_read_b64_tr_b16 a[28:29], v221 offset:12416\n"
    "v_fma_f32 v4, v4, s36, v212\n"
    "v_mfma_f32_16x16x32_bf16 v[12:15], a[80:83], a[128:131], v[12:15]\n"
    "ds_read_b64_tr_b16 a[30:31], v221 offset:18560\n"
    "v_fma_f32 v5, v5, s36, v212\n"
    "v_mfma_f32_16x16x32_bf16 v[12:15], a[84:87], a[132:135], v[12:15]\n"
    "ds_read_b64_tr_b16 a[32:33], v218 offset:12544\n"
    "v_fma_f32 v6, v6, s36, v212\n"
    "v_mfma_f32_16x16x32_bf16 v[12:15], a[88:91], a[136:139], v[12:15]\n"
    "ds_read_b64_tr_b16 a[34:35], v218 offset:18688\n"
    "v_fma_f32 v7, v7, s36, v212\n"
    "v_mfma_f32_16x16x32_bf16 v[12:15], a[92:95], a[140:143], v[12:15]\n"
    "ds_read_b64_tr_b16 a[36:37], v219 offset:12544\n"
    "v_mfma_f32_16x16x32_bf16 v[16:19], a[48:51], a[144:147], 0\n"
    "ds_read_b64_tr_b16 a[38:39], v219 offset:18688\n"
    "v_mfma_f32_16x16x32_bf16 v[16:19], a[52:55], a[148:151], v[16:19]\n"
    "ds_read_b64_tr_b16 a[40:41], v220 offset:12544\n"
    "v_fma_f32 v8, v8, s36, v213\n"
    "v_mfma_f32_16x16x32_bf16 v[16:19], a[56:59], a[152:155], v[16:19]\n"
    "ds_read_b64_tr_b16 a[42:43], v220 offset:18688\n"
    "v_fma_f32 v9, v9, s36, v213\n"
    "v_mfma_f32_16x16x32_bf16 v[16:19], a[60:63], a[156:159], v[16:19]\n"
    "ds_read_b64_tr_b16 a[44:45], v221 offset:12544\n"
    "v_fma_f32 v10, v10, s36, v213\n"
    "v_mfma_f32_16x16x32_bf16 v[16:19], a[64:67], a[160:163], v[16:19]\n"
    "ds_read_b64_tr_b16 a[46:47], v221 offset:18688\n"
    "v_fma_f32 v11, v11, s36, v213\n"
    "v_mfma_f32_16x16x32_bf16 v[16:19], a[68:71], a[164:167], v[16:19]\n"
    "v_exp_f32 v0, v0\n"
    "v_mfma_f32_16x16x32_bf16 v[20:23], a[72:75], a[144:147], 0\n"
    "v_exp_f32 v1, v1\n"
    "v_mfma_f32_16x16x32_bf16 v[20:23], a[76:79], a[148:151], v[20:23]\n"
    "v_exp_f32 v2, v2\n"
    "v_mfma_f32_16x16x32_bf16 v[20:23], a[80:83], a[152:155], v[20:23]\n"
    "v_exp_f32 v3, v3\n"
    "v_mfma_f32_16x16x32_bf16 v[20:23], a[84:87], a[156:159], v[20:23]\n"
    "v_exp_f32 v4, v4\n"
    "v_mfma_f32_16x16x32_bf16 v[20:23], a[88:91], a[160:163], v[20:23]\n"
    "v_exp_f32 v5, v5\n"
    "v_mfma_f32_16x16x32_bf16 v[20:23], a[92:95], a[164:167], v[20:23]\n"
    "v_exp_f32 v6, v6\n"
    "v_mfma_f32_16x16x32_bf16 v[24:27], a[48:51], a[168:171], 0\n"
    "v_exp_f32 v7, v7\n"
    "v_mfma_f32_16x16x32_bf16 v[24:27], a[52:55], a[172:175], v[24:27]\n"
    "v_cvt_pk_bf16_f32 v0, v0, v1\n"
    "v_cvt_pk_bf16_f32 v1, v2, v3\n"
    "v_mfma_f32_16x16x32_bf16 v[24:27], a[56:59], a[176:179], v[24:27]\n"
    "v_cvt_pk_bf16_f32 v2, v4, v5\n"
    "v_cvt_pk_bf16_f32 v3, v6, v7\n"
    "v_mfma_f32_16x16x32_bf16 v[24:27], a[60:63], a[180:183], v[24:27]\n"
    "v_exp_f32 v8, v8\n"
    "v_mfma_f32_16x16x32_bf16 v[24:27], a[64:67], a[184:187], v[24:27]\n"
    "v_exp_f32 v9, v9\n"
    "v_mfma_f32_16x16x32_bf16 v[24:27], a[68:71], a[188:191], v[24:27]\n"
    "v_exp_f32 v10, v10\n"
    "v_mfma_f32_16x16x32_bf16 v[28:31], a[72:75], a[168:171], 0\n"
    "v_exp_f32 v11, v11\n"
    "v_mfma_f32_16x16x32_bf16 v[28:31], a[76:79], a[172:175], v[28:31]\n"
    "v_fma_f32 v12, v12, s36, v213\n"
    "v_fma_f32 v13, v13, s36, v213\n"
    "v_mfma_f32_16x16x32_bf16 v[28:31], a[80:83], a[176:179], v[28:31]\n"
    "v_exp_f32 v12, v12\n"
    "v_mfma_f32_16x16x32_bf16 v[28:31], a[84:87], a[180:183], v[28:31]\n"
    "v_exp_f32 v13, v13\n"
    "v_mfma_f32_16x16x32_bf16 v[28:31], a[88:91], a[184:187], v[28:31]\n"
    "v_fma_f32 v14, v14, s36, v213\n"
    "v_fma_f32 v15, v15, s36, v213\n"
    "v_mfma_f32_16x16x32_bf16 v[28:31], a[92:95], a[188:191], v[28:31]\n"
    "s_waitcnt vmcnt(14) lgkmcnt(0)\n"
    "s_barrier\n"
    "L_ph17:\n"
    "v_mfma_f32_16x16x32_bf16 v[64:67], a[0:3], v[32:35], v[64:67]\n"
    "buffer_load_dwordx4 v222, s[20:23], s42 offen nt lds\n"
    "v_mfma_f32_16x16x32_bf16 v[80:83], a[4:7], v[32:35], v[80:83]\n"
    "ds_read_b128 a[48:51], v216 offset:24576\n"
    "v_cvt_pk_bf16_f32 v8, v8, v9\n"
    "v_mfma_f32_16x16x32_bf16 v[96:99], a[8:11], v[32:35], v[96:99]\n"
    "buffer_load_dwordx4 v223, s[20:23], s42 offen offset:1024 nt lds\n"
    "v_mfma_f32_16x16x32_bf16 v[112:115], a[12:15], v[32:35], v[112:115]\n"
    "ds_read_b128 a[52:55], v217 offset:24576\n"
    "v_cvt_pk_bf16_f32 v9, v10, v11\n"
    "v_mfma_f32_16x16x32_bf16 v[128:131], a[16:19], v[32:35], v[128:131]\n"
    "buffer_load_dwordx4 v224, s[20:23], s42 offen offset:2048 nt lds\n"
    "v_mfma_f32_16x16x32_bf16 v[144:147], a[20:23], v[32:35], v[144:147]\n"
    "s_add_u32 m0, s44, 12288\n"
    "s_add_u32 s43, s41, 12288\n"
    "v_mfma_f32_16x16x32_bf16 v[160:163], a[24:27], v[32:35], v[160:163]\n"
    "buffer_load_dwordx4 v225, s[16:19], s43 offen nt lds\n"
    "v_mfma_f32_16x16x32_bf16 v[176:179], a[28:31], v[32:35], v[176:179]\n"
    "ds_read_b128 a[56:59], v216 offset:24704\n"
    "s_add_u32 m0, s37, 24576\n"
    "v_mfma_f32_16x16x32_bf16 a[192:195], a[32:35], v[32:35], a[192:195]\n"
    "ds_read_b128 a[60:63], v217 offset:24704\n"
    "s_add_u32 s42, s38, 122880\n"
    "v_mfma_f32_16x16x32_bf16 a[208:211], a[36:39], v[32:35], a[208:211]\n"
    "ds_read_b128 a[64:67], v216 offset:24832\n"
    "v_cvt_pk_bf16_f32 v10, v12, v13\n"
    "v_mfma_f32_16x16x32_bf16 a[224:227], a[40:43], v[32:35], a[224:227]\n"
    "ds_read_b128 a[68:71], v217 offset:24832\n"
    "v_fma_f32 v16, v16, s36, v214\n"
    "v_mfma_f32_16x16x32_bf16 a[240:243], a[44:47], v[32:35], a[240:243]\n"
    "ds_read_b128 a[72:75], v216 offset:30720\n"
    "v_fma_f32 v17, v17, s36, v214\n"
    "v_mfma_f32_16x16x32_bf16 v[192:195], v[208:211], v[32:35], v[192:195]\n"
    "ds_read_b128 a[76:79], v217 offset:30720\n"
    "v_fma_f32 v18, v18, s36, v214\n"
    "v_mfma_f32_16x16x32_bf16 v[68:71], a[0:3], v[40:43], v[68:71]\n"
    "ds_read_b128 a[80:83], v216 offset:30848\n"
    "v_fma_f32 v19, v19, s36, v214\n"
    "v_mfma_f32_16x16x32_bf16 v[84:87], a[4:7], v[40:43], v[84:87]\n"
    "ds_read_b128 a[84:87], v217 offset:30848\n"
    "v_fma_f32 v20, v20, s36, v214\n"
    "v_mfma_f32_16x16x32_bf16 v[100:103], a[8:11], v[40:43], v[100:103]\n"
    "ds_read_b128 a[88:91], v216 offset:30976\n"
    "v_fma_f32 v21, v21, s36, v214\n"
    "v_mfma_f32_16x16x32_bf16 v[116:119], a[12:15], v[40:43], v[116:119]\n"
    "ds_read_b128 a[92:95], v217 offset:30976\n"
    "v_fma_f32 v22, v22, s36, v214\n"
    "v_mfma_f32_16x16x32_bf16 v[132:135], a[16:19], v[40:43], v[132:135]\n"
    "v_exp_f32 v14, v14\n"
    "v_mfma_f32_16x16x32_bf16 v[148:151], a[20:23], v[40:43], v[148:151]\n"
    "v_exp_f32 v15, v15\n"
    "v_mfma_f32_16x16x32_bf16 v[164:167], a[24:27], v[40:43], v[164:167]\n"
    "v_cvt_pk_bf16_f32 v11, v14, v15\n"
    "v_fma_f32 v23, v23, s36, v214\n"
    "v_mfma_f32_16x16x32_bf16 v[180:183], a[28:31], v[40:43], v[180:183]\n"
    "v_exp_f32 v16, v16\n"
    "v_mfma_f32_16x16x32_bf16 a[196:199], a[32:35], v[40:43], a[196:199]\n"
    "v_exp_f32 v17, v17\n"
    "v_mfma_f32_16x16x32_bf16 a[212:215], a[36:39], v[40:43], a[212:215]\n"
    "v_exp_f32 v18, v18\n"
    "v_mfma_f32_16x16x32_bf16 a[228:231], a[40:43], v[40:43], a[228:231]\n"
    "v_exp_f32 v19, v19\n"
    "v_mfma_f32_16x16x32_bf16 a[244:247], a[44:47], v[40:43], a[244:247]\n"
    "v_exp_f32 v20, v20\n"
    "v_mfma_f32_16x16x32_bf16 v[196:199], v[208:211], v[40:43], v[196:199]\n"
    "v_exp_f32 v21, v21\n"
    "v_mfma_f32_16x16x32_bf16 v[72:75], a[0:3], v[48:51], v[72:75]\n"
    "v_exp_f32 v22, v22\n"
    "v_mfma_f32_16x16x32_bf16 v[88:91], a[4:7], v[48:51], v[88:91]\n"
    "v_exp_f32 v23, v23\n"
    "v_mfma_f32_16x16x32_bf16 v[104:107], a[8:11], v[48:51], v[104:107]\n"
    "v_cvt_pk_bf16_f32 v16, v16, v17\n"
    "v_cvt_pk_bf16_f32 v17, v18, v19\n"
    "v_mfma_f32_16x16x32_bf16 v[120:123], a[12:15], v[48:51], v[120:123]\n"
    "v_cvt_pk_bf16_f32 v18, v20, v21\n"
    "v_cvt_pk_bf16_f32 v19, v22, v23\n"
    "v_mfma_f32_16x16x32_bf16 v[136:139], a[16:19], v[48:51], v[136:139]\n"
    "v_fma_f32 v24, v24, s36, v215\n"
    "v_fma_f32 v25, v25, s36, v215\n"
    "v_mfma_f32_16x16x32_bf16 v[152:155], a[20:23], v[48:51], v[152:155]\n"
    "v_exp_f32 v24, v24\n"
    "v_mfma_f32_16x16x32_bf16 v[168:171], a[24:27], v[48:51], v[168:171]\n"
    "v_exp_f32 v25, v25\n"
    "v_mfma_f32_16x16x32_bf16 v[184:187], a[28:31], v[48:51], v[184:187]\n"
    "v_fma_f32 v26, v26, s36, v215\n"
    "v_fma_f32 v27, v27, s36, v215\n"
    "v_mfma_f32_16x16x32_bf16 a[200:203], a[32:35], v[48:51], a[200:203]\n"
    "v_exp_f32 v26, v26\n"
    "v_mfma_f32_16x16x32_bf16 a[216:219], a[36:39], v[48:51], a[216:219]\n"
    "v_exp_f32 v27, v27\n"
    "v_mfma_f32_16x16x32_bf16 a[232:235], a[40:43], v[48:51], a[232:235]\n"
    "v_fma_f32 v28, v28, s36, v215\n"
    "v_fma_f32 v29, v29, s36, v215\n"
    "v_mfma_f32_16x16x32_bf16 a[248:251], a[44:47], v[48:51], a[248:251]\n"
    "v_exp_f32 v28, v28\n"
    "v_mfma_f32_16x16x32_bf16 v[200:203], v[208:211], v[48:51], v[200:203]\n"
    "v_exp_f32 v29, v29\n"
    "v_mfma_f32_16x16x32_bf16 v[76:79], a[0:3], v[56:59], v[76:79]\n"
    "v_fma_f32 v30, v30, s36, v215\n"
    "v_fma_f32 v31, v31, s36, v215\n"
    "v_mfma_f32_16x16x32_bf16 v[92:95], a[4:7], v[56:59], v[92:95]\n"
    "v_exp_f32 v30, v30\n"
    "v_mfma_f32_16x16x32_bf16 v[108:111], a[8:11], v[56:59], v[108:111]\n"
    "v_exp_f32 v31, v31\n"
    "v_mfma_f32_16x16x32_bf16 v[124:127], a[12:15], v[56:59], v[124:127]\n"
    "v_cvt_pk_bf16_f32 v24, v24, v25\n"
    "v_cvt_pk_bf16_f32 v25, v26, v27\n"
    "v_mfma_f32_16x16x32_bf16 v[140:143], a[16:19], v[56:59], v[140:143]\n"
    "v_cvt_pk_bf16_f32 v26, v28, v29\n"
    "v_cvt_pk_bf16_f32 v27, v30, v31\n"
    "v_mfma_f32_16x16x32_bf16 v[156:159], a[20:23], v[56:59], v[156:159]\n"
    "v_mfma_f32_16x16x32_bf16 v[172:175], a[24:27], v[56:59], v[172:175]\n"
    "v_mfma_f32_16x16x32_bf16 v[188:191], a[28:31], v[56:59], v[188:191]\n"
    "v_mfma_f32_16x16x32_bf16 a[204:207], a[32:35], v[56:59], a[204:207]\n"
    "v_mfma_f32_16x16x32_bf16 a[220:223], a[36:39], v[56:59], a[220:223]\n"
    "v_mfma_f32_16x16x32_bf16 a[236:239], a[40:43], v[56:59], a[236:239]\n"
    "v_mfma_f32_16x16x32_bf16 a[252:255], a[44:47], v[56:59], a[252:255]\n"
    "v_mfma_f32_16x16x32_bf16 v[204:207], v[208:211], v[56:59], v[204:207]\n"
    "s_waitcnt vmcnt(14) lgkmcnt(0)\n"
    "s_barrier\n"
    "L_ph18:\n"
    "v_mfma_f32_16x16x32_bf16 v[32:35], a[48:51], a[96:99], 0\n"
    "buffer_load_dwordx4 v231, s[24:27], s42 offen nt lds\n"
    "v_mfma_f32_16x16x32_bf16 v[32:35], a[52:55], a[100:103], v[32:35]\n"
    "ds_read_b64_tr_b16 a[0:1], v218 offset:36864\n"
    "v_mfma_f32_16x16x32_bf16 v[32:35], a[56:59], a[104:107], v[32:35]\n"
    "buffer_load_dwordx4 v232, s[24:27], s42 offen offset:1024 nt lds\n"
    "v_mfma_f32_16x16x32_bf16 v[32:35], a[60:63], a[108:111], v[32:35]\n"
    "ds_read_b64_tr_b16 a[2:3], v218 offset:43008\n"
    "v_mfma_f32_16x16x32_bf16 v[32:35], a[64:67], a[112:115], v[32:35]\n"
    "buffer_load_dwordx4 v233, s[24:27], s42 offen offset:2048 nt lds\n"
    "v_mfma_f32_16x16x32_bf16 v[32:35], a[68:71], a[116:119], v[32:35]\n"
    "s_add_u32 m0, s44, 12288\n"
    "s_add_u32 s43, s41, 12288\n"
    "v_mfma_f32_16x16x32_bf16 v[36:39], a[72:75], a[96:99], 0\n"
    "buffer_load_dwordx4 v226, s[16:19], s43 offen offset:1024 nt lds\n"
    "v_mfma_f32_16x16x32_bf16 v[36:39], a[76:79], a[100:103], v[36:39]\n"
    "ds_read_b64_tr_b16 a[4:5], v219 offset:36864\n"
    "s_add_u32 m0, s37, 36864\n"
    "v_mfma_f32_16x16x32_bf16 v[36:39], a[80:83], a[104:107], v[36:39]\n"
    "ds_read_b64_tr_b16 a[6:7], v219 offset:43008\n"
    "s_add_u32 s42, s38, 147456\n"
    "v_mfma_f32_16x16x32_bf16 v[36:39], a[84:87], a[108:111], v[36:39]\n"
    "ds_read_b64_tr_b16 a[8:9], v220 offset:36864\n"
    "v_mfma_f32_16x16x32_bf16 v[36:39], a[88:91], a[112:115], v[36:39]\n"
    "ds_read_b64_tr_b16 a[10:11], v220 offset:43008\n"
    "v_mfma_f32_16x16x32_bf16 v[36:39], a[92:95], a[116:119], v[36:39]\n"
    "ds_read_b64_tr_b16 a[12:13], v221 offset:36864\n"
    "v_mfma_f32_16x16x32_bf16 v[40:43], a[48:51], a[120:123], 0\n"
    "ds_read_b64_tr_b16 a[14:15], v221 offset:43008\n"
    "v_mfma_f32_16x16x32_bf16 v[40:43], a[52:55], a[124:127], v[40:43]\n"
    "ds_read_b64_tr_b16 a[16:17], v218 offset:36992\n"
    "v_fma_f32 v32, v32, s36, v212\n"
    "v_mfma_f32_16x16x32_bf16 v[40:43], a[56:59], a[128:131], v[40:43]\n"
    "ds_read_b64_tr_b16 a[18:19], v218 offset:43136\n"
    "v_fma_f32 v33, v33, s36, v212\n"
    "v_mfma_f32_16x16x32_bf16 v[40:43], a[60:63], a[132:135], v[40:43]\n"
    "ds_read_b64_tr_b16 a[20:21], v219 offset:36992\n"
    "v_fma_f32 v34, v34, s36, v212\n"
    "v_mfma_f32_16x16x32_bf16 v[40:43], a[64:67], a[136:139], v[40:43]\n"
    "ds_read_b64_tr_b16 a[22:23], v219 offset:43136\n"
    "v_fma_f32 v35, v35, s36, v212\n"
    "v_mfma_f32_16x16x32_bf16 v[40:43], a[68:71], a[140:143], v[40:43]\n"
    "ds_read_b64_tr_b16 a[24:25], v220 offset:36992\n"
    "v_mfma_f32_16x16x32_bf16 v[44:47], a[72:75], a[120:123], 0\n"
    "ds_read_b64_tr_b16 a[26:27], v220 offset:43136\n"
    "v_mfma_f32_16x16x32_bf16 v[44:47], a[76:79], a[124:127], v[44:47]\n"
    "ds_read_b64_tr_b16 a[28:29], v221 offset:36992\n"
    "v_fma_f32 v36, v36, s36, v212\n"
    "v_mfma_f32_16x16x32_bf16 v[44:47], a[80:83], a[128:131], v[44:47]\n"
    "ds_read_b64_tr_b16 a[30:31], v221 offset:43136\n"
    "v_fma_f32 v37, v37, s36, v212\n"
    "v_mfma_f32_16x16x32_bf16 v[44:47], a[84:87], a[132:135], v[44:47]\n"
    "ds_read_b64_tr_b16 a[32:33], v218 offset:37120\n"
    "v_fma_f32 v38, v38, s36, v212\n"
    "v_mfma_f32_16x16x32_bf16 v[44:47], a[88:91], a[136:139], v[44:47]\n"
    "ds_read_b64_tr_b16 a[34:35], v218 offset:43264\n"
    "v_fma_f32 v39, v39, s36, v212\n"
    "v_mfma_f32_16x16x32_bf16 v[44:47], a[92:95], a[140:143], v[44:47]\n"
    "ds_read_b64_tr_b16 a[36:37], v219 offset:37120\n"
    "v_mfma_f32_16x16x32_bf16 v[48:51], a[48:51], a[144:147], 0\n"
    "ds_read_b64_tr_b16 a[38:39], v219 offset:43264\n"
    "v_mfma_f32_16x16x32_bf16 v[48:51], a[52:55], a[148:151], v[48:51]\n"
    "ds_read_b64_tr_b16 a[40:41], v220 offset:37120\n"
    "v_fma_f32 v40, v40, s36, v213\n"
    "v_mfma_f32_16x16x32_bf16 v[48:51], a[56:59], a[152:155], v[48:51]\n"
    "ds_read_b64_tr_b16 a[42:43], v220 offset:43264\n"
    "v_fma_f32 v41, v41, s36, v213\n"
    "v_mfma_f32_16x16x32_bf16 v[48:51], a[60:63], a[156:159], v[48:51]\n"
    "ds_read_b64_tr_b16 a[44:45], v221 offset:37120\n"
    "v_fma_f32 v42, v42, s36, v213\n"
    "v_mfma_f32_16x16x32_bf16 v[48:51], a[64:67], a[160:163], v[48:51]\n"
    "ds_read_b64_tr_b16 a[46:47], v221 offset:43264\n"
    "v_fma_f32 v43, v43, s36, v213\n"
    "v_mfma_f32_16x16x32_bf16 v[48:51], a[68:71], a[164:167], v[48:51]\n"
    "v_exp_f32 v32, v32\n"
    "v_mfma_f32_16x16x32_bf16 v[52:55], a[72:75], a[144:147], 0\n"
    "v_exp_f32 v33, v33\n"
    "v_mfma_f32_16x16x32_bf16 v[52:55], a[76:79], a[148:151], v[52:55]\n"
    "v_exp_f32 v34, v34\n"
    "v_mfma_f32_16x16x32_bf16 v[52:55], a[80:83], a[152:155], v[52:55]\n"
    "v_exp_f32 v35, v35\n"
    "v_mfma_f32_16x16x32_bf16 v[52:55], a[84:87], a[156:159], v[52:55]\n"
    "v_exp_f32 v36, v36\n"
    "v_mfma_f32_16x16x32_bf16 v[52:55], a[88:91], a[160:163], v[52:55]\n"
    "v_exp_f32 v37, v37\n"
    "v_mfma_f32_16x16x32_bf16 v[52:55], a[92:95], a[164:167], v[52:55]\n"
    "v_exp_f32 v38, v38\n"
    "v_mfma_f32_16x16x32_bf16 v[56:59], a[48:51], a[168:171], 0\n"
    "v_exp_f32 v39, v39\n"
    "v_mfma_f32_16x16x32_bf16 v[56:59], a[52:55], a[172:175], v[56:59]\n"
    "v_cvt_pk_bf16_f32 v32, v32, v33\n"
    "v_cvt_pk_bf16_f32 v33, v34, v35\n"
    "v_mfma_f32_16x16x32_bf16 v[56:59], a[56:59], a[176:179], v[56:59]\n"
    "v_cvt_pk_bf16_f32 v34, v36, v37\n"
    "v_cvt_pk_bf16_f32 v35, v38, v39\n"
    "v_mfma_f32_16x16x32_bf16 v[56:59], a[60:63], a[180:183], v[56:59]\n"
    "v_exp_f32 v40, v40\n"
    "v_mfma_f32_16x16x32_bf16 v[56:59], a[64:67], a[184:187], v[56:59]\n"
    "v_exp_f32 v41, v41\n"
    "v_mfma_f32_16x16x32_bf16 v[56:59], a[68:71], a[188:191], v[56:59]\n"
    "v_exp_f32 v42, v42\n"
    "v_mfma_f32_16x16x32_bf16 v[60:63], a[72:75], a[168:171], 0\n"
    "v_exp_f32 v43, v43\n"
    "v_mfma_f32_16x16x32_bf16 v[60:63], a[76:79], a[172:175], v[60:63]\n"
    "v_fma_f32 v44, v44, s36, v213\n"
    "v_fma_f32 v45, v45, s36, v213\n"
    "v_mfma_f32_16x16x32_bf16 v[60:63], a[80:83], a[176:179], v[60:63]\n"
    "v_exp_f32 v44, v44\n"
    "v_mfma_f32_16x16x32_bf16 v[60:63], a[84:87], a[180:183], v[60:63]\n"
    "v_exp_f32 v45, v45\n"
    "v_mfma_f32_16x16x32_bf16 v[60:63], a[88:91], a[184:187], v[60:63]\n"
    "v_fma_f32 v46, v46, s36, v213\n"
    "v_fma_f32 v47, v47, s36, v213\n"
    "v_mfma_f32_16x16x32_bf16 v[60:63], a[92:95], a[188:191], v[60:63]\n"
    "s_waitcnt vmcnt(14) lgkmcnt(0)\n"
    "s_barrier\n"
    "L_ph19:\n"
    "v_mfma_f32_16x16x32_bf16 v[64:67], a[0:3], v[0:3], v[64:67]\n"
    "buffer_load_dwordx4 v222, s[20:23], s42 offen nt lds\n"
    "v_mfma_f32_16x16x32_bf16 v[80:83], a[4:7], v[0:3], v[80:83]\n"
    "ds_read_b128 a[48:51], v216 offset:49152\n"
    "v_cvt_pk_bf16_f32 v40, v40, v41\n"
    "v_mfma_f32_16x16x32_bf16 v[96:99], a[8:11], v[0:3], v[96:99]\n"
    "buffer_load_dwordx4 v223, s[20:23], s42 offen offset:1024 nt lds\n"
    "v_mfma_f32_16x16x32_bf16 v[112:115], a[12:15], v[0:3], v[112:115]\n"
    "ds_read_b128 a[52:55], v217 offset:49152\n"
    "v_cvt_pk_bf16_f32 v41, v42, v43\n"
    "v_mfma_f32_16x16x32_bf16 v[128:131], a[16:19], v[0:3], v[128:131]\n"
    "buffer_load_dwordx4 v224, s[20:23], s42 offen offset:2048 nt lds\n"
    "v_mfma_f32_16x16x32_bf16 v[144:147], a[20:23], v[0:3], v[144:147]\n"
    "s_add_u32 m0, s44, 12288\n"
    "s_add_u32 s43, s41, 12288\n"
    "v_mfma_f32_16x16x32_bf16 v[160:163], a[24:27], v[0:3], v[160:163]\n"
    "buffer_load_dwordx4 v227, s[16:19], s43 offen offset:2048 nt lds\n"
    "v_mfma_f32_16x16x32_bf16 v[176:179], a[28:31], v[0:3], v[176:179]\n"
    "s_mov_b32 m0, 159744\n"
    "s_mov_b32 s51, L_ph23-L_code_start\n"
    "v_mfma_f32_16x16x32_bf16 a[192:195], a[32:35], v[0:3], a[192:195]\n"
    "buffer_load_dword v249, s[52:55], s51 offen lds\n"
    "v_mfma_f32_16x16x32_bf16 a[208:211], a[36:39], v[0:3], a[208:211]\n"
    "ds_read_b128 a[56:59], v216 offset:49280\n"
    "s_add_u32 m0, s37, 49152\n"
    "v_mfma_f32_16x16x32_bf16 a[224:227], a[40:43], v[0:3], a[224:227]\n"
    "ds_read_b128 a[60:63], v217 offset:49280\n"
    "s_add_u32 s42, s38, 135168\n"
    "v_mfma_f32_16x16x32_bf16 a[240:243], a[44:47], v[0:3], a[240:243]\n"
    "ds_read_b128 a[64:67], v216 offset:49408\n"
    "v_cvt_pk_bf16_f32 v42, v44, v45\n"
    "v_mfma_f32_16x16x32_bf16 v[192:195], v[208:211], v[0:3], v[192:195]\n"
    "ds_read_b128 a[68:71], v217 offset:49408\n"
    "v_fma_f32 v48, v48, s36, v214\n"
    "v_mfma_f32_16x16x32_bf16 v[68:71], a[0:3], v[8:11], v[68:71]\n"
    "ds_read_b128 a[72:75], v216 offset:55296\n"
    "v_fma_f32 v49, v49, s36, v214\n"
    "v_mfma_f32_16x16x32_bf16 v[84:87], a[4:7], v[8:11], v[84:87]\n"
    "ds_read_b128 a[76:79], v217 offset:55296\n"
    "v_fma_f32 v50, v50, s36, v214\n"
    "v_mfma_f32_16x16x32_bf16 v[100:103], a[8:11], v[8:11], v[100:103]\n"
    "ds_read_b128 a[80:83], v216 offset:55424\n"
    "v_fma_f32 v51, v51, s36, v214\n"
    "v_mfma_f32_16x16x32_bf16 v[116:119], a[12:15], v[8:11], v[116:119]\n"
    "ds_read_b128 a[84:87], v217 offset:55424\n"
    "v_fma_f32 v52, v52, s36, v214\n"
    "v_mfma_f32_16x16x32_bf16 v[132:135], a[16:19], v[8:11], v[132:135]\n"
    "ds_read_b128 a[88:91], v216 offset:55552\n"
    "v_fma_f32 v53, v53, s36, v214\n"
    "v_mfma_f32_16x16x32_bf16 v[148:151], a[20:23], v[8:11], v[148:151]\n"
    "ds_read_b128 a[92:95], v217 offset:55552\n"
    "v_fma_f32 v54, v54, s36, v214\n"
    "v_mfma_f32_16x16x32_bf16 v[164:167], a[24:27], v[8:11], v[164:167]\n"
    "v_exp_f32 v46, v46\n"
    "v_mfma_f32_16x16x32_bf16 v[180:183], a[28:31], v[8:11], v[180:183]\n"
    "v_exp_f32 v47, v47\n"
    "v_mfma_f32_16x16x32_bf16 a[196:199], a[32:35], v[8:11], a[196:199]\n"
    "v_cvt_pk_bf16_f32 v43, v46, v47\n"
    "v_fma_f32 v55, v55, s36, v214\n"
    "v_mfma_f32_16x16x32_bf16 a[212:215], a[36:39], v[8:11], a[212:215]\n"
    "v_exp_f32 v48, v48\n"
    "v_mfma_f32_16x16x32_bf16 a[228:231], a[40:43], v[8:11], a[228:231]\n"
    "v_exp_f32 v49, v49\n"
    "v_mfma_f32_16x16x32_bf16 a[244:247], a[44:47], v[8:11], a[244:247]\n"
    "v_exp_f32 v50, v50\n"
    "v_mfma_f32_16x16x32_bf16 v[196:199], v[208:211], v[8:11], v[196:199]\n"
    "v_exp_f32 v51, v51\n"
    "v_mfma_f32_16x16x32_bf16 v[72:75], a[0:3], v[16:19], v[72:75]\n"
    "v_exp_f32 v52, v52\n"
    "v_mfma_f32_16x16x32_bf16 v[88:91], a[4:7], v[16:19], v[88:91]\n"
    "v_exp_f32 v53, v53\n"
    "v_mfma_f32_16x16x32_bf16 v[104:107], a[8:11], v[16:19], v[104:107]\n"
    "v_exp_f32 v54, v54\n"
    "v_mfma_f32_16x16x32_bf16 v[120:123], a[12:15], v[16:19], v[120:123]\n"
    "v_exp_f32 v55, v55\n"
    "v_mfma_f32_16x16x32_bf16 v[136:139], a[16:19], v[16:19], v[136:139]\n"
    "v_cvt_pk_bf16_f32 v48, v48, v49\n"
    "v_cvt_pk_bf16_f32 v49, v50, v51\n"
    "v_mfma_f32_16x16x32_bf16 v[152:155], a[20:23], v[16:19], v[152:155]\n"
    "v_cvt_pk_bf16_f32 v50, v52, v53\n"
    "v_cvt_pk_bf16_f32 v51, v54, v55\n"
    "v_mfma_f32_16x16x32_bf16 v[168:171], a[24:27], v[16:19], v[168:171]\n"
    "v_fma_f32 v56, v56, s36, v215\n"
    "v_fma_f32 v57, v57, s36, v215\n"
    "v_mfma_f32_16x16x32_bf16 v[184:187], a[28:31], v[16:19], v[184:187]\n"
    "v_exp_f32 v56, v56\n"
    "v_mfma_f32_16x16x32_bf16 a[200:203], a[32:35], v[16:19], a[200:203]\n"
    "v_exp_f32 v57, v57\n"
    "v_mfma_f32_16x16x32_bf16 a[216:219], a[36:39], v[16:19], a[216:219]\n"
    "v_fma_f32 v58, v58, s36, v215\n"
    "v_fma_f32 v59, v59, s36, v215\n"
    "v_mfma_f32_16x16x32_bf16 a[232:235], a[40:43], v[16:19], a[232:235]\n"
    "v_exp_f32 v58, v58\n"
    "v_mfma_f32_16x16x32_bf16 a[248:251], a[44:47], v[16:19], a[248:251]\n"
    "v_exp_f32 v59, v59\n"
    "v_mfma_f32_16x16x32_bf16 v[200:203], v[208:211], v[16:19], v[200:203]\n"
    "v_fma_f32 v60, v60, s36, v215\n"
    "v_fma_f32 v61, v61, s36, v215\n"
    "v_mfma_f32_16x16x32_bf16 v[76:79], a[0:3], v[24:27], v[76:79]\n"
    "v_exp_f32 v60, v60\n"
    "v_mfma_f32_16x16x32_bf16 v[92:95], a[4:7], v[24:27], v[92:95]\n"
    "v_exp_f32 v61, v61\n"
    "v_mfma_f32_16x16x32_bf16 v[108:111], a[8:11], v[24:27], v[108:111]\n"
    "v_fma_f32 v62, v62, s36, v215\n"
    "v_fma_f32 v63, v63, s36, v215\n"
    "v_mfma_f32_16x16x32_bf16 v[124:127], a[12:15], v[24:27], v[124:127]\n"
    "v_exp_f32 v62, v62\n"
    "v_mfma_f32_16x16x32_bf16 v[140:143], a[16:19], v[24:27], v[140:143]\n"
    "v_exp_f32 v63, v63\n"
    "v_mfma_f32_16x16x32_bf16 v[156:159], a[20:23], v[24:27], v[156:159]\n"
    "v_cvt_pk_bf16_f32 v56, v56, v57\n"
    "v_cvt_pk_bf16_f32 v57, v58, v59\n"
    "v_mfma_f32_16x16x32_bf16 v[172:175], a[24:27], v[24:27], v[172:175]\n"
    "v_cvt_pk_bf16_f32 v58, v60, v61\n"
    "v_cvt_pk_bf16_f32 v59, v62, v63\n"
    "v_mfma_f32_16x16x32_bf16 v[188:191], a[28:31], v[24:27], v[188:191]\n"
    "v_mfma_f32_16x16x32_bf16 a[204:207], a[32:35], v[24:27], a[204:207]\n"
    "v_mfma_f32_16x16x32_bf16 a[220:223], a[36:39], v[24:27], a[220:223]\n"
    "v_mfma_f32_16x16x32_bf16 a[236:239], a[40:43], v[24:27], a[236:239]\n"
    "v_mfma_f32_16x16x32_bf16 a[252:255], a[44:47], v[24:27], a[252:255]\n"
    "v_mfma_f32_16x16x32_bf16 v[204:207], v[208:211], v[24:27], v[204:207]\n"
    "s_waitcnt vmcnt(14) lgkmcnt(0)\n"
    "s_barrier\n"
    "L_ph20:\n"
    "v_mfma_f32_16x16x32_bf16 v[0:3], a[48:51], a[96:99], 0\n"
    "buffer_load_dwordx4 v231, s[24:27], s42 offen nt lds\n"
    "v_mfma_f32_16x16x32_bf16 v[0:3], a[52:55], a[100:103], v[0:3]\n"
    "ds_read_b64_tr_b16 a[0:1], v218 offset:0\n"
    "v_mfma_f32_16x16x32_bf16 v[0:3], a[56:59], a[104:107], v[0:3]\n"
    "buffer_load_dwordx4 v232, s[24:27], s42 offen offset:1024 nt lds\n"
    "v_mfma_f32_16x16x32_bf16 v[0:3], a[60:63], a[108:111], v[0:3]\n"
    "ds_read_b64_tr_b16 a[2:3], v218 offset:6144\n"
    "v_mfma_f32_16x16x32_bf16 v[0:3], a[64:67], a[112:115], v[0:3]\n"
    "buffer_load_dwordx4 v233, s[24:27], s42 offen offset:2048 nt lds\n"
    "v_mfma_f32_16x16x32_bf16 v[0:3], a[68:71], a[116:119], v[0:3]\n"
    "s_add_u32 m0, s44, 15360\n"
    "s_add_u32 s43, s41, 15360\n"
    "v_mfma_f32_16x16x32_bf16 v[4:7], a[72:75], a[96:99], 0\n"
    "buffer_load_dwordx4 v228, s[16:19], s43 offen nt lds\n"
    "v_mfma_f32_16x16x32_bf16 v[4:7], a[76:79], a[100:103], v[4:7]\n"
    "ds_read_b64_tr_b16 a[4:5], v219 offset:0\n"
    "s_add_u32 m0, s37, 0\n"
    "v_mfma_f32_16x16x32_bf16 v[4:7], a[80:83], a[104:107], v[4:7]\n"
    "ds_read_b64_tr_b16 a[6:7], v219 offset:6144\n"
    "s_add_u32 s42, s38, 159744\n"
    "v_mfma_f32_16x16x32_bf16 v[4:7], a[84:87], a[108:111], v[4:7]\n"
    "ds_read_b64_tr_b16 a[8:9], v220 offset:0\n"
    "v_mfma_f32_16x16x32_bf16 v[4:7], a[88:91], a[112:115], v[4:7]\n"
    "ds_read_b64_tr_b16 a[10:11], v220 offset:6144\n"
    "v_mfma_f32_16x16x32_bf16 v[4:7], a[92:95], a[116:119], v[4:7]\n"
    "ds_read_b64_tr_b16 a[12:13], v221 offset:0\n"
    "v_mfma_f32_16x16x32_bf16 v[8:11], a[48:51], a[120:123], 0\n"
    "ds_read_b64_tr_b16 a[14:15], v221 offset:6144\n"
    "v_mfma_f32_16x16x32_bf16 v[8:11], a[52:55], a[124:127], v[8:11]\n"
    "ds_read_b64_tr_b16 a[16:17], v218 offset:128\n"
    "v_fma_f32 v0, v0, s36, v212\n"
    "v_mfma_f32_16x16x32_bf16 v[8:11], a[56:59], a[128:131], v[8:11]\n"
    "ds_read_b64_tr_b16 a[18:19], v218 offset:6272\n"
    "v_fma_f32 v1, v1, s36, v212\n"
    "v_mfma_f32_16x16x32_bf16 v[8:11], a[60:63], a[132:135], v[8:11]\n"
    "ds_read_b64_tr_b16 a[20:21], v219 offset:128\n"
    "v_fma_f32 v2, v2, s36, v212\n"
    "v_mfma_f32_16x16x32_bf16 v[8:11], a[64:67], a[136:139], v[8:11]\n"
    "ds_read_b64_tr_b16 a[22:23], v219 offset:6272\n"
    "v_fma_f32 v3, v3, s36, v212\n"
    "v_mfma_f32_16x16x32_bf16 v[8:11], a[68:71], a[140:143], v[8:11]\n"
    "ds_read_b64_tr_b16 a[24:25], v220 offset:128\n"
    "v_mfma_f32_16x16x32_bf16 v[12:15], a[72:75], a[120:123], 0\n"
    "ds_read_b64_tr_b16 a[26:27], v220 offset:6272\n"
    "v_mfma_f32_16x16x32_bf16 v[12:15], a[76:79], a[124:127], v[12:15]\n"
    "ds_read_b64_tr_b16 a[28:29], v221 offset:128\n"
    "v_fma_f32 v4, v4, s36, v212\n"
    "v_mfma_f32_16x16x32_bf16 v[12:15], a[80:83], a[128:131], v[12:15]\n"
    "ds_read_b64_tr_b16 a[30:31], v221 offset:6272\n"
    "v_fma_f32 v5, v5, s36, v212\n"
    "v_mfma_f32_16x16x32_bf16 v[12:15], a[84:87], a[132:135], v[12:15]\n"
    "ds_read_b64_tr_b16 a[32:33], v218 offset:256\n"
    "v_fma_f32 v6, v6, s36, v212\n"
    "v_mfma_f32_16x16x32_bf16 v[12:15], a[88:91], a[136:139], v[12:15]\n"
    "ds_read_b64_tr_b16 a[34:35], v218 offset:6400\n"
    "v_fma_f32 v7, v7, s36, v212\n"
    "v_mfma_f32_16x16x32_bf16 v[12:15], a[92:95], a[140:143], v[12:15]\n"
    "ds_read_b64_tr_b16 a[36:37], v219 offset:256\n"
    "v_mfma_f32_16x16x32_bf16 v[16:19], a[48:51], a[144:147], 0\n"
    "ds_read_b64_tr_b16 a[38:39], v219 offset:6400\n"
    "v_mfma_f32_16x16x32_bf16 v[16:19], a[52:55], a[148:151], v[16:19]\n"
    "ds_read_b64_tr_b16 a[40:41], v220 offset:256\n"
    "v_fma_f32 v8, v8, s36, v213\n"
    "v_mfma_f32_16x16x32_bf16 v[16:19], a[56:59], a[152:155], v[16:19]\n"
    "ds_read_b64_tr_b16 a[42:43], v220 offset:6400\n"
    "v_fma_f32 v9, v9, s36, v213\n"
    "v_mfma_f32_16x16x32_bf16 v[16:19], a[60:63], a[156:159], v[16:19]\n"
    "ds_read_b64_tr_b16 a[44:45], v221 offset:256\n"
    "v_fma_f32 v10, v10, s36, v213\n"
    "v_mfma_f32_16x16x32_bf16 v[16:19], a[64:67], a[160:163], v[16:19]\n"
    "ds_read_b64_tr_b16 a[46:47], v221 offset:6400\n"
    "v_fma_f32 v11, v11, s36, v213\n"
    "v_mfma_f32_16x16x32_bf16 v[16:19], a[68:71], a[164:167], v[16:19]\n"
    "v_exp_f32 v0, v0\n"
    "v_mfma_f32_16x16x32_bf16 v[20:23], a[72:75], a[144:147], 0\n"
    "v_exp_f32 v1, v1\n"
    "v_mfma_f32_16x16x32_bf16 v[20:23], a[76:79], a[148:151], v[20:23]\n"
    "v_exp_f32 v2, v2\n"
    "v_mfma_f32_16x16x32_bf16 v[20:23], a[80:83], a[152:155], v[20:23]\n"
    "v_exp_f32 v3, v3\n"
    "v_mfma_f32_16x16x32_bf16 v[20:23], a[84:87], a[156:159], v[20:23]\n"
    "v_exp_f32 v4, v4\n"
    "v_mfma_f32_16x16x32_bf16 v[20:23], a[88:91], a[160:163], v[20:23]\n"
    "v_exp_f32 v5, v5\n"
    "v_mfma_f32_16x16x32_bf16 v[20:23], a[92:95], a[164:167], v[20:23]\n"
    "v_exp_f32 v6, v6\n"
    "v_mfma_f32_16x16x32_bf16 v[24:27], a[48:51], a[168:171], 0\n"
    "v_exp_f32 v7, v7\n"
    "v_mfma_f32_16x16x32_bf16 v[24:27], a[52:55], a[172:175], v[24:27]\n"
    "v_cvt_pk_bf16_f32 v0, v0, v1\n"
    "v_cvt_pk_bf16_f32 v1, v2, v3\n"
    "v_mfma_f32_16x16x32_bf16 v[24:27], a[56:59], a[176:179], v[24:27]\n"
    "v_cvt_pk_bf16_f32 v2, v4, v5\n"
    "v_cvt_pk_bf16_f32 v3, v6, v7\n"
    "v_mfma_f32_16x16x32_bf16 v[24:27], a[60:63], a[180:183], v[24:27]\n"
    "v_exp_f32 v8, v8\n"
    "v_mfma_f32_16x16x32_bf16 v[24:27], a[64:67], a[184:187], v[24:27]\n"
    "v_exp_f32 v9, v9\n"
    "v_mfma_f32_16x16x32_bf16 v[24:27], a[68:71], a[188:191], v[24:27]\n"
    "v_exp_f32 v10, v10\n"
    "v_mfma_f32_16x16x32_bf16 v[28:31], a[72:75], a[168:171], 0\n"
    "v_exp_f32 v11, v11\n"
    "v_mfma_f32_16x16x32_bf16 v[28:31], a[76:79], a[172:175], v[28:31]\n"
    "v_fma_f32 v12, v12, s36, v213\n"
    "v_fma_f32 v13, v13, s36, v213\n"
    "v_mfma_f32_16x16x32_bf16 v[28:31], a[80:83], a[176:179], v[28:31]\n"
    "v_exp_f32 v12, v12\n"
    "v_mfma_f32_16x16x32_bf16 v[28:31], a[84:87], a[180:183], v[28:31]\n"
    "v_exp_f32 v13, v13\n"
    "v_mfma_f32_16x16x32_bf16 v[28:31], a[88:91], a[184:187], v[28:31]\n"
    "v_fma_f32 v14, v14, s36, v213\n"
    "v_fma_f32 v15, v15, s36, v213\n"
    "v_mfma_f32_16x16x32_bf16 v[28:31], a[92:95], a[188:191], v[28:31]\n"
    "s_waitcnt vmcnt(14) lgkmcnt(0)\n"
    "s_barrier\n"
    "L_ph21:\n"
    "v_mfma_f32_16x16x32_bf16 v[64:67], a[0:3], v[32:35], v[64:67]\n"
    "buffer_load_dwordx4 v222, s[20:23], s42 offen nt lds\n"
    "v_mfma_f32_16x16x32_bf16 v[80:83], a[4:7], v[32:35], v[80:83]\n"
    "ds_read_b128 a[48:51], v216 offset:12288\n"
    "v_cvt_pk_bf16_f32 v8, v8, v9\n"
    "v_mfma_f32_16x16x32_bf16 v[96:99], a[8:11], v[32:35], v[96:99]\n"
    "buffer_load_dwordx4 v223, s[20:23], s42 offen offset:1024 nt lds\n"
    "v_mfma_f32_16x16x32_bf16 v[112:115], a[12:15], v[32:35], v[112:115]\n"
    "ds_read_b128 a[52:55], v217 offset:12288\n"
    "v_cvt_pk_bf16_f32 v9, v10, v11\n"
    "v_mfma_f32_16x16x32_bf16 v[128:131], a[16:19], v[32:35], v[128:131]\n"
    "buffer_load_dwordx4 v224, s[20:23], s42 offen offset:2048 nt lds\n"
    "v_mfma_f32_16x16x32_bf16 v[144:147], a[20:23], v[32:35], v[144:147]\n"
    "s_add_u32 m0, s44, 15360\n"
    "s_add_u32 s43, s41, 15360\n"
    "v_mfma_f32_16x16x32_bf16 v[160:163], a[24:27], v[32:35], v[160:163]\n"
    "buffer_load_dwordx4 v229, s[16:19], s43 offen offset:1024 nt lds\n"
    "v_mfma_f32_16x16x32_bf16 v[176:179], a[28:31], v[32:35], v[176:179]\n"
    "ds_read_b128 a[56:59], v216 offset:12416\n"
    "s_add_u32 m0, s37, 12288\n"
    "v_mfma_f32_16x16x32_bf16 a[192:195], a[32:35], v[32:35], a[192:195]\n"
    "ds_read_b128 a[60:63], v217 offset:12416\n"
    "s_add_u32 s42, s38, 147456\n"
    "v_mfma_f32_16x16x32_bf16 a[208:211], a[36:39], v[32:35], a[208:211]\n"
    "ds_read_b128 a[64:67], v216 offset:12544\n"
    "v_cvt_pk_bf16_f32 v10, v12, v13\n"
    "v_mfma_f32_16x16x32_bf16 a[224:227], a[40:43], v[32:35], a[224:227]\n"
    "ds_read_b128 a[68:71], v217 offset:12544\n"
    "v_fma_f32 v16, v16, s36, v214\n"
    "v_mfma_f32_16x16x32_bf16 a[240:243], a[44:47], v[32:35], a[240:243]\n"
    "ds_read_b128 a[72:75], v216 offset:18432\n"
    "v_fma_f32 v17, v17, s36, v214\n"
    "v_mfma_f32_16x16x32_bf16 v[192:195], v[208:211], v[32:35], v[192:195]\n"
    "ds_read_b128 a[76:79], v217 offset:18432\n"
    "v_fma_f32 v18, v18, s36, v214\n"
    "v_mfma_f32_16x16x32_bf16 v[68:71], a[0:3], v[40:43], v[68:71]\n"
    "ds_read_b128 a[80:83], v216 offset:18560\n"
    "v_fma_f32 v19, v19, s36, v214\n"
    "v_mfma_f32_16x16x32_bf16 v[84:87], a[4:7], v[40:43], v[84:87]\n"
    "ds_read_b128 a[84:87], v217 offset:18560\n"
    "v_fma_f32 v20, v20, s36, v214\n"
    "v_mfma_f32_16x16x32_bf16 v[100:103], a[8:11], v[40:43], v[100:103]\n"
    "ds_read_b128 a[88:91], v216 offset:18688\n"
    "v_fma_f32 v21, v21, s36, v214\n"
    "v_mfma_f32_16x16x32_bf16 v[116:119], a[12:15], v[40:43], v[116:119]\n"
    "ds_read_b128 a[92:95], v217 offset:18688\n"
    "v_fma_f32 v22, v22, s36, v214\n"
    "v_mfma_f32_16x16x32_bf16 v[132:135], a[16:19], v[40:43], v[132:135]\n"
    "v_exp_f32 v14, v14\n"
    "v_mfma_f32_16x16x32_bf16 v[148:151], a[20:23], v[40:43], v[148:151]\n"
    "v_exp_f32 v15, v15\n"
    "v_mfma_f32_16x16x32_bf16 v[164:167], a[24:27], v[40:43], v[164:167]\n"
    "v_cvt_pk_bf16_f32 v11, v14, v15\n"
    "v_fma_f32 v23, v23, s36, v214\n"
    "v_mfma_f32_16x16x32_bf16 v[180:183], a[28:31], v[40:43], v[180:183]\n"
    "v_exp_f32 v16, v16\n"
    "v_mfma_f32_16x16x32_bf16 a[196:199], a[32:35], v[40:43], a[196:199]\n"
    "v_exp_f32 v17, v17\n"
    "v_mfma_f32_16x16x32_bf16 a[212:215], a[36:39], v[40:43], a[212:215]\n"
    "v_exp_f32 v18, v18\n"
    "v_mfma_f32_16x16x32_bf16 a[228:231], a[40:43], v[40:43], a[228:231]\n"
    "v_exp_f32 v19, v19\n"
    "v_mfma_f32_16x16x32_bf16 a[244:247], a[44:47], v[40:43], a[244:247]\n"
    "v_exp_f32 v20, v20\n"
    "v_mfma_f32_16x16x32_bf16 v[196:199], v[208:211], v[40:43], v[196:199]\n"
    "v_exp_f32 v21, v21\n"
    "v_mfma_f32_16x16x32_bf16 v[72:75], a[0:3], v[48:51], v[72:75]\n"
    "v_exp_f32 v22, v22\n"
    "v_mfma_f32_16x16x32_bf16 v[88:91], a[4:7], v[48:51], v[88:91]\n"
    "v_exp_f32 v23, v23\n"
    "v_mfma_f32_16x16x32_bf16 v[104:107], a[8:11], v[48:51], v[104:107]\n"
    "v_cvt_pk_bf16_f32 v16, v16, v17\n"
    "v_cvt_pk_bf16_f32 v17, v18, v19\n"
    "v_mfma_f32_16x16x32_bf16 v[120:123], a[12:15], v[48:51], v[120:123]\n"
    "v_cvt_pk_bf16_f32 v18, v20, v21\n"
    "v_cvt_pk_bf16_f32 v19, v22, v23\n"
    "v_mfma_f32_16x16x32_bf16 v[136:139], a[16:19], v[48:51], v[136:139]\n"
    "v_fma_f32 v24, v24, s36, v215\n"
    "v_fma_f32 v25, v25, s36, v215\n"
    "v_mfma_f32_16x16x32_bf16 v[152:155], a[20:23], v[48:51], v[152:155]\n"
    "v_exp_f32 v24, v24\n"
    "v_mfma_f32_16x16x32_bf16 v[168:171], a[24:27], v[48:51], v[168:171]\n"
    "v_exp_f32 v25, v25\n"
    "v_mfma_f32_16x16x32_bf16 v[184:187], a[28:31], v[48:51], v[184:187]\n"
    "v_fma_f32 v26, v26, s36, v215\n"
    "v_fma_f32 v27, v27, s36, v215\n"
    "v_mfma_f32_16x16x32_bf16 a[200:203], a[32:35], v[48:51], a[200:203]\n"
    "v_exp_f32 v26, v26\n"
    "v_mfma_f32_16x16x32_bf16 a[216:219], a[36:39], v[48:51], a[216:219]\n"
    "v_exp_f32 v27, v27\n"
    "v_mfma_f32_16x16x32_bf16 a[232:235], a[40:43], v[48:51], a[232:235]\n"
    "v_fma_f32 v28, v28, s36, v215\n"
    "v_fma_f32 v29, v29, s36, v215\n"
    "v_mfma_f32_16x16x32_bf16 a[248:251], a[44:47], v[48:51], a[248:251]\n"
    "v_exp_f32 v28, v28\n"
    "v_mfma_f32_16x16x32_bf16 v[200:203], v[208:211], v[48:51], v[200:203]\n"
    "v_exp_f32 v29, v29\n"
    "v_mfma_f32_16x16x32_bf16 v[76:79], a[0:3], v[56:59], v[76:79]\n"
    "v_fma_f32 v30, v30, s36, v215\n"
    "v_fma_f32 v31, v31, s36, v215\n"
    "v_mfma_f32_16x16x32_bf16 v[92:95], a[4:7], v[56:59], v[92:95]\n"
    "v_exp_f32 v30, v30\n"
    "v_mfma_f32_16x16x32_bf16 v[108:111], a[8:11], v[56:59], v[108:111]\n"
    "v_exp_f32 v31, v31\n"
    "v_mfma_f32_16x16x32_bf16 v[124:127], a[12:15], v[56:59], v[124:127]\n"
    "v_cvt_pk_bf16_f32 v24, v24, v25\n"
    "v_cvt_pk_bf16_f32 v25, v26, v27\n"
    "v_mfma_f32_16x16x32_bf16 v[140:143], a[16:19], v[56:59], v[140:143]\n"
    "v_cvt_pk_bf16_f32 v26, v28, v29\n"
    "v_cvt_pk_bf16_f32 v27, v30, v31\n"
    "v_mfma_f32_16x16x32_bf16 v[156:159], a[20:23], v[56:59], v[156:159]\n"
    "v_mfma_f32_16x16x32_bf16 v[172:175], a[24:27], v[56:59], v[172:175]\n"
    "v_mfma_f32_16x16x32_bf16 v[188:191], a[28:31], v[56:59], v[188:191]\n"
    "v_mfma_f32_16x16x32_bf16 a[204:207], a[32:35], v[56:59], a[204:207]\n"
    "v_mfma_f32_16x16x32_bf16 a[220:223], a[36:39], v[56:59], a[220:223]\n"
    "v_mfma_f32_16x16x32_bf16 a[236:239], a[40:43], v[56:59], a[236:239]\n"
    "v_mfma_f32_16x16x32_bf16 a[252:255], a[44:47], v[56:59], a[252:255]\n"
    "v_mfma_f32_16x16x32_bf16 v[204:207], v[208:211], v[56:59], v[204:207]\n"
    "s_waitcnt vmcnt(14) lgkmcnt(0)\n"
    "s_barrier\n"
    "L_ph22:\n"
    "v_mfma_f32_16x16x32_bf16 v[32:35], a[48:51], a[96:99], 0\n"
    "buffer_load_dwordx4 v231, s[24:27], s42 offen nt lds\n"
    "v_mfma_f32_16x16x32_bf16 v[32:35], a[52:55], a[100:103], v[32:35]\n"
    "ds_read_b64_tr_b16 a[0:1], v218 offset:24576\n"
    "v_mfma_f32_16x16x32_bf16 v[32:35], a[56:59], a[104:107], v[32:35]\n"
    "buffer_load_dwordx4 v232, s[24:27], s42 offen offset:1024 nt lds\n"
    "v_mfma_f32_16x16x32_bf16 v[32:35], a[60:63], a[108:111], v[32:35]\n"
    "ds_read_b64_tr_b16 a[2:3], v218 offset:30720\n"
    "v_mfma_f32_16x16x32_bf16 v[32:35], a[64:67], a[112:115], v[32:35]\n"
    "buffer_load_dwordx4 v233, s[24:27], s42 offen offset:2048 nt lds\n"
    "v_mfma_f32_16x16x32_bf16 v[32:35], a[68:71], a[116:119], v[32:35]\n"
    "s_add_u32 m0, s44, 15360\n"
    "s_add_u32 s43, s41, 15360\n"
    "v_mfma_f32_16x16x32_bf16 v[36:39], a[72:75], a[96:99], 0\n"
    "buffer_load_dwordx4 v230, s[16:19], s43 offen offset:2048 nt lds\n"
    "v_mfma_f32_16x16x32_bf16 v[36:39], a[76:79], a[100:103], v[36:39]\n"
    "ds_read_b64_tr_b16 a[4:5], v219 offset:24576\n"
    "s_add_u32 m0, s37, 24576\n"
    "v_mfma_f32_16x16x32_bf16 v[36:39], a[80:83], a[104:107], v[36:39]\n"
    "ds_read_b64_tr_b16 a[6:7], v219 offset:30720\n"
    "s_add_u32 s42, s38, 172032\n"
    "v_mfma_f32_16x16x32_bf16 v[36:39], a[84:87], a[108:111], v[36:39]\n"
    "ds_read_b64_tr_b16 a[8:9], v220 offset:24576\n"
    "v_mfma_f32_16x16x32_bf16 v[36:39], a[88:91], a[112:115], v[36:39]\n"
    "ds_read_b64_tr_b16 a[10:11], v220 offset:30720\n"
    "v_mfma_f32_16x16x32_bf16 v[36:39], a[92:95], a[116:119], v[36:39]\n"
    "ds_read_b64_tr_b16 a[12:13], v221 offset:24576\n"
    "v_mfma_f32_16x16x32_bf16 v[40:43], a[48:51], a[120:123], 0\n"
    "ds_read_b64_tr_b16 a[14:15], v221 offset:30720\n"
    "v_mfma_f32_16x16x32_bf16 v[40:43], a[52:55], a[124:127], v[40:43]\n"
    "ds_read_b64_tr_b16 a[16:17], v218 offset:24704\n"
    "v_fma_f32 v32, v32, s36, v212\n"
    "v_mfma_f32_16x16x32_bf16 v[40:43], a[56:59], a[128:131], v[40:43]\n"
    "ds_read_b64_tr_b16 a[18:19], v218 offset:30848\n"
    "v_fma_f32 v33, v33, s36, v212\n"
    "v_mfma_f32_16x16x32_bf16 v[40:43], a[60:63], a[132:135], v[40:43]\n"
    "ds_read_b64_tr_b16 a[20:21], v219 offset:24704\n"
    "v_fma_f32 v34, v34, s36, v212\n"
    "v_mfma_f32_16x16x32_bf16 v[40:43], a[64:67], a[136:139], v[40:43]\n"
    "ds_read_b64_tr_b16 a[22:23], v219 offset:30848\n"
    "v_fma_f32 v35, v35, s36, v212\n"
    "v_mfma_f32_16x16x32_bf16 v[40:43], a[68:71], a[140:143], v[40:43]\n"
    "ds_read_b64_tr_b16 a[24:25], v220 offset:24704\n"
    "v_mfma_f32_16x16x32_bf16 v[44:47], a[72:75], a[120:123], 0\n"
    "ds_read_b64_tr_b16 a[26:27], v220 offset:30848\n"
    "v_mfma_f32_16x16x32_bf16 v[44:47], a[76:79], a[124:127], v[44:47]\n"
    "ds_read_b64_tr_b16 a[28:29], v221 offset:24704\n"
    "v_fma_f32 v36, v36, s36, v212\n"
    "v_mfma_f32_16x16x32_bf16 v[44:47], a[80:83], a[128:131], v[44:47]\n"
    "ds_read_b64_tr_b16 a[30:31], v221 offset:30848\n"
    "v_fma_f32 v37, v37, s36, v212\n"
    "v_mfma_f32_16x16x32_bf16 v[44:47], a[84:87], a[132:135], v[44:47]\n"
    "ds_read_b64_tr_b16 a[32:33], v218 offset:24832\n"
    "v_fma_f32 v38, v38, s36, v212\n"
    "v_mfma_f32_16x16x32_bf16 v[44:47], a[88:91], a[136:139], v[44:47]\n"
    "ds_read_b64_tr_b16 a[34:35], v218 offset:30976\n"
    "v_fma_f32 v39, v39, s36, v212\n"
    "v_mfma_f32_16x16x32_bf16 v[44:47], a[92:95], a[140:143], v[44:47]\n"
    "ds_read_b64_tr_b16 a[36:37], v219 offset:24832\n"
    "v_mfma_f32_16x16x32_bf16 v[48:51], a[48:51], a[144:147], 0\n"
    "ds_read_b64_tr_b16 a[38:39], v219 offset:30976\n"
    "v_mfma_f32_16x16x32_bf16 v[48:51], a[52:55], a[148:151], v[48:51]\n"
    "ds_read_b64_tr_b16 a[40:41], v220 offset:24832\n"
    "v_fma_f32 v40, v40, s36, v213\n"
    "v_mfma_f32_16x16x32_bf16 v[48:51], a[56:59], a[152:155], v[48:51]\n"
    "ds_read_b64_tr_b16 a[42:43], v220 offset:30976\n"
    "v_fma_f32 v41, v41, s36, v213\n"
    "v_mfma_f32_16x16x32_bf16 v[48:51], a[60:63], a[156:159], v[48:51]\n"
    "ds_read_b64_tr_b16 a[44:45], v221 offset:24832\n"
    "v_fma_f32 v42, v42, s36, v213\n"
    "v_mfma_f32_16x16x32_bf16 v[48:51], a[64:67], a[160:163], v[48:51]\n"
    "ds_read_b64_tr_b16 a[46:47], v221 offset:30976\n"
    "v_fma_f32 v43, v43, s36, v213\n"
    "v_mfma_f32_16x16x32_bf16 v[48:51], a[68:71], a[164:167], v[48:51]\n"
    "v_exp_f32 v32, v32\n"
    "v_mfma_f32_16x16x32_bf16 v[52:55], a[72:75], a[144:147], 0\n"
    "v_exp_f32 v33, v33\n"
    "v_mfma_f32_16x16x32_bf16 v[52:55], a[76:79], a[148:151], v[52:55]\n"
    "v_exp_f32 v34, v34\n"
    "v_mfma_f32_16x16x32_bf16 v[52:55], a[80:83], a[152:155], v[52:55]\n"
    "v_exp_f32 v35, v35\n"
    "v_mfma_f32_16x16x32_bf16 v[52:55], a[84:87], a[156:159], v[52:55]\n"
    "v_exp_f32 v36, v36\n"
    "v_mfma_f32_16x16x32_bf16 v[52:55], a[88:91], a[160:163], v[52:55]\n"
    "v_exp_f32 v37, v37\n"
    "v_mfma_f32_16x16x32_bf16 v[52:55], a[92:95], a[164:167], v[52:55]\n"
    "v_exp_f32 v38, v38\n"
    "v_mfma_f32_16x16x32_bf16 v[56:59], a[48:51], a[168:171], 0\n"
    "v_exp_f32 v39, v39\n"
    "v_mfma_f32_16x16x32_bf16 v[56:59], a[52:55], a[172:175], v[56:59]\n"
    "v_cvt_pk_bf16_f32 v32, v32, v33\n"
    "v_cvt_pk_bf16_f32 v33, v34, v35\n"
    "v_mfma_f32_16x16x32_bf16 v[56:59], a[56:59], a[176:179], v[56:59]\n"
    "v_cvt_pk_bf16_f32 v34, v36, v37\n"
    "v_cvt_pk_bf16_f32 v35, v38, v39\n"
    "v_mfma_f32_16x16x32_bf16 v[56:59], a[60:63], a[180:183], v[56:59]\n"
    "v_exp_f32 v40, v40\n"
    "v_mfma_f32_16x16x32_bf16 v[56:59], a[64:67], a[184:187], v[56:59]\n"
    "v_exp_f32 v41, v41\n"
    "v_mfma_f32_16x16x32_bf16 v[56:59], a[68:71], a[188:191], v[56:59]\n"
    "v_exp_f32 v42, v42\n"
    "v_mfma_f32_16x16x32_bf16 v[60:63], a[72:75], a[168:171], 0\n"
    "v_exp_f32 v43, v43\n"
    "v_mfma_f32_16x16x32_bf16 v[60:63], a[76:79], a[172:175], v[60:63]\n"
    "v_fma_f32 v44, v44, s36, v213\n"
    "v_fma_f32 v45, v45, s36, v213\n"
    "v_mfma_f32_16x16x32_bf16 v[60:63], a[80:83], a[176:179], v[60:63]\n"
    "v_exp_f32 v44, v44\n"
    "v_mfma_f32_16x16x32_bf16 v[60:63], a[84:87], a[180:183], v[60:63]\n"
    "v_exp_f32 v45, v45\n"
    "v_mfma_f32_16x16x32_bf16 v[60:63], a[88:91], a[184:187], v[60:63]\n"
    "v_fma_f32 v46, v46, s36, v213\n"
    "v_fma_f32 v47, v47, s36, v213\n"
    "v_mfma_f32_16x16x32_bf16 v[60:63], a[92:95], a[188:191], v[60:63]\n"
    "s_waitcnt vmcnt(14) lgkmcnt(0)\n"
    "s_barrier\n"
    "L_ph23:\n"
    "v_mfma_f32_16x16x32_bf16 v[64:67], a[0:3], v[0:3], v[64:67]\n"
    "buffer_load_dwordx4 v222, s[20:23], s42 offen nt lds\n"
    "v_mfma_f32_16x16x32_bf16 v[80:83], a[4:7], v[0:3], v[80:83]\n"
    "ds_read_b128 a[48:51], v216 offset:36864\n"
    "v_cvt_pk_bf16_f32 v40, v40, v41\n"
    "v_mfma_f32_16x16x32_bf16 v[96:99], a[8:11], v[0:3], v[96:99]\n"
    "buffer_load_dwordx4 v223, s[20:23], s42 offen offset:1024 nt lds\n"
    "v_mfma_f32_16x16x32_bf16 v[112:115], a[12:15], v[0:3], v[112:115]\n"
    "ds_read_b128 a[52:55], v217 offset:36864\n"
    "v_cvt_pk_bf16_f32 v41, v42, v43\n"
    "v_mfma_f32_16x16x32_bf16 v[128:131], a[16:19], v[0:3], v[128:131]\n"
    "buffer_load_dwordx4 v224, s[20:23], s42 offen offset:2048 nt lds\n"
    "v_mfma_f32_16x16x32_bf16 v[144:147], a[20:23], v[0:3], v[144:147]\n"
    "s_add_u32 m0, s44, 18432\n"
    "s_add_u32 s43, s41, 18432\n"
    "v_mfma_f32_16x16x32_bf16 v[160:163], a[24:27], v[0:3], v[160:163]\n"
    "buffer_load_dwordx4 v225, s[16:19], s43 offen nt lds\n"
    "v_mfma_f32_16x16x32_bf16 v[176:179], a[28:31], v[0:3], v[176:179]\n"
    "s_mov_b32 m0, 159744\n"
    "s_mov_b32 s51, L_ph27-L_code_start\n"
    "v_mfma_f32_16x16x32_bf16 a[192:195], a[32:35], v[0:3], a[192:195]\n"
    "buffer_load_dword v249, s[52:55], s51 offen lds\n"
    "v_mfma_f32_16x16x32_bf16 a[208:211], a[36:39], v[0:3], a[208:211]\n"
    "ds_read_b128 a[56:59], v216 offset:36992\n"
    "s_add_u32 m0, s37, 36864\n"
    "v_mfma_f32_16x16x32_bf16 a[224:227], a[40:43], v[0:3], a[224:227]\n"
    "ds_read_b128 a[60:63], v217 offset:36992\n"
    "s_add_u32 s42, s38, 159744\n"
    "v_mfma_f32_16x16x32_bf16 a[240:243], a[44:47], v[0:3], a[240:243]\n"
    "ds_read_b128 a[64:67], v216 offset:37120\n"
    "v_cvt_pk_bf16_f32 v42, v44, v45\n"
    "v_mfma_f32_16x16x32_bf16 v[192:195], v[208:211], v[0:3], v[192:195]\n"
    "ds_read_b128 a[68:71], v217 offset:37120\n"
    "v_fma_f32 v48, v48, s36, v214\n"
    "v_mfma_f32_16x16x32_bf16 v[68:71], a[0:3], v[8:11], v[68:71]\n"
    "ds_read_b128 a[72:75], v216 offset:43008\n"
    "v_fma_f32 v49, v49, s36, v214\n"
    "v_mfma_f32_16x16x32_bf16 v[84:87], a[4:7], v[8:11], v[84:87]\n"
    "ds_read_b128 a[76:79], v217 offset:43008\n"
    "v_fma_f32 v50, v50, s36, v214\n"
    "v_mfma_f32_16x16x32_bf16 v[100:103], a[8:11], v[8:11], v[100:103]\n"
    "ds_read_b128 a[80:83], v216 offset:43136\n"
    "v_fma_f32 v51, v51, s36, v214\n"
    "v_mfma_f32_16x16x32_bf16 v[116:119], a[12:15], v[8:11], v[116:119]\n"
    "ds_read_b128 a[84:87], v217 offset:43136\n"
    "v_fma_f32 v52, v52, s36, v214\n"
    "v_mfma_f32_16x16x32_bf16 v[132:135], a[16:19], v[8:11], v[132:135]\n"
    "ds_read_b128 a[88:91], v216 offset:43264\n"
    "v_fma_f32 v53, v53, s36, v214\n"
    "v_mfma_f32_16x16x32_bf16 v[148:151], a[20:23], v[8:11], v[148:151]\n"
    "ds_read_b128 a[92:95], v217 offset:43264\n"
    "v_fma_f32 v54, v54, s36, v214\n"
    "v_mfma_f32_16x16x32_bf16 v[164:167], a[24:27], v[8:11], v[164:167]\n"
    "v_exp_f32 v46, v46\n"
    "v_mfma_f32_16x16x32_bf16 v[180:183], a[28:31], v[8:11], v[180:183]\n"
    "v_exp_f32 v47, v47\n"
    "v_mfma_f32_16x16x32_bf16 a[196:199], a[32:35], v[8:11], a[196:199]\n"
    "v_cvt_pk_bf16_f32 v43, v46, v47\n"
    "v_fma_f32 v55, v55, s36, v214\n"
    "v_mfma_f32_16x16x32_bf16 a[212:215], a[36:39], v[8:11], a[212:215]\n"
    "v_exp_f32 v48, v48\n"
    "v_mfma_f32_16x16x32_bf16 a[228:231], a[40:43], v[8:11], a[228:231]\n"
    "v_exp_f32 v49, v49\n"
    "v_mfma_f32_16x16x32_bf16 a[244:247], a[44:47], v[8:11], a[244:247]\n"
    "v_exp_f32 v50, v50\n"
    "v_mfma_f32_16x16x32_bf16 v[196:199], v[208:211], v[8:11], v[196:199]\n"
    "v_exp_f32 v51, v51\n"
    "v_mfma_f32_16x16x32_bf16 v[72:75], a[0:3], v[16:19], v[72:75]\n"
    "v_exp_f32 v52, v52\n"
    "v_mfma_f32_16x16x32_bf16 v[88:91], a[4:7], v[16:19], v[88:91]\n"
    "v_exp_f32 v53, v53\n"
    "v_mfma_f32_16x16x32_bf16 v[104:107], a[8:11], v[16:19], v[104:107]\n"
    "v_exp_f32 v54, v54\n"
    "v_mfma_f32_16x16x32_bf16 v[120:123], a[12:15], v[16:19], v[120:123]\n"
    "v_exp_f32 v55, v55\n"
    "v_mfma_f32_16x16x32_bf16 v[136:139], a[16:19], v[16:19], v[136:139]\n"
    "v_cvt_pk_bf16_f32 v48, v48, v49\n"
    "v_cvt_pk_bf16_f32 v49, v50, v51\n"
    "v_mfma_f32_16x16x32_bf16 v[152:155], a[20:23], v[16:19], v[152:155]\n"
    "v_cvt_pk_bf16_f32 v50, v52, v53\n"
    "v_cvt_pk_bf16_f32 v51, v54, v55\n"
    "v_mfma_f32_16x16x32_bf16 v[168:171], a[24:27], v[16:19], v[168:171]\n"
    "v_fma_f32 v56, v56, s36, v215\n"
    "v_fma_f32 v57, v57, s36, v215\n"
    "v_mfma_f32_16x16x32_bf16 v[184:187], a[28:31], v[16:19], v[184:187]\n"
    "v_exp_f32 v56, v56\n"
    "v_mfma_f32_16x16x32_bf16 a[200:203], a[32:35], v[16:19], a[200:203]\n"
    "v_exp_f32 v57, v57\n"
    "v_mfma_f32_16x16x32_bf16 a[216:219], a[36:39], v[16:19], a[216:219]\n"
    "v_fma_f32 v58, v58, s36, v215\n"
    "v_fma_f32 v59, v59, s36, v215\n"
    "v_mfma_f32_16x16x32_bf16 a[232:235], a[40:43], v[16:19], a[232:235]\n"
    "v_exp_f32 v58, v58\n"
    "v_mfma_f32_16x16x32_bf16 a[248:251], a[44:47], v[16:19], a[248:251]\n"
    "v_exp_f32 v59, v59\n"
    "v_mfma_f32_16x16x32_bf16 v[200:203], v[208:211], v[16:19], v[200:203]\n"
    "v_fma_f32 v60, v60, s36, v215\n"
    "v_fma_f32 v61, v61, s36, v215\n"
    "v_mfma_f32_16x16x32_bf16 v[76:79], a[0:3], v[24:27], v[76:79]\n"
    "v_exp_f32 v60, v60\n"
    "v_mfma_f32_16x16x32_bf16 v[92:95], a[4:7], v[24:27], v[92:95]\n"
    "v_exp_f32 v61, v61\n"
    "v_mfma_f32_16x16x32_bf16 v[108:111], a[8:11], v[24:27], v[108:111]\n"
    "v_fma_f32 v62, v62, s36, v215\n"
    "v_fma_f32 v63, v63, s36, v215\n"
    "v_mfma_f32_16x16x32_bf16 v[124:127], a[12:15], v[24:27], v[124:127]\n"
    "v_exp_f32 v62, v62\n"
    "v_mfma_f32_16x16x32_bf16 v[140:143], a[16:19], v[24:27], v[140:143]\n"
    "v_exp_f32 v63, v63\n"
    "v_mfma_f32_16x16x32_bf16 v[156:159], a[20:23], v[24:27], v[156:159]\n"
    "v_cvt_pk_bf16_f32 v56, v56, v57\n"
    "v_cvt_pk_bf16_f32 v57, v58, v59\n"
    "v_mfma_f32_16x16x32_bf16 v[172:175], a[24:27], v[24:27], v[172:175]\n"
    "v_cvt_pk_bf16_f32 v58, v60, v61\n"
    "v_cvt_pk_bf16_f32 v59, v62, v63\n"
    "v_mfma_f32_16x16x32_bf16 v[188:191], a[28:31], v[24:27], v[188:191]\n"
    "v_mfma_f32_16x16x32_bf16 a[204:207], a[32:35], v[24:27], a[204:207]\n"
    "v_mfma_f32_16x16x32_bf16 a[220:223], a[36:39], v[24:27], a[220:223]\n"
    "v_mfma_f32_16x16x32_bf16 a[236:239], a[40:43], v[24:27], a[236:239]\n"
    "v_mfma_f32_16x16x32_bf16 a[252:255], a[44:47], v[24:27], a[252:255]\n"
    "v_mfma_f32_16x16x32_bf16 v[204:207], v[208:211], v[24:27], v[204:207]\n"
    "s_waitcnt vmcnt(14) lgkmcnt(0)\n"
    "s_barrier\n"
    "L_ph24:\n"
    "v_mfma_f32_16x16x32_bf16 v[0:3], a[48:51], a[96:99], 0\n"
    "buffer_load_dwordx4 v231, s[24:27], s42 offen nt lds\n"
    "v_mfma_f32_16x16x32_bf16 v[0:3], a[52:55], a[100:103], v[0:3]\n"
    "ds_read_b64_tr_b16 a[0:1], v218 offset:49152\n"
    "v_mfma_f32_16x16x32_bf16 v[0:3], a[56:59], a[104:107], v[0:3]\n"
    "buffer_load_dwordx4 v232, s[24:27], s42 offen offset:1024 nt lds\n"
    "v_mfma_f32_16x16x32_bf16 v[0:3], a[60:63], a[108:111], v[0:3]\n"
    "ds_read_b64_tr_b16 a[2:3], v218 offset:55296\n"
    "v_mfma_f32_16x16x32_bf16 v[0:3], a[64:67], a[112:115], v[0:3]\n"
    "buffer_load_dwordx4 v233, s[24:27], s42 offen offset:2048 nt lds\n"
    "v_mfma_f32_16x16x32_bf16 v[0:3], a[68:71], a[116:119], v[0:3]\n"
    "s_add_u32 m0, s44, 18432\n"
    "s_add_u32 s43, s41, 18432\n"
    "v_mfma_f32_16x16x32_bf16 v[4:7], a[72:75], a[96:99], 0\n"
    "buffer_load_dwordx4 v226, s[16:19], s43 offen offset:1024 nt lds\n"
    "v_mfma_f32_16x16x32_bf16 v[4:7], a[76:79], a[100:103], v[4:7]\n"
    "ds_read_b64_tr_b16 a[4:5], v219 offset:49152\n"
    "s_add_u32 m0, s37, 49152\n"
    "v_mfma_f32_16x16x32_bf16 v[4:7], a[80:83], a[104:107], v[4:7]\n"
    "ds_read_b64_tr_b16 a[6:7], v219 offset:55296\n"
    "s_add_u32 s42, s38, 184320\n"
    "v_mfma_f32_16x16x32_bf16 v[4:7], a[84:87], a[108:111], v[4:7]\n"
    "ds_read_b64_tr_b16 a[8:9], v220 offset:49152\n"
    "v_mfma_f32_16x16x32_bf16 v[4:7], a[88:91], a[112:115], v[4:7]\n"
    "ds_read_b64_tr_b16 a[10:11], v220 offset:55296\n"
    "v_mfma_f32_16x16x32_bf16 v[4:7], a[92:95], a[116:119], v[4:7]\n"
    "ds_read_b64_tr_b16 a[12:13], v221 offset:49152\n"
    "v_mfma_f32_16x16x32_bf16 v[8:11], a[48:51], a[120:123], 0\n"
    "ds_read_b64_tr_b16 a[14:15], v221 offset:55296\n"
    "v_mfma_f32_16x16x32_bf16 v[8:11], a[52:55], a[124:127], v[8:11]\n"
    "ds_read_b64_tr_b16 a[16:17], v218 offset:49280\n"
    "v_fma_f32 v0, v0, s36, v212\n"
    "v_mfma_f32_16x16x32_bf16 v[8:11], a[56:59], a[128:131], v[8:11]\n"
    "ds_read_b64_tr_b16 a[18:19], v218 offset:55424\n"
    "v_fma_f32 v1, v1, s36, v212\n"
    "v_mfma_f32_16x16x32_bf16 v[8:11], a[60:63], a[132:135], v[8:11]\n"
    "ds_read_b64_tr_b16 a[20:21], v219 offset:49280\n"
    "v_fma_f32 v2, v2, s36, v212\n"
    "v_mfma_f32_16x16x32_bf16 v[8:11], a[64:67], a[136:139], v[8:11]\n"
    "ds_read_b64_tr_b16 a[22:23], v219 offset:55424\n"
    "v_fma_f32 v3, v3, s36, v212\n"
    "v_mfma_f32_16x16x32_bf16 v[8:11], a[68:71], a[140:143], v[8:11]\n"
    "ds_read_b64_tr_b16 a[24:25], v220 offset:49280\n"
    "v_mfma_f32_16x16x32_bf16 v[12:15], a[72:75], a[120:123], 0\n"
    "ds_read_b64_tr_b16 a[26:27], v220 offset:55424\n"
    "v_mfma_f32_16x16x32_bf16 v[12:15], a[76:79], a[124:127], v[12:15]\n"
    "ds_read_b64_tr_b16 a[28:29], v221 offset:49280\n"
    "v_fma_f32 v4, v4, s36, v212\n"
    "v_mfma_f32_16x16x32_bf16 v[12:15], a[80:83], a[128:131], v[12:15]\n"
    "ds_read_b64_tr_b16 a[30:31], v221 offset:55424\n"
    "v_fma_f32 v5, v5, s36, v212\n"
    "v_mfma_f32_16x16x32_bf16 v[12:15], a[84:87], a[132:135], v[12:15]\n"
    "ds_read_b64_tr_b16 a[32:33], v218 offset:49408\n"
    "v_fma_f32 v6, v6, s36, v212\n"
    "v_mfma_f32_16x16x32_bf16 v[12:15], a[88:91], a[136:139], v[12:15]\n"
    "ds_read_b64_tr_b16 a[34:35], v218 offset:55552\n"
    "v_fma_f32 v7, v7, s36, v212\n"
    "v_mfma_f32_16x16x32_bf16 v[12:15], a[92:95], a[140:143], v[12:15]\n"
    "ds_read_b64_tr_b16 a[36:37], v219 offset:49408\n"
    "v_mfma_f32_16x16x32_bf16 v[16:19], a[48:51], a[144:147], 0\n"
    "ds_read_b64_tr_b16 a[38:39], v219 offset:55552\n"
    "v_mfma_f32_16x16x32_bf16 v[16:19], a[52:55], a[148:151], v[16:19]\n"
    "ds_read_b64_tr_b16 a[40:41], v220 offset:49408\n"
    "v_fma_f32 v8, v8, s36, v213\n"
    "v_mfma_f32_16x16x32_bf16 v[16:19], a[56:59], a[152:155], v[16:19]\n"
    "ds_read_b64_tr_b16 a[42:43], v220 offset:55552\n"
    "v_fma_f32 v9, v9, s36, v213\n"
    "v_mfma_f32_16x16x32_bf16 v[16:19], a[60:63], a[156:159], v[16:19]\n"
    "ds_read_b64_tr_b16 a[44:45], v221 offset:49408\n"
    "v_fma_f32 v10, v10, s36, v213\n"
    "v_mfma_f32_16x16x32_bf16 v[16:19], a[64:67], a[160:163], v[16:19]\n"
    "ds_read_b64_tr_b16 a[46:47], v221 offset:55552\n"
    "v_fma_f32 v11, v11, s36, v213\n"
    "v_mfma_f32_16x16x32_bf16 v[16:19], a[68:71], a[164:167], v[16:19]\n"
    "v_exp_f32 v0, v0\n"
    "v_mfma_f32_16x16x32_bf16 v[20:23], a[72:75], a[144:147], 0\n"
    "v_exp_f32 v1, v1\n"
    "v_mfma_f32_16x16x32_bf16 v[20:23], a[76:79], a[148:151], v[20:23]\n"
    "v_exp_f32 v2, v2\n"
    "v_mfma_f32_16x16x32_bf16 v[20:23], a[80:83], a[152:155], v[20:23]\n"
    "v_exp_f32 v3, v3\n"
    "v_mfma_f32_16x16x32_bf16 v[20:23], a[84:87], a[156:159], v[20:23]\n"
    "v_exp_f32 v4, v4\n"
    "v_mfma_f32_16x16x32_bf16 v[20:23], a[88:91], a[160:163], v[20:23]\n"
    "v_exp_f32 v5, v5\n"
    "v_mfma_f32_16x16x32_bf16 v[20:23], a[92:95], a[164:167], v[20:23]\n"
    "v_exp_f32 v6, v6\n"
    "v_mfma_f32_16x16x32_bf16 v[24:27], a[48:51], a[168:171], 0\n"
    "v_exp_f32 v7, v7\n"
    "v_mfma_f32_16x16x32_bf16 v[24:27], a[52:55], a[172:175], v[24:27]\n"
    "v_cvt_pk_bf16_f32 v0, v0, v1\n"
    "v_cvt_pk_bf16_f32 v1, v2, v3\n"
    "v_mfma_f32_16x16x32_bf16 v[24:27], a[56:59], a[176:179], v[24:27]\n"
    "v_cvt_pk_bf16_f32 v2, v4, v5\n"
    "v_cvt_pk_bf16_f32 v3, v6, v7\n"
    "v_mfma_f32_16x16x32_bf16 v[24:27], a[60:63], a[180:183], v[24:27]\n"
    "v_exp_f32 v8, v8\n"
    "v_mfma_f32_16x16x32_bf16 v[24:27], a[64:67], a[184:187], v[24:27]\n"
    "v_exp_f32 v9, v9\n"
    "v_mfma_f32_16x16x32_bf16 v[24:27], a[68:71], a[188:191], v[24:27]\n"
    "v_exp_f32 v10, v10\n"
    "v_mfma_f32_16x16x32_bf16 v[28:31], a[72:75], a[168:171], 0\n"
    "v_exp_f32 v11, v11\n"
    "v_mfma_f32_16x16x32_bf16 v[28:31], a[76:79], a[172:175], v[28:31]\n"
    "v_fma_f32 v12, v12, s36, v213\n"
    "v_fma_f32 v13, v13, s36, v213\n"
    "v_mfma_f32_16x16x32_bf16 v[28:31], a[80:83], a[176:179], v[28:31]\n"
    "v_exp_f32 v12, v12\n"
    "v_mfma_f32_16x16x32_bf16 v[28:31], a[84:87], a[180:183], v[28:31]\n"
    "v_exp_f32 v13, v13\n"
    "v_mfma_f32_16x16x32_bf16 v[28:31], a[88:91], a[184:187], v[28:31]\n"
    "v_fma_f32 v14, v14, s36, v213\n"
    "v_fma_f32 v15, v15, s36, v213\n"
    "v_mfma_f32_16x16x32_bf16 v[28:31], a[92:95], a[188:191], v[28:31]\n"
    "s_waitcnt vmcnt(14) lgkmcnt(0)\n"
    "s_barrier\n"
    "L_ph25:\n"
    "v_mfma_f32_16x16x32_bf16 v[64:67], a[0:3], v[32:35], v[64:67]\n"
    "buffer_load_dwordx4 v222, s[20:23], s42 offen nt lds\n"
    "v_mfma_f32_16x16x32_bf16 v[80:83], a[4:7], v[32:35], v[80:83]\n"
    "ds_read_b128 a[48:51], v216 offset:0\n"
    "v_cvt_pk_bf16_f32 v8, v8, v9\n"
    "v_mfma_f32_16x16x32_bf16 v[96:99], a[8:11], v[32:35], v[96:99]\n"
    "buffer_load_dwordx4 v223, s[20:23], s42 offen offset:1024 nt lds\n"
    "v_mfma_f32_16x16x32_bf16 v[112:115], a[12:15], v[32:35], v[112:115]\n"
    "ds_read_b128 a[52:55], v217 offset:0\n"
    "v_cvt_pk_bf16_f32 v9, v10, v11\n"
    "v_mfma_f32_16x16x32_bf16 v[128:131], a[16:19], v[32:35], v[128:131]\n"
    "buffer_load_dwordx4 v224, s[20:23], s42 offen offset:2048 nt lds\n"
    "v_mfma_f32_16x16x32_bf16 v[144:147], a[20:23], v[32:35], v[144:147]\n"
    "s_add_u32 m0, s44, 18432\n"
    "s_add_u32 s43, s41, 18432\n"
    "v_mfma_f32_16x16x32_bf16 v[160:163], a[24:27], v[32:35], v[160:163]\n"
    "buffer_load_dwordx4 v227, s[16:19], s43 offen offset:2048 nt lds\n"
    "v_mfma_f32_16x16x32_bf16 v[176:179], a[28:31], v[32:35], v[176:179]\n"
    "ds_read_b128 a[56:59], v216 offset:128\n"
    "s_add_u32 m0, s37, 0\n"
    "v_mfma_f32_16x16x32_bf16 a[192:195], a[32:35], v[32:35], a[192:195]\n"
    "ds_read_b128 a[60:63], v217 offset:128\n"
    "s_add_u32 s42, s38, 172032\n"
    "v_mfma_f32_16x16x32_bf16 a[208:211], a[36:39], v[32:35], a[208:211]\n"
    "ds_read_b128 a[64:67], v216 offset:256\n"
    "v_cvt_pk_bf16_f32 v10, v12, v13\n"
    "v_mfma_f32_16x16x32_bf16 a[224:227], a[40:43], v[32:35], a[224:227]\n"
    "ds_read_b128 a[68:71], v217 offset:256\n"
    "v_fma_f32 v16, v16, s36, v214\n"
    "v_mfma_f32_16x16x32_bf16 a[240:243], a[44:47], v[32:35], a[240:243]\n"
    "ds_read_b128 a[72:75], v216 offset:6144\n"
    "v_fma_f32 v17, v17, s36, v214\n"
    "v_mfma_f32_16x16x32_bf16 v[192:195], v[208:211], v[32:35], v[192:195]\n"
    "ds_read_b128 a[76:79], v217 offset:6144\n"
    "v_fma_f32 v18, v18, s36, v214\n"
    "v_mfma_f32_16x16x32_bf16 v[68:71], a[0:3], v[40:43], v[68:71]\n"
    "ds_read_b128 a[80:83], v216 offset:6272\n"
    "v_fma_f32 v19, v19, s36, v214\n"
    "v_mfma_f32_16x16x32_bf16 v[84:87], a[4:7], v[40:43], v[84:87]\n"
    "ds_read_b128 a[84:87], v217 offset:6272\n"
    "v_fma_f32 v20, v20, s36, v214\n"
    "v_mfma_f32_16x16x32_bf16 v[100:103], a[8:11], v[40:43], v[100:103]\n"
    "ds_read_b128 a[88:91], v216 offset:6400\n"
    "v_fma_f32 v21, v21, s36, v214\n"
    "v_mfma_f32_16x16x32_bf16 v[116:119], a[12:15], v[40:43], v[116:119]\n"
    "ds_read_b128 a[92:95], v217 offset:6400\n"
    "v_fma_f32 v22, v22, s36, v214\n"
    "v_mfma_f32_16x16x32_bf16 v[132:135], a[16:19], v[40:43], v[132:135]\n"
    "v_exp_f32 v14, v14\n"
    "v_mfma_f32_16x16x32_bf16 v[148:151], a[20:23], v[40:43], v[148:151]\n"
    "v_exp_f32 v15, v15\n"
    "v_mfma_f32_16x16x32_bf16 v[164:167], a[24:27], v[40:43], v[164:167]\n"
    "v_cvt_pk_bf16_f32 v11, v14, v15\n"
    "v_fma_f32 v23, v23, s36, v214\n"
    "v_mfma_f32_16x16x32_bf16 v[180:183], a[28:31], v[40:43], v[180:183]\n"
    "v_exp_f32 v16, v16\n"
    "v_mfma_f32_16x16x32_bf16 a[196:199], a[32:35], v[40:43], a[196:199]\n"
    "v_exp_f32 v17, v17\n"
    "v_mfma_f32_16x16x32_bf16 a[212:215], a[36:39], v[40:43], a[212:215]\n"
    "v_exp_f32 v18, v18\n"
    "v_mfma_f32_16x16x32_bf16 a[228:231], a[40:43], v[40:43], a[228:231]\n"
    "v_exp_f32 v19, v19\n"
    "v_mfma_f32_16x16x32_bf16 a[244:247], a[44:47], v[40:43], a[244:247]\n"
    "v_exp_f32 v20, v20\n"
    "v_mfma_f32_16x16x32_bf16 v[196:199], v[208:211], v[40:43], v[196:199]\n"
    "v_exp_f32 v21, v21\n"
    "v_mfma_f32_16x16x32_bf16 v[72:75], a[0:3], v[48:51], v[72:75]\n"
    "v_exp_f32 v22, v22\n"
    "v_mfma_f32_16x16x32_bf16 v[88:91], a[4:7], v[48:51], v[88:91]\n"
    "v_exp_f32 v23, v23\n"
    "v_mfma_f32_16x16x32_bf16 v[104:107], a[8:11], v[48:51], v[104:107]\n"
    "v_cvt_pk_bf16_f32 v16, v16, v17\n"
    "v_cvt_pk_bf16_f32 v17, v18, v19\n"
    "v_mfma_f32_16x16x32_bf16 v[120:123], a[12:15], v[48:51], v[120:123]\n"
    "v_cvt_pk_bf16_f32 v18, v20, v21\n"
    "v_cvt_pk_bf16_f32 v19, v22, v23\n"
    "v_mfma_f32_16x16x32_bf16 v[136:139], a[16:19], v[48:51], v[136:139]\n"
    "v_fma_f32 v24, v24, s36, v215\n"
    "v_fma_f32 v25, v25, s36, v215\n"
    "v_mfma_f32_16x16x32_bf16 v[152:155], a[20:23], v[48:51], v[152:155]\n"
    "v_exp_f32 v24, v24\n"
    "v_mfma_f32_16x16x32_bf16 v[168:171], a[24:27], v[48:51], v[168:171]\n"
    "v_exp_f32 v25, v25\n"
    "v_mfma_f32_16x16x32_bf16 v[184:187], a[28:31], v[48:51], v[184:187]\n"
    "v_fma_f32 v26, v26, s36, v215\n"
    "v_fma_f32 v27, v27, s36, v215\n"
    "v_mfma_f32_16x16x32_bf16 a[200:203], a[32:35], v[48:51], a[200:203]\n"
    "v_exp_f32 v26, v26\n"
    "v_mfma_f32_16x16x32_bf16 a[216:219], a[36:39], v[48:51], a[216:219]\n"
    "v_exp_f32 v27, v27\n"
    "v_mfma_f32_16x16x32_bf16 a[232:235], a[40:43], v[48:51], a[232:235]\n"
    "v_fma_f32 v28, v28, s36, v215\n"
    "v_fma_f32 v29, v29, s36, v215\n"
    "v_mfma_f32_16x16x32_bf16 a[248:251], a[44:47], v[48:51], a[248:251]\n"
    "v_exp_f32 v28, v28\n"
    "v_mfma_f32_16x16x32_bf16 v[200:203], v[208:211], v[48:51], v[200:203]\n"
    "v_exp_f32 v29, v29\n"
    "v_mfma_f32_16x16x32_bf16 v[76:79], a[0:3], v[56:59], v[76:79]\n"
    "v_fma_f32 v30, v30, s36, v215\n"
    "v_fma_f32 v31, v31, s36, v215\n"
    "v_mfma_f32_16x16x32_bf16 v[92:95], a[4:7], v[56:59], v[92:95]\n"
    "v_exp_f32 v30, v30\n"
    "v_mfma_f32_16x16x32_bf16 v[108:111], a[8:11], v[56:59], v[108:111]\n"
    "v_exp_f32 v31, v31\n"
    "v_mfma_f32_16x16x32_bf16 v[124:127], a[12:15], v[56:59], v[124:127]\n"
    "v_cvt_pk_bf16_f32 v24, v24, v25\n"
    "v_cvt_pk_bf16_f32 v25, v26, v27\n"
    "v_mfma_f32_16x16x32_bf16 v[140:143], a[16:19], v[56:59], v[140:143]\n"
    "v_cvt_pk_bf16_f32 v26, v28, v29\n"
    "v_cvt_pk_bf16_f32 v27, v30, v31\n"
    "v_mfma_f32_16x16x32_bf16 v[156:159], a[20:23], v[56:59], v[156:159]\n"
    "v_mfma_f32_16x16x32_bf16 v[172:175], a[24:27], v[56:59], v[172:175]\n"
    "v_mfma_f32_16x16x32_bf16 v[188:191], a[28:31], v[56:59], v[188:191]\n"
    "v_mfma_f32_16x16x32_bf16 a[204:207], a[32:35], v[56:59], a[204:207]\n"
    "v_mfma_f32_16x16x32_bf16 a[220:223], a[36:39], v[56:59], a[220:223]\n"
    "v_mfma_f32_16x16x32_bf16 a[236:239], a[40:43], v[56:59], a[236:239]\n"
    "v_mfma_f32_16x16x32_bf16 a[252:255], a[44:47], v[56:59], a[252:255]\n"
    "v_mfma_f32_16x16x32_bf16 v[204:207], v[208:211], v[56:59], v[204:207]\n"
    "s_waitcnt vmcnt(14) lgkmcnt(0)\n"
    "s_barrier\n"
    "L_ph26:\n"
    "v_mfma_f32_16x16x32_bf16 v[32:35], a[48:51], a[96:99], 0\n"
    "buffer_load_dwordx4 v231, s[24:27], s42 offen nt lds\n"
    "v_mfma_f32_16x16x32_bf16 v[32:35], a[52:55], a[100:103], v[32:35]\n"
    "ds_read_b64_tr_b16 a[0:1], v218 offset:12288\n"
    "v_mfma_f32_16x16x32_bf16 v[32:35], a[56:59], a[104:107], v[32:35]\n"
    "buffer_load_dwordx4 v232, s[24:27], s42 offen offset:1024 nt lds\n"
    "v_mfma_f32_16x16x32_bf16 v[32:35], a[60:63], a[108:111], v[32:35]\n"
    "ds_read_b64_tr_b16 a[2:3], v218 offset:18432\n"
    "v_mfma_f32_16x16x32_bf16 v[32:35], a[64:67], a[112:115], v[32:35]\n"
    "buffer_load_dwordx4 v233, s[24:27], s42 offen offset:2048 nt lds\n"
    "v_mfma_f32_16x16x32_bf16 v[32:35], a[68:71], a[116:119], v[32:35]\n"
    "s_add_u32 m0, s44, 21504\n"
    "s_add_u32 s43, s41, 21504\n"
    "v_mfma_f32_16x16x32_bf16 v[36:39], a[72:75], a[96:99], 0\n"
    "buffer_load_dwordx4 v228, s[16:19], s43 offen nt lds\n"
    "v_mfma_f32_16x16x32_bf16 v[36:39], a[76:79], a[100:103], v[36:39]\n"
    "ds_read_b64_tr_b16 a[4:5], v219 offset:12288\n"
    "s_add_u32 m0, s37, 12288\n"
    "v_mfma_f32_16x16x32_bf16 v[36:39], a[80:83], a[104:107], v[36:39]\n"
    "ds_read_b64_tr_b16 a[6:7], v219 offset:18432\n"
    "s_add_u32 s42, s39, 0\n"
    "v_mfma_f32_16x16x32_bf16 v[36:39], a[84:87], a[108:111], v[36:39]\n"
    "ds_read_b64_tr_b16 a[8:9], v220 offset:12288\n"
    "v_mfma_f32_16x16x32_bf16 v[36:39], a[88:91], a[112:115], v[36:39]\n"
    "ds_read_b64_tr_b16 a[10:11], v220 offset:18432\n"
    "v_mfma_f32_16x16x32_bf16 v[36:39], a[92:95], a[116:119], v[36:39]\n"
    "ds_read_b64_tr_b16 a[12:13], v221 offset:12288\n"
    "v_mfma_f32_16x16x32_bf16 v[40:43], a[48:51], a[120:123], 0\n"
    "ds_read_b64_tr_b16 a[14:15], v221 offset:18432\n"
    "v_mfma_f32_16x16x32_bf16 v[40:43], a[52:55], a[124:127], v[40:43]\n"
    "ds_read_b64_tr_b16 a[16:17], v218 offset:12416\n"
    "v_fma_f32 v32, v32, s36, v212\n"
    "v_mfma_f32_16x16x32_bf16 v[40:43], a[56:59], a[128:131], v[40:43]\n"
    "ds_read_b64_tr_b16 a[18:19], v218 offset:18560\n"
    "v_fma_f32 v33, v33, s36, v212\n"
    "v_mfma_f32_16x16x32_bf16 v[40:43], a[60:63], a[132:135], v[40:43]\n"
    "ds_read_b64_tr_b16 a[20:21], v219 offset:12416\n"
    "v_fma_f32 v34, v34, s36, v212\n"
    "v_mfma_f32_16x16x32_bf16 v[40:43], a[64:67], a[136:139], v[40:43]\n"
    "ds_read_b64_tr_b16 a[22:23], v219 offset:18560\n"
    "v_fma_f32 v35, v35, s36, v212\n"
    "v_mfma_f32_16x16x32_bf16 v[40:43], a[68:71], a[140:143], v[40:43]\n"
    "ds_read_b64_tr_b16 a[24:25], v220 offset:12416\n"
    "v_mfma_f32_16x16x32_bf16 v[44:47], a[72:75], a[120:123], 0\n"
    "ds_read_b64_tr_b16 a[26:27], v220 offset:18560\n"
    "v_mfma_f32_16x16x32_bf16 v[44:47], a[76:79], a[124:127], v[44:47]\n"
    "ds_read_b64_tr_b16 a[28:29], v221 offset:12416\n"
    "v_fma_f32 v36, v36, s36, v212\n"
    "v_mfma_f32_16x16x32_bf16 v[44:47], a[80:83], a[128:131], v[44:47]\n"
    "ds_read_b64_tr_b16 a[30:31], v221 offset:18560\n"
    "v_fma_f32 v37, v37, s36, v212\n"
    "v_mfma_f32_16x16x32_bf16 v[44:47], a[84:87], a[132:135], v[44:47]\n"
    "ds_read_b64_tr_b16 a[32:33], v218 offset:12544\n"
    "v_fma_f32 v38, v38, s36, v212\n"
    "v_mfma_f32_16x16x32_bf16 v[44:47], a[88:91], a[136:139], v[44:47]\n"
    "ds_read_b64_tr_b16 a[34:35], v218 offset:18688\n"
    "v_fma_f32 v39, v39, s36, v212\n"
    "v_mfma_f32_16x16x32_bf16 v[44:47], a[92:95], a[140:143], v[44:47]\n"
    "ds_read_b64_tr_b16 a[36:37], v219 offset:12544\n"
    "v_mfma_f32_16x16x32_bf16 v[48:51], a[48:51], a[144:147], 0\n"
    "ds_read_b64_tr_b16 a[38:39], v219 offset:18688\n"
    "v_mfma_f32_16x16x32_bf16 v[48:51], a[52:55], a[148:151], v[48:51]\n"
    "ds_read_b64_tr_b16 a[40:41], v220 offset:12544\n"
    "v_fma_f32 v40, v40, s36, v213\n"
    "v_mfma_f32_16x16x32_bf16 v[48:51], a[56:59], a[152:155], v[48:51]\n"
    "ds_read_b64_tr_b16 a[42:43], v220 offset:18688\n"
    "v_fma_f32 v41, v41, s36, v213\n"
    "v_mfma_f32_16x16x32_bf16 v[48:51], a[60:63], a[156:159], v[48:51]\n"
    "ds_read_b64_tr_b16 a[44:45], v221 offset:12544\n"
    "v_fma_f32 v42, v42, s36, v213\n"
    "v_mfma_f32_16x16x32_bf16 v[48:51], a[64:67], a[160:163], v[48:51]\n"
    "ds_read_b64_tr_b16 a[46:47], v221 offset:18688\n"
    "v_fma_f32 v43, v43, s36, v213\n"
    "v_mfma_f32_16x16x32_bf16 v[48:51], a[68:71], a[164:167], v[48:51]\n"
    "v_exp_f32 v32, v32\n"
    "v_mfma_f32_16x16x32_bf16 v[52:55], a[72:75], a[144:147], 0\n"
    "v_exp_f32 v33, v33\n"
    "v_mfma_f32_16x16x32_bf16 v[52:55], a[76:79], a[148:151], v[52:55]\n"
    "v_exp_f32 v34, v34\n"
    "v_mfma_f32_16x16x32_bf16 v[52:55], a[80:83], a[152:155], v[52:55]\n"
    "v_exp_f32 v35, v35\n"
    "v_mfma_f32_16x16x32_bf16 v[52:55], a[84:87], a[156:159], v[52:55]\n"
    "v_exp_f32 v36, v36\n"
    "v_mfma_f32_16x16x32_bf16 v[52:55], a[88:91], a[160:163], v[52:55]\n"
    "v_exp_f32 v37, v37\n"
    "v_mfma_f32_16x16x32_bf16 v[52:55], a[92:95], a[164:167], v[52:55]\n"
    "v_exp_f32 v38, v38\n"
    "v_mfma_f32_16x16x32_bf16 v[56:59], a[48:51], a[168:171], 0\n"
    "v_exp_f32 v39, v39\n"
    "v_mfma_f32_16x16x32_bf16 v[56:59], a[52:55], a[172:175], v[56:59]\n"
    "v_cvt_pk_bf16_f32 v32, v32, v33\n"
    "v_cvt_pk_bf16_f32 v33, v34, v35\n"
    "v_mfma_f32_16x16x32_bf16 v[56:59], a[56:59], a[176:179], v[56:59]\n"
    "v_cvt_pk_bf16_f32 v34, v36, v37\n"
    "v_cvt_pk_bf16_f32 v35, v38, v39\n"
    "v_mfma_f32_16x16x32_bf16 v[56:59], a[60:63], a[180:183], v[56:59]\n"
    "v_exp_f32 v40, v40\n"
    "v_mfma_f32_16x16x32_bf16 v[56:59], a[64:67], a[184:187], v[56:59]\n"
    "v_exp_f32 v41, v41\n"
    "v_mfma_f32_16x16x32_bf16 v[56:59], a[68:71], a[188:191], v[56:59]\n"
    "v_exp_f32 v42, v42\n"
    "v_mfma_f32_16x16x32_bf16 v[60:63], a[72:75], a[168:171], 0\n"
    "v_exp_f32 v43, v43\n"
    "v_mfma_f32_16x16x32_bf16 v[60:63], a[76:79], a[172:175], v[60:63]\n"
    "v_fma_f32 v44, v44, s36, v213\n"
    "v_fma_f32 v45, v45, s36, v213\n"
    "v_mfma_f32_16x16x32_bf16 v[60:63], a[80:83], a[176:179], v[60:63]\n"
    "v_exp_f32 v44, v44\n"
    "v_mfma_f32_16x16x32_bf16 v[60:63], a[84:87], a[180:183], v[60:63]\n"
    "v_exp_f32 v45, v45\n"
    "v_mfma_f32_16x16x32_bf16 v[60:63], a[88:91], a[184:187], v[60:63]\n"
    "v_fma_f32 v46, v46, s36, v213\n"
    "v_fma_f32 v47, v47, s36, v213\n"
    "v_mfma_f32_16x16x32_bf16 v[60:63], a[92:95], a[188:191], v[60:63]\n"
    "s_waitcnt vmcnt(14) lgkmcnt(0)\n"
    "s_barrier\n"
    "L_ph27:\n"
    "v_mfma_f32_16x16x32_bf16 v[64:67], a[0:3], v[0:3], v[64:67]\n"
    "buffer_load_dwordx4 v222, s[20:23], s42 offen nt lds\n"
    "v_mfma_f32_16x16x32_bf16 v[80:83], a[4:7], v[0:3], v[80:83]\n"
    "ds_read_b128 a[48:51], v216 offset:24576\n"
    "v_cvt_pk_bf16_f32 v40, v40, v41\n"
    "v_mfma_f32_16x16x32_bf16 v[96:99], a[8:11], v[0:3], v[96:99]\n"
    "buffer_load_dwordx4 v223, s[20:23], s42 offen offset:1024 nt lds\n"
    "v_mfma_f32_16x16x32_bf16 v[112:115], a[12:15], v[0:3], v[112:115]\n"
    "ds_read_b128 a[52:55], v217 offset:24576\n"
    "v_cvt_pk_bf16_f32 v41, v42, v43\n"
    "v_mfma_f32_16x16x32_bf16 v[128:131], a[16:19], v[0:3], v[128:131]\n"
    "buffer_load_dwordx4 v224, s[20:23], s42 offen offset:2048 nt lds\n"
    "v_mfma_f32_16x16x32_bf16 v[144:147], a[20:23], v[0:3], v[144:147]\n"
    "s_add_u32 m0, s44, 21504\n"
    "s_add_u32 s43, s41, 21504\n"
    "v_mfma_f32_16x16x32_bf16 v[160:163], a[24:27], v[0:3], v[160:163]\n"
    "buffer_load_dwordx4 v229, s[16:19], s43 offen offset:1024 nt lds\n"
    "v_mfma_f32_16x16x32_bf16 v[176:179], a[28:31], v[0:3], v[176:179]\n"
    "s_mov_b32 m0, 159744\n"
    "s_mov_b32 s51, L_ph31-L_code_start\n"
    "v_mfma_f32_16x16x32_bf16 a[192:195], a[32:35], v[0:3], a[192:195]\n"
    "buffer_load_dword v249, s[52:55], s51 offen lds\n"
    "v_mfma_f32_16x16x32_bf16 a[208:211], a[36:39], v[0:3], a[208:211]\n"
    "ds_read_b128 a[56:59], v216 offset:24704\n"
    "s_add_u32 m0, s37, 24576\n"
    "v_mfma_f32_16x16x32_bf16 a[224:227], a[40:43], v[0:3], a[224:227]\n"
    "ds_read_b128 a[60:63], v217 offset:24704\n"
    "s_add_u32 s42, s38, 184320\n"
    "v_mfma_f32_16x16x32_bf16 a[240:243], a[44:47], v[0:3], a[240:243]\n"
    "ds_read_b128 a[64:67], v216 offset:24832\n"
    "v_cvt_pk_bf16_f32 v42, v44, v45\n"
    "v_mfma_f32_16x16x32_bf16 v[192:195], v[208:211], v[0:3], v[192:195]\n"
    "ds_read_b128 a[68:71], v217 offset:24832\n"
    "v_fma_f32 v48, v48, s36, v214\n"
    "v_mfma_f32_16x16x32_bf16 v[68:71], a[0:3], v[8:11], v[68:71]\n"
    "ds_read_b128 a[72:75], v216 offset:30720\n"
    "v_fma_f32 v49, v49, s36, v214\n"
    "v_mfma_f32_16x16x32_bf16 v[84:87], a[4:7], v[8:11], v[84:87]\n"
    "ds_read_b128 a[76:79], v217 offset:30720\n"
    "v_fma_f32 v50, v50, s36, v214\n"
    "v_mfma_f32_16x16x32_bf16 v[100:103], a[8:11], v[8:11], v[100:103]\n"
    "ds_read_b128 a[80:83], v216 offset:30848\n"
    "v_fma_f32 v51, v51, s36, v214\n"
    "v_mfma_f32_16x16x32_bf16 v[116:119], a[12:15], v[8:11], v[116:119]\n"
    "ds_read_b128 a[84:87], v217 offset:30848\n"
    "v_fma_f32 v52, v52, s36, v214\n"
    "v_mfma_f32_16x16x32_bf16 v[132:135], a[16:19], v[8:11], v[132:135]\n"
    "ds_read_b128 a[88:91], v216 offset:30976\n"
    "v_fma_f32 v53, v53, s36, v214\n"
    "v_mfma_f32_16x16x32_bf16 v[148:151], a[20:23], v[8:11], v[148:151]\n"
    "ds_read_b128 a[92:95], v217 offset:30976\n"
    "v_fma_f32 v54, v54, s36, v214\n"
    "v_mfma_f32_16x16x32_bf16 v[164:167], a[24:27], v[8:11], v[164:167]\n"
    "v_exp_f32 v46, v46\n"
    "v_mfma_f32_16x16x32_bf16 v[180:183], a[28:31], v[8:11], v[180:183]\n"
    "v_exp_f32 v47, v47\n"
    "v_mfma_f32_16x16x32_bf16 a[196:199], a[32:35], v[8:11], a[196:199]\n"
    "v_cvt_pk_bf16_f32 v43, v46, v47\n"
    "v_fma_f32 v55, v55, s36, v214\n"
    "v_mfma_f32_16x16x32_bf16 a[212:215], a[36:39], v[8:11], a[212:215]\n"
    "v_exp_f32 v48, v48\n"
    "v_mfma_f32_16x16x32_bf16 a[228:231], a[40:43], v[8:11], a[228:231]\n"
    "v_exp_f32 v49, v49\n"
    "v_mfma_f32_16x16x32_bf16 a[244:247], a[44:47], v[8:11], a[244:247]\n"
    "v_exp_f32 v50, v50\n"
    "v_mfma_f32_16x16x32_bf16 v[196:199], v[208:211], v[8:11], v[196:199]\n"
    "v_exp_f32 v51, v51\n"
    "v_mfma_f32_16x16x32_bf16 v[72:75], a[0:3], v[16:19], v[72:75]\n"
    "v_exp_f32 v52, v52\n"
    "v_mfma_f32_16x16x32_bf16 v[88:91], a[4:7], v[16:19], v[88:91]\n"
    "v_exp_f32 v53, v53\n"
    "v_mfma_f32_16x16x32_bf16 v[104:107], a[8:11], v[16:19], v[104:107]\n"
    "v_exp_f32 v54, v54\n"
    "v_mfma_f32_16x16x32_bf16 v[120:123], a[12:15], v[16:19], v[120:123]\n"
    "v_exp_f32 v55, v55\n"
    "v_mfma_f32_16x16x32_bf16 v[136:139], a[16:19], v[16:19], v[136:139]\n"
    "v_cvt_pk_bf16_f32 v48, v48, v49\n"
    "v_cvt_pk_bf16_f32 v49, v50, v51\n"
    "v_mfma_f32_16x16x32_bf16 v[152:155], a[20:23], v[16:19], v[152:155]\n"
    "v_cvt_pk_bf16_f32 v50, v52, v53\n"
    "v_cvt_pk_bf16_f32 v51, v54, v55\n"
    "v_mfma_f32_16x16x32_bf16 v[168:171], a[24:27], v[16:19], v[168:171]\n"
    "v_fma_f32 v56, v56, s36, v215\n"
    "v_fma_f32 v57, v57, s36, v215\n"
    "v_mfma_f32_16x16x32_bf16 v[184:187], a[28:31], v[16:19], v[184:187]\n"
    "v_exp_f32 v56, v56\n"
    "v_mfma_f32_16x16x32_bf16 a[200:203], a[32:35], v[16:19], a[200:203]\n"
    "v_exp_f32 v57, v57\n"
    "v_mfma_f32_16x16x32_bf16 a[216:219], a[36:39], v[16:19], a[216:219]\n"
    "v_fma_f32 v58, v58, s36, v215\n"
    "v_fma_f32 v59, v59, s36, v215\n"
    "v_mfma_f32_16x16x32_bf16 a[232:235], a[40:43], v[16:19], a[232:235]\n"
    "v_exp_f32 v58, v58\n"
    "v_mfma_f32_16x16x32_bf16 a[248:251], a[44:47], v[16:19], a[248:251]\n"
    "v_exp_f32 v59, v59\n"
    "v_mfma_f32_16x16x32_bf16 v[200:203], v[208:211], v[16:19], v[200:203]\n"
    "v_fma_f32 v60, v60, s36, v215\n"
    "v_fma_f32 v61, v61, s36, v215\n"
    "v_mfma_f32_16x16x32_bf16 v[76:79], a[0:3], v[24:27], v[76:79]\n"
    "v_exp_f32 v60, v60\n"
    "v_mfma_f32_16x16x32_bf16 v[92:95], a[4:7], v[24:27], v[92:95]\n"
    "v_exp_f32 v61, v61\n"
    "v_mfma_f32_16x16x32_bf16 v[108:111], a[8:11], v[24:27], v[108:111]\n"
    "v_fma_f32 v62, v62, s36, v215\n"
    "v_fma_f32 v63, v63, s36, v215\n"
    "v_mfma_f32_16x16x32_bf16 v[124:127], a[12:15], v[24:27], v[124:127]\n"
    "v_exp_f32 v62, v62\n"
    "v_mfma_f32_16x16x32_bf16 v[140:143], a[16:19], v[24:27], v[140:143]\n"
    "v_exp_f32 v63, v63\n"
    "v_mfma_f32_16x16x32_bf16 v[156:159], a[20:23], v[24:27], v[156:159]\n"
    "v_cvt_pk_bf16_f32 v56, v56, v57\n"
    "v_cvt_pk_bf16_f32 v57, v58, v59\n"
    "v_mfma_f32_16x16x32_bf16 v[172:175], a[24:27], v[24:27], v[172:175]\n"
    "v_cvt_pk_bf16_f32 v58, v60, v61\n"
    "v_cvt_pk_bf16_f32 v59, v62, v63\n"
    "v_mfma_f32_16x16x32_bf16 v[188:191], a[28:31], v[24:27], v[188:191]\n"
    "v_mfma_f32_16x16x32_bf16 a[204:207], a[32:35], v[24:27], a[204:207]\n"
    "v_mfma_f32_16x16x32_bf16 a[220:223], a[36:39], v[24:27], a[220:223]\n"
    "v_mfma_f32_16x16x32_bf16 a[236:239], a[40:43], v[24:27], a[236:239]\n"
    "v_mfma_f32_16x16x32_bf16 a[252:255], a[44:47], v[24:27], a[252:255]\n"
    "v_mfma_f32_16x16x32_bf16 v[204:207], v[208:211], v[24:27], v[204:207]\n"
    "s_waitcnt vmcnt(14) lgkmcnt(0)\n"
    "s_barrier\n"
    "L_ph28:\n"
    "v_mfma_f32_16x16x32_bf16 v[0:3], a[48:51], a[96:99], 0\n"
    "buffer_load_dwordx4 v231, s[24:27], s42 offen nt lds\n"
    "v_mfma_f32_16x16x32_bf16 v[0:3], a[52:55], a[100:103], v[0:3]\n"
    "ds_read_b64_tr_b16 a[0:1], v218 offset:36864\n"
    "v_mfma_f32_16x16x32_bf16 v[0:3], a[56:59], a[104:107], v[0:3]\n"
    "buffer_load_dwordx4 v232, s[24:27], s42 offen offset:1024 nt lds\n"
    "v_mfma_f32_16x16x32_bf16 v[0:3], a[60:63], a[108:111], v[0:3]\n"
    "ds_read_b64_tr_b16 a[2:3], v218 offset:43008\n"
    "v_mfma_f32_16x16x32_bf16 v[0:3], a[64:67], a[112:115], v[0:3]\n"
    "buffer_load_dwordx4 v233, s[24:27], s42 offen offset:2048 nt lds\n"
    "v_mfma_f32_16x16x32_bf16 v[0:3], a[68:71], a[116:119], v[0:3]\n"
    "s_add_u32 m0, s44, 21504\n"
    "s_add_u32 s43, s41, 21504\n"
    "v_mfma_f32_16x16x32_bf16 v[4:7], a[72:75], a[96:99], 0\n"
    "buffer_load_dwordx4 v230, s[16:19], s43 offen offset:2048 nt lds\n"
    "v_mfma_f32_16x16x32_bf16 v[4:7], a[76:79], a[100:103], v[4:7]\n"
    "ds_read_b64_tr_b16 a[4:5], v219 offset:36864\n"
    "s_add_u32 m0, s37, 36864\n"
    "v_mfma_f32_16x16x32_bf16 v[4:7], a[80:83], a[104:107], v[4:7]\n"
    "ds_read_b64_tr_b16 a[6:7], v219 offset:43008\n"
    "s_add_u32 s42, s39, 12288\n"
    "v_mfma_f32_16x16x32_bf16 v[4:7], a[84:87], a[108:111], v[4:7]\n"
    "ds_read_b64_tr_b16 a[8:9], v220 offset:36864\n"
    "v_mfma_f32_16x16x32_bf16 v[4:7], a[88:91], a[112:115], v[4:7]\n"
    "ds_read_b64_tr_b16 a[10:11], v220 offset:43008\n"
    "v_mfma_f32_16x16x32_bf16 v[4:7], a[92:95], a[116:119], v[4:7]\n"
    "ds_read_b64_tr_b16 a[12:13], v221 offset:36864\n"
    "v_mfma_f32_16x16x32_bf16 v[8:11], a[48:51], a[120:123], 0\n"
    "ds_read_b64_tr_b16 a[14:15], v221 offset:43008\n"
    "v_mfma_f32_16x16x32_bf16 v[8:11], a[52:55], a[124:127], v[8:11]\n"
    "ds_read_b64_tr_b16 a[16:17], v218 offset:36992\n"
    "v_fma_f32 v0, v0, s36, v212\n"
    "v_mfma_f32_16x16x32_bf16 v[8:11], a[56:59], a[128:131], v[8:11]\n"
    "ds_read_b64_tr_b16 a[18:19], v218 offset:43136\n"
    "v_fma_f32 v1, v1, s36, v212\n"
    "v_mfma_f32_16x16x32_bf16 v[8:11], a[60:63], a[132:135], v[8:11]\n"
    "ds_read_b64_tr_b16 a[20:21], v219 offset:36992\n"
    "v_fma_f32 v2, v2, s36, v212\n"
    "v_mfma_f32_16x16x32_bf16 v[8:11], a[64:67], a[136:139], v[8:11]\n"
    "ds_read_b64_tr_b16 a[22:23], v219 offset:43136\n"
    "v_fma_f32 v3, v3, s36, v212\n"
    "v_mfma_f32_16x16x32_bf16 v[8:11], a[68:71], a[140:143], v[8:11]\n"
    "ds_read_b64_tr_b16 a[24:25], v220 offset:36992\n"
    "v_mfma_f32_16x16x32_bf16 v[12:15], a[72:75], a[120:123], 0\n"
    "ds_read_b64_tr_b16 a[26:27], v220 offset:43136\n"
    "v_mfma_f32_16x16x32_bf16 v[12:15], a[76:79], a[124:127], v[12:15]\n"
    "ds_read_b64_tr_b16 a[28:29], v221 offset:36992\n"
    "v_fma_f32 v4, v4, s36, v212\n"
    "v_mfma_f32_16x16x32_bf16 v[12:15], a[80:83], a[128:131], v[12:15]\n"
    "ds_read_b64_tr_b16 a[30:31], v221 offset:43136\n"
    "v_fma_f32 v5, v5, s36, v212\n"
    "v_mfma_f32_16x16x32_bf16 v[12:15], a[84:87], a[132:135], v[12:15]\n"
    "ds_read_b64_tr_b16 a[32:33], v218 offset:37120\n"
    "v_fma_f32 v6, v6, s36, v212\n"
    "v_mfma_f32_16x16x32_bf16 v[12:15], a[88:91], a[136:139], v[12:15]\n"
    "ds_read_b64_tr_b16 a[34:35], v218 offset:43264\n"
    "v_fma_f32 v7, v7, s36, v212\n"
    "v_mfma_f32_16x16x32_bf16 v[12:15], a[92:95], a[140:143], v[12:15]\n"
    "ds_read_b64_tr_b16 a[36:37], v219 offset:37120\n"
    "v_mfma_f32_16x16x32_bf16 v[16:19], a[48:51], a[144:147], 0\n"
    "ds_read_b64_tr_b16 a[38:39], v219 offset:43264\n"
    "v_mfma_f32_16x16x32_bf16 v[16:19], a[52:55], a[148:151], v[16:19]\n"
    "ds_read_b64_tr_b16 a[40:41], v220 offset:37120\n"
    "v_fma_f32 v8, v8, s36, v213\n"
    "v_mfma_f32_16x16x32_bf16 v[16:19], a[56:59], a[152:155], v[16:19]\n"
    "ds_read_b64_tr_b16 a[42:43], v220 offset:43264\n"
    "v_fma_f32 v9, v9, s36, v213\n"
    "v_mfma_f32_16x16x32_bf16 v[16:19], a[60:63], a[156:159], v[16:19]\n"
    "ds_read_b64_tr_b16 a[44:45], v221 offset:37120\n"
    "v_fma_f32 v10, v10, s36, v213\n"
    "v_mfma_f32_16x16x32_bf16 v[16:19], a[64:67], a[160:163], v[16:19]\n"
    "ds_read_b64_tr_b16 a[46:47], v221 offset:43264\n"
    "v_fma_f32 v11, v11, s36, v213\n"
    "v_mfma_f32_16x16x32_bf16 v[16:19], a[68:71], a[164:167], v[16:19]\n"
    "v_exp_f32 v0, v0\n"
    "v_mfma_f32_16x16x32_bf16 v[20:23], a[72:75], a[144:147], 0\n"
    "v_exp_f32 v1, v1\n"
    "v_mfma_f32_16x16x32_bf16 v[20:23], a[76:79], a[148:151], v[20:23]\n"
    "v_exp_f32 v2, v2\n"
    "v_mfma_f32_16x16x32_bf16 v[20:23], a[80:83], a[152:155], v[20:23]\n"
    "v_exp_f32 v3, v3\n"
    "v_mfma_f32_16x16x32_bf16 v[20:23], a[84:87], a[156:159], v[20:23]\n"
    "v_exp_f32 v4, v4\n"
    "v_mfma_f32_16x16x32_bf16 v[20:23], a[88:91], a[160:163], v[20:23]\n"
    "v_exp_f32 v5, v5\n"
    "v_mfma_f32_16x16x32_bf16 v[20:23], a[92:95], a[164:167], v[20:23]\n"
    "v_exp_f32 v6, v6\n"
    "v_mfma_f32_16x16x32_bf16 v[24:27], a[48:51], a[168:171], 0\n"
    "v_exp_f32 v7, v7\n"
    "v_mfma_f32_16x16x32_bf16 v[24:27], a[52:55], a[172:175], v[24:27]\n"
    "v_cvt_pk_bf16_f32 v0, v0, v1\n"
    "v_cvt_pk_bf16_f32 v1, v2, v3\n"
    "v_mfma_f32_16x16x32_bf16 v[24:27], a[56:59], a[176:179], v[24:27]\n"
    "v_cvt_pk_bf16_f32 v2, v4, v5\n"
    "v_cvt_pk_bf16_f32 v3, v6, v7\n"
    "v_mfma_f32_16x16x32_bf16 v[24:27], a[60:63], a[180:183], v[24:27]\n"
    "v_exp_f32 v8, v8\n"
    "v_mfma_f32_16x16x32_bf16 v[24:27], a[64:67], a[184:187], v[24:27]\n"
    "v_exp_f32 v9, v9\n"
    "v_mfma_f32_16x16x32_bf16 v[24:27], a[68:71], a[188:191], v[24:27]\n"
    "v_exp_f32 v10, v10\n"
    "v_mfma_f32_16x16x32_bf16 v[28:31], a[72:75], a[168:171], 0\n"
    "v_exp_f32 v11, v11\n"
    "v_mfma_f32_16x16x32_bf16 v[28:31], a[76:79], a[172:175], v[28:31]\n"
    "v_fma_f32 v12, v12, s36, v213\n"
    "v_fma_f32 v13, v13, s36, v213\n"
    "v_mfma_f32_16x16x32_bf16 v[28:31], a[80:83], a[176:179], v[28:31]\n"
    "v_exp_f32 v12, v12\n"
    "v_mfma_f32_16x16x32_bf16 v[28:31], a[84:87], a[180:183], v[28:31]\n"
    "v_exp_f32 v13, v13\n"
    "v_mfma_f32_16x16x32_bf16 v[28:31], a[88:91], a[184:187], v[28:31]\n"
    "v_fma_f32 v14, v14, s36, v213\n"
    "v_fma_f32 v15, v15, s36, v213\n"
    "v_mfma_f32_16x16x32_bf16 v[28:31], a[92:95], a[188:191], v[28:31]\n"
    "s_waitcnt vmcnt(14) lgkmcnt(0)\n"
    "s_barrier\n"
    "L_ph29:\n"
    "v_mfma_f32_16x16x32_bf16 v[64:67], a[0:3], v[32:35], v[64:67]\n"
    "buffer_load_dwordx4 v222, s[20:23], s42 offen nt lds\n"
    "v_mfma_f32_16x16x32_bf16 v[80:83], a[4:7], v[32:35], v[80:83]\n"
    "ds_read_b128 a[48:51], v216 offset:49152\n"
    "v_cvt_pk_bf16_f32 v8, v8, v9\n"
    "v_mfma_f32_16x16x32_bf16 v[96:99], a[8:11], v[32:35], v[96:99]\n"
    "buffer_load_dwordx4 v223, s[20:23], s42 offen offset:1024 nt lds\n"
    "v_mfma_f32_16x16x32_bf16 v[112:115], a[12:15], v[32:35], v[112:115]\n"
    "ds_read_b128 a[52:55], v217 offset:49152\n"
    "v_cvt_pk_bf16_f32 v9, v10, v11\n"
    "v_mfma_f32_16x16x32_bf16 v[128:131], a[16:19], v[32:35], v[128:131]\n"
    "buffer_load_dwordx4 v224, s[20:23], s42 offen offset:2048 nt lds\n"
    "v_mfma_f32_16x16x32_bf16 v[144:147], a[20:23], v[32:35], v[144:147]\n"
    "s_mov_b32 m0, 159744\n"
    "s_mov_b32 s51, L_ph33+2304-L_code_start\n"
    "v_mfma_f32_16x16x32_bf16 v[160:163], a[24:27], v[32:35], v[160:163]\n"
    "buffer_load_dword v249, s[52:55], s51 offen lds\n"
    "v_mfma_f32_16x16x32_bf16 v[176:179], a[28:31], v[32:35], v[176:179]\n"
    "ds_read_b128 a[56:59], v216 offset:49280\n"
    "s_add_u32 m0, s37, 49152\n"
    "v_mfma_f32_16x16x32_bf16 a[192:195], a[32:35], v[32:35], a[192:195]\n"
    "ds_read_b128 a[60:63], v217 offset:49280\n"
    "s_add_u32 s42, s39, 0\n"
    "v_mfma_f32_16x16x32_bf16 a[208:211], a[36:39], v[32:35], a[208:211]\n"
    "ds_read_b128 a[64:67], v216 offset:49408\n"
    "v_cvt_pk_bf16_f32 v10, v12, v13\n"
    "v_mfma_f32_16x16x32_bf16 a[224:227], a[40:43], v[32:35], a[224:227]\n"
    "ds_read_b128 a[68:71], v217 offset:49408\n"
    "v_fma_f32 v16, v16, s36, v214\n"
    "v_mfma_f32_16x16x32_bf16 a[240:243], a[44:47], v[32:35], a[240:243]\n"
    "ds_read_b128 a[72:75], v216 offset:55296\n"
    "v_fma_f32 v17, v17, s36, v214\n"
    "v_mfma_f32_16x16x32_bf16 v[192:195], v[208:211], v[32:35], v[192:195]\n"
    "ds_read_b128 a[76:79], v217 offset:55296\n"
    "v_fma_f32 v18, v18, s36, v214\n"
    "v_mfma_f32_16x16x32_bf16 v[68:71], a[0:3], v[40:43], v[68:71]\n"
    "ds_read_b128 a[80:83], v216 offset:55424\n"
    "v_fma_f32 v19, v19, s36, v214\n"
    "v_mfma_f32_16x16x32_bf16 v[84:87], a[4:7], v[40:43], v[84:87]\n"
    "ds_read_b128 a[84:87], v217 offset:55424\n"
    "v_fma_f32 v20, v20, s36, v214\n"
    "v_mfma_f32_16x16x32_bf16 v[100:103], a[8:11], v[40:43], v[100:103]\n"
    "ds_read_b128 a[88:91], v216 offset:55552\n"
    "v_fma_f32 v21, v21, s36, v214\n"
    "v_mfma_f32_16x16x32_bf16 v[116:119], a[12:15], v[40:43], v[116:119]\n"
    "ds_read_b128 a[92:95], v217 offset:55552\n"
    "v_fma_f32 v22, v22, s36, v214\n"
    "v_mfma_f32_16x16x32_bf16 v[132:135], a[16:19], v[40:43], v[132:135]\n"
    "v_exp_f32 v14, v14\n"
    "v_mfma_f32_16x16x32_bf16 v[148:151], a[20:23], v[40:43], v[148:151]\n"
    "v_exp_f32 v15, v15\n"
    "v_mfma_f32_16x16x32_bf16 v[164:167], a[24:27], v[40:43], v[164:167]\n"
    "v_cvt_pk_bf16_f32 v11, v14, v15\n"
    "v_fma_f32 v23, v23, s36, v214\n"
    "v_mfma_f32_16x16x32_bf16 v[180:183], a[28:31], v[40:43], v[180:183]\n"
    "v_exp_f32 v16, v16\n"
    "v_mfma_f32_16x16x32_bf16 a[196:199], a[32:35], v[40:43], a[196:199]\n"
    "v_exp_f32 v17, v17\n"
    "v_mfma_f32_16x16x32_bf16 a[212:215], a[36:39], v[40:43], a[212:215]\n"
    "v_exp_f32 v18, v18\n"
    "v_mfma_f32_16x16x32_bf16 a[228:231], a[40:43], v[40:43], a[228:231]\n"
    "v_exp_f32 v19, v19\n"
    "v_mfma_f32_16x16x32_bf16 a[244:247], a[44:47], v[40:43], a[244:247]\n"
    "v_exp_f32 v20, v20\n"
    "v_mfma_f32_16x16x32_bf16 v[196:199], v[208:211], v[40:43], v[196:199]\n"
    "v_exp_f32 v21, v21\n"
    "v_mfma_f32_16x16x32_bf16 v[72:75], a[0:3], v[48:51], v[72:75]\n"
    "v_exp_f32 v22, v22\n"
    "v_mfma_f32_16x16x32_bf16 v[88:91], a[4:7], v[48:51], v[88:91]\n"
    "v_exp_f32 v23, v23\n"
    "v_mfma_f32_16x16x32_bf16 v[104:107], a[8:11], v[48:51], v[104:107]\n"
    "v_cvt_pk_bf16_f32 v16, v16, v17\n"
    "v_cvt_pk_bf16_f32 v17, v18, v19\n"
    "v_mfma_f32_16x16x32_bf16 v[120:123], a[12:15], v[48:51], v[120:123]\n"
    "v_cvt_pk_bf16_f32 v18, v20, v21\n"
    "v_cvt_pk_bf16_f32 v19, v22, v23\n"
    "v_mfma_f32_16x16x32_bf16 v[136:139], a[16:19], v[48:51], v[136:139]\n"
    "v_fma_f32 v24, v24, s36, v215\n"
    "v_fma_f32 v25, v25, s36, v215\n"
    "v_mfma_f32_16x16x32_bf16 v[152:155], a[20:23], v[48:51], v[152:155]\n"
    "v_exp_f32 v24, v24\n"
    "v_mfma_f32_16x16x32_bf16 v[168:171], a[24:27], v[48:51], v[168:171]\n"
    "v_exp_f32 v25, v25\n"
    "v_mfma_f32_16x16x32_bf16 v[184:187], a[28:31], v[48:51], v[184:187]\n"
    "v_fma_f32 v26, v26, s36, v215\n"
    "v_fma_f32 v27, v27, s36, v215\n"
    "v_mfma_f32_16x16x32_bf16 a[200:203], a[32:35], v[48:51], a[200:203]\n"
    "v_exp_f32 v26, v26\n"
    "v_mfma_f32_16x16x32_bf16 a[216:219], a[36:39], v[48:51], a[216:219]\n"
    "v_exp_f32 v27, v27\n"
    "v_mfma_f32_16x16x32_bf16 a[232:235], a[40:43], v[48:51], a[232:235]\n"
    "v_fma_f32 v28, v28, s36, v215\n"
    "v_fma_f32 v29, v29, s36, v215\n"
    "v_mfma_f32_16x16x32_bf16 a[248:251], a[44:47], v[48:51], a[248:251]\n"
    "v_exp_f32 v28, v28\n"
    "v_mfma_f32_16x16x32_bf16 v[200:203], v[208:211], v[48:51], v[200:203]\n"
    "v_exp_f32 v29, v29\n"
    "v_mfma_f32_16x16x32_bf16 v[76:79], a[0:3], v[56:59], v[76:79]\n"
    "v_fma_f32 v30, v30, s36, v215\n"
    "v_fma_f32 v31, v31, s36, v215\n"
    "v_mfma_f32_16x16x32_bf16 v[92:95], a[4:7], v[56:59], v[92:95]\n"
    "v_exp_f32 v30, v30\n"
    "v_mfma_f32_16x16x32_bf16 v[108:111], a[8:11], v[56:59], v[108:111]\n"
    "v_exp_f32 v31, v31\n"
    "v_mfma_f32_16x16x32_bf16 v[124:127], a[12:15], v[56:59], v[124:127]\n"
    "v_cvt_pk_bf16_f32 v24, v24, v25\n"
    "v_cvt_pk_bf16_f32 v25, v26, v27\n"
    "v_mfma_f32_16x16x32_bf16 v[140:143], a[16:19], v[56:59], v[140:143]\n"
    "v_cvt_pk_bf16_f32 v26, v28, v29\n"
    "v_cvt_pk_bf16_f32 v27, v30, v31\n"
    "v_mfma_f32_16x16x32_bf16 v[156:159], a[20:23], v[56:59], v[156:159]\n"
    "v_mfma_f32_16x16x32_bf16 v[172:175], a[24:27], v[56:59], v[172:175]\n"
    "v_mfma_f32_16x16x32_bf16 v[188:191], a[28:31], v[56:59], v[188:191]\n"
    "v_mfma_f32_16x16x32_bf16 a[204:207], a[32:35], v[56:59], a[204:207]\n"
    "v_mfma_f32_16x16x32_bf16 a[220:223], a[36:39], v[56:59], a[220:223]\n"
    "v_mfma_f32_16x16x32_bf16 a[236:239], a[40:43], v[56:59], a[236:239]\n"
    "v_mfma_f32_16x16x32_bf16 a[252:255], a[44:47], v[56:59], a[252:255]\n"
    "v_mfma_f32_16x16x32_bf16 v[204:207], v[208:211], v[56:59], v[204:207]\n"
    "s_waitcnt vmcnt(14) lgkmcnt(0)\n"
    "s_barrier\n"
    "L_ph30:\n"
    "v_mfma_f32_16x16x32_bf16 v[32:35], a[48:51], a[96:99], 0\n"
    "buffer_load_dwordx4 v231, s[24:27], s42 offen nt lds\n"
    "v_mfma_f32_16x16x32_bf16 v[32:35], a[52:55], a[100:103], v[32:35]\n"
    "ds_read_b64_tr_b16 a[0:1], v218 offset:0\n"
    "v_mfma_f32_16x16x32_bf16 v[32:35], a[56:59], a[104:107], v[32:35]\n"
    "buffer_load_dwordx4 v232, s[24:27], s42 offen offset:1024 nt lds\n"
    "v_mfma_f32_16x16x32_bf16 v[32:35], a[60:63], a[108:111], v[32:35]\n"
    "ds_read_b64_tr_b16 a[2:3], v218 offset:6144\n"
    "v_mfma_f32_16x16x32_bf16 v[32:35], a[64:67], a[112:115], v[32:35]\n"
    "buffer_load_dwordx4 v233, s[24:27], s42 offen offset:2048 nt lds\n"
    "v_mfma_f32_16x16x32_bf16 v[32:35], a[68:71], a[116:119], v[32:35]\n"
    "ds_read_b64_tr_b16 a[4:5], v219 offset:0\n"
    "s_add_u32 m0, s37, 0\n"
    "v_mfma_f32_16x16x32_bf16 v[36:39], a[72:75], a[96:99], 0\n"
    "ds_read_b64_tr_b16 a[6:7], v219 offset:6144\n"
    "s_add_u32 s42, s39, 24576\n"
    "v_mfma_f32_16x16x32_bf16 v[36:39], a[76:79], a[100:103], v[36:39]\n"
    "ds_read_b64_tr_b16 a[8:9], v220 offset:0\n"
    "v_mfma_f32_16x16x32_bf16 v[36:39], a[80:83], a[104:107], v[36:39]\n"
    "ds_read_b64_tr_b16 a[10:11], v220 offset:6144\n"
    "v_mfma_f32_16x16x32_bf16 v[36:39], a[84:87], a[108:111], v[36:39]\n"
    "ds_read_b64_tr_b16 a[12:13], v221 offset:0\n"
    "v_mfma_f32_16x16x32_bf16 v[36:39], a[88:91], a[112:115], v[36:39]\n"
    "ds_read_b64_tr_b16 a[14:15], v221 offset:6144\n"
    "v_mfma_f32_16x16x32_bf16 v[36:39], a[92:95], a[116:119], v[36:39]\n"
    "ds_read_b64_tr_b16 a[16:17], v218 offset:128\n"
    "v_mfma_f32_16x16x32_bf16 v[40:43], a[48:51], a[120:123], 0\n"
    "ds_read_b64_tr_b16 a[18:19], v218 offset:6272\n"
    "v_mfma_f32_16x16x32_bf16 v[40:43], a[52:55], a[124:127], v[40:43]\n"
    "ds_read_b64_tr_b16 a[20:21], v219 offset:128\n"
    "v_fma_f32 v32, v32, s36, v212\n"
    "v_mfma_f32_16x16x32_bf16 v[40:43], a[56:59], a[128:131], v[40:43]\n"
    "ds_read_b64_tr_b16 a[22:23], v219 offset:6272\n"
    "v_fma_f32 v33, v33, s36, v212\n"
    "v_mfma_f32_16x16x32_bf16 v[40:43], a[60:63], a[132:135], v[40:43]\n"
    "ds_read_b64_tr_b16 a[24:25], v220 offset:128\n"
    "v_fma_f32 v34, v34, s36, v212\n"
    "v_mfma_f32_16x16x32_bf16 v[40:43], a[64:67], a[136:139], v[40:43]\n"
    "ds_read_b64_tr_b16 a[26:27], v220 offset:6272\n"
    "v_fma_f32 v35, v35, s36, v212\n"
    "v_mfma_f32_16x16x32_bf16 v[40:43], a[68:71], a[140:143], v[40:43]\n"
    "ds_read_b64_tr_b16 a[28:29], v221 offset:128\n"
    "v_mfma_f32_16x16x32_bf16 v[44:47], a[72:75], a[120:123], 0\n"
    "ds_read_b64_tr_b16 a[30:31], v221 offset:6272\n"
    "v_mfma_f32_16x16x32_bf16 v[44:47], a[76:79], a[124:127], v[44:47]\n"
    "ds_read_b64_tr_b16 a[32:33], v218 offset:256\n"
    "v_fma_f32 v36, v36, s36, v212\n"
    "v_mfma_f32_16x16x32_bf16 v[44:47], a[80:83], a[128:131], v[44:47]\n"
    "ds_read_b64_tr_b16 a[34:35], v218 offset:6400\n"
    "v_fma_f32 v37, v37, s36, v212\n"
    "v_mfma_f32_16x16x32_bf16 v[44:47], a[84:87], a[132:135], v[44:47]\n"
    "ds_read_b64_tr_b16 a[36:37], v219 offset:256\n"
    "v_fma_f32 v38, v38, s36, v212\n"
    "v_mfma_f32_16x16x32_bf16 v[44:47], a[88:91], a[136:139], v[44:47]\n"
    "ds_read_b64_tr_b16 a[38:39], v219 offset:6400\n"
    "v_fma_f32 v39, v39, s36, v212\n"
    "v_mfma_f32_16x16x32_bf16 v[44:47], a[92:95], a[140:143], v[44:47]\n"
    "ds_read_b64_tr_b16 a[40:41], v220 offset:256\n"
    "v_mfma_f32_16x16x32_bf16 v[48:51], a[48:51], a[144:147], 0\n"
    "ds_read_b64_tr_b16 a[42:43], v220 offset:6400\n"
    "v_mfma_f32_16x16x32_bf16 v[48:51], a[52:55], a[148:151], v[48:51]\n"
    "ds_read_b64_tr_b16 a[44:45], v221 offset:256\n"
    "v_fma_f32 v40, v40, s36, v213\n"
    "v_mfma_f32_16x16x32_bf16 v[48:51], a[56:59], a[152:155], v[48:51]\n"
    "ds_read_b64_tr_b16 a[46:47], v221 offset:6400\n"
    "v_fma_f32 v41, v41, s36, v213\n"
    "v_mfma_f32_16x16x32_bf16 v[48:51], a[60:63], a[156:159], v[48:51]\n"
    "v_exp_f32 v32, v32\n"
    "v_mfma_f32_16x16x32_bf16 v[48:51], a[64:67], a[160:163], v[48:51]\n"
    "v_exp_f32 v33, v33\n"
    "v_mfma_f32_16x16x32_bf16 v[48:51], a[68:71], a[164:167], v[48:51]\n"
    "v_exp_f32 v34, v34\n"
    "v_mfma_f32_16x16x32_bf16 v[52:55], a[72:75], a[144:147], 0\n"
    "v_exp_f32 v35, v35\n"
    "v_mfma_f32_16x16x32_bf16 v[52:55], a[76:79], a[148:151], v[52:55]\n"
    "v_exp_f32 v36, v36\n"
    "v_mfma_f32_16x16x32_bf16 v[52:55], a[80:83], a[152:155], v[52:55]\n"
    "v_exp_f32 v37, v37\n"
    "v_mfma_f32_16x16x32_bf16 v[52:55], a[84:87], a[156:159], v[52:55]\n"
    "v_exp_f32 v38, v38\n"
    "v_mfma_f32_16x16x32_bf16 v[52:55], a[88:91], a[160:163], v[52:55]\n"
    "v_exp_f32 v39, v39\n"
    "v_mfma_f32_16x16x32_bf16 v[52:55], a[92:95], a[164:167], v[52:55]\n"
    "v_cvt_pk_bf16_f32 v32, v32, v33\n"
    "v_cvt_pk_bf16_f32 v33, v34, v35\n"
    "v_mfma_f32_16x16x32_bf16 v[56:59], a[48:51], a[168:171], 0\n"
    "v_cvt_pk_bf16_f32 v34, v36, v37\n"
    "v_cvt_pk_bf16_f32 v35, v38, v39\n"
    "v_mfma_f32_16x16x32_bf16 v[56:59], a[52:55], a[172:175], v[56:59]\n"
    "v_exp_f32 v40, v40\n"
    "v_mfma_f32_16x16x32_bf16 v[56:59], a[56:59], a[176:179], v[56:59]\n"
    "v_exp_f32 v41, v41\n"
    "v_mfma_f32_16x16x32_bf16 v[56:59], a[60:63], a[180:183], v[56:59]\n"
    "v_fma_f32 v42, v42, s36, v213\n"
    "v_fma_f32 v43, v43, s36, v213\n"
    "v_mfma_f32_16x16x32_bf16 v[56:59], a[64:67], a[184:187], v[56:59]\n"
    "v_exp_f32 v42, v42\n"
    "v_mfma_f32_16x16x32_bf16 v[56:59], a[68:71], a[188:191], v[56:59]\n"
    "v_exp_f32 v43, v43\n"
    "v_mfma_f32_16x16x32_bf16 v[60:63], a[72:75], a[168:171], 0\n"
    "v_fma_f32 v44, v44, s36, v213\n"
    "v_fma_f32 v45, v45, s36, v213\n"
    "v_mfma_f32_16x16x32_bf16 v[60:63], a[76:79], a[172:175], v[60:63]\n"
    "v_exp_f32 v44, v44\n"
    "v_mfma_f32_16x16x32_bf16 v[60:63], a[80:83], a[176:179], v[60:63]\n"
    "v_exp_f32 v45, v45\n"
    "v_mfma_f32_16x16x32_bf16 v[60:63], a[84:87], a[180:183], v[60:63]\n"
    "v_fma_f32 v46, v46, s36, v213\n"
    "v_fma_f32 v47, v47, s36, v213\n"
    "v_mfma_f32_16x16x32_bf16 v[60:63], a[88:91], a[184:187], v[60:63]\n"
    "v_exp_f32 v46, v46\n"
    "v_mfma_f32_16x16x32_bf16 v[60:63], a[92:95], a[188:191], v[60:63]\n"
    "s_waitcnt vmcnt(7) lgkmcnt(0)\n"
    "s_barrier\n"
    "L_ph31:\n"
    "v_mfma_f32_16x16x32_bf16 v[64:67], a[0:3], v[0:3], v[64:67]\n"
    "buffer_load_dwordx4 v222, s[20:23], s42 offen nt lds\n"
    "v_mfma_f32_16x16x32_bf16 v[80:83], a[4:7], v[0:3], v[80:83]\n"
    "ds_read_b128 a[48:51], v216 offset:12288\n"
    "v_cvt_pk_bf16_f32 v40, v40, v41\n"
    "v_mfma_f32_16x16x32_bf16 v[96:99], a[8:11], v[0:3], v[96:99]\n"
    "buffer_load_dwordx4 v223, s[20:23], s42 offen offset:1024 nt lds\n"
    "v_mfma_f32_16x16x32_bf16 v[112:115], a[12:15], v[0:3], v[112:115]\n"
    "ds_read_b128 a[52:55], v217 offset:12288\n"
    "v_cvt_pk_bf16_f32 v41, v42, v43\n"
    "v_mfma_f32_16x16x32_bf16 v[128:131], a[16:19], v[0:3], v[128:131]\n"
    "buffer_load_dwordx4 v224, s[20:23], s42 offen offset:2048 nt lds\n"
    "v_mfma_f32_16x16x32_bf16 v[144:147], a[20:23], v[0:3], v[144:147]\n"
    "s_mov_b32 m0, 159744\n"
    "s_mov_b32 s51, L_ph35-L_code_start\n"
    "v_mfma_f32_16x16x32_bf16 v[160:163], a[24:27], v[0:3], v[160:163]\n"
    "buffer_load_dword v249, s[52:55], s51 offen lds\n"
    "v_mfma_f32_16x16x32_bf16 v[176:179], a[28:31], v[0:3], v[176:179]\n"
    "ds_read_b128 a[56:59], v216 offset:12416\n"
    "s_add_u32 m0, s37, 12288\n"
    "v_mfma_f32_16x16x32_bf16 a[192:195], a[32:35], v[0:3], a[192:195]\n"
    "ds_read_b128 a[60:63], v217 offset:12416\n"
    "s_add_u32 s42, s39, 12288\n"
    "v_mfma_f32_16x16x32_bf16 a[208:211], a[36:39], v[0:3], a[208:211]\n"
    "ds_read_b128 a[64:67], v216 offset:12544\n"
    "v_cvt_pk_bf16_f32 v42, v44, v45\n"
    "v_mfma_f32_16x16x32_bf16 a[224:227], a[40:43], v[0:3], a[224:227]\n"
    "ds_read_b128 a[68:71], v217 offset:12544\n"
    "v_fma_f32 v48, v48, s36, v214\n"
    "v_mfma_f32_16x16x32_bf16 a[240:243], a[44:47], v[0:3], a[240:243]\n"
    "ds_read_b128 a[72:75], v216 offset:18432\n"
    "v_fma_f32 v49, v49, s36, v214\n"
    "v_mfma_f32_16x16x32_bf16 v[192:195], v[208:211], v[0:3], v[192:195]\n"
    "ds_read_b128 a[76:79], v217 offset:18432\n"
    "v_fma_f32 v50, v50, s36, v214\n"
    "v_mfma_f32_16x16x32_bf16 v[68:71], a[0:3], v[8:11], v[68:71]\n"
    "ds_read_b128 a[80:83], v216 offset:18560\n"
    "v_fma_f32 v51, v51, s36, v214\n"
    "v_mfma_f32_16x16x32_bf16 v[84:87], a[4:7], v[8:11], v[84:87]\n"
    "ds_read_b128 a[84:87], v217 offset:18560\n"
    "v_fma_f32 v52, v52, s36, v214\n"
    "v_mfma_f32_16x16x32_bf16 v[100:103], a[8:11], v[8:11], v[100:103]\n"
    "ds_read_b128 a[88:91], v216 offset:18688\n"
    "v_fma_f32 v53, v53, s36, v214\n"
    "v_mfma_f32_16x16x32_bf16 v[116:119], a[12:15], v[8:11], v[116:119]\n"
    "ds_read_b128 a[92:95], v217 offset:18688\n"
    "v_fma_f32 v54, v54, s36, v214\n"
    "v_mfma_f32_16x16x32_bf16 v[132:135], a[16:19], v[8:11], v[132:135]\n"
    "ds_read_b128 a[96:99], v234 offset:0\n"
    "v_fma_f32 v55, v55, s36, v214\n"
    "v_mfma_f32_16x16x32_bf16 v[148:151], a[20:23], v[8:11], v[148:151]\n"
    "ds_read_b128 a[100:103], v235 offset:0\n"
    "v_fma_f32 v56, v56, s36, v215\n"
    "v_mfma_f32_16x16x32_bf16 v[164:167], a[24:27], v[8:11], v[164:167]\n"
    "ds_read_b128 a[104:107], v234 offset:128\n"
    "v_fma_f32 v57, v57, s36, v215\n"
    "v_mfma_f32_16x16x32_bf16 v[180:183], a[28:31], v[8:11], v[180:183]\n"
    "ds_read_b128 a[108:111], v235 offset:128\n"
    "v_fma_f32 v58, v58, s36, v215\n"
    "v_mfma_f32_16x16x32_bf16 a[196:199], a[32:35], v[8:11], a[196:199]\n"
    "ds_read_b128 a[112:115], v234 offset:256\n"
    "v_fma_f32 v59, v59, s36, v215\n"
    "v_mfma_f32_16x16x32_bf16 a[212:215], a[36:39], v[8:11], a[212:215]\n"
    "ds_read_b128 a[116:119], v235 offset:256\n"
    "v_fma_f32 v60, v60, s36, v215\n"
    "v_mfma_f32_16x16x32_bf16 a[228:231], a[40:43], v[8:11], a[228:231]\n"
    "ds_read_b128 a[120:123], v234 offset:6144\n"
    "v_fma_f32 v61, v61, s36, v215\n"
    "v_mfma_f32_16x16x32_bf16 a[244:247], a[44:47], v[8:11], a[244:247]\n"
    "ds_read_b128 a[124:127], v235 offset:6144\n"
    "v_fma_f32 v62, v62, s36, v215\n"
    "v_mfma_f32_16x16x32_bf16 v[196:199], v[208:211], v[8:11], v[196:199]\n"
    "ds_read_b128 a[128:131], v234 offset:6272\n"
    "v_fma_f32 v63, v63, s36, v215\n"
    "v_mfma_f32_16x16x32_bf16 v[72:75], a[0:3], v[16:19], v[72:75]\n"
    "ds_read_b128 a[132:135], v235 offset:6272\n"
    "v_mfma_f32_16x16x32_bf16 v[88:91], a[4:7], v[16:19], v[88:91]\n"
    "ds_read_b128 a[136:139], v234 offset:6400\n"
    "v_mfma_f32_16x16x32_bf16 v[104:107], a[8:11], v[16:19], v[104:107]\n"
    "ds_read_b128 a[140:143], v235 offset:6400\n"
    "v_mfma_f32_16x16x32_bf16 v[120:123], a[12:15], v[16:19], v[120:123]\n"
    "ds_read_b128 a[144:147], v234 offset:12288\n"
    "v_mfma_f32_16x16x32_bf16 v[136:139], a[16:19], v[16:19], v[136:139]\n"
    "ds_read_b128 a[148:151], v235 offset:12288\n"
    "v_mfma_f32_16x16x32_bf16 v[152:155], a[20:23], v[16:19], v[152:155]\n"
    "ds_read_b128 a[152:155], v234 offset:12416\n"
    "v_mfma_f32_16x16x32_bf16 v[168:171], a[24:27], v[16:19], v[168:171]\n"
    "ds_read_b128 a[156:159], v235 offset:12416\n"
    "v_mfma_f32_16x16x32_bf16 v[184:187], a[28:31], v[16:19], v[184:187]\n"
    "ds_read_b128 a[160:163], v234 offset:12544\n"
    "v_mfma_f32_16x16x32_bf16 a[200:203], a[32:35], v[16:19], a[200:203]\n"
    "ds_read_b128 a[164:167], v235 offset:12544\n"
    "v_mfma_f32_16x16x32_bf16 a[216:219], a[36:39], v[16:19], a[216:219]\n"
    "ds_read_b128 a[168:171], v234 offset:18432\n"
    "v_mfma_f32_16x16x32_bf16 a[232:235], a[40:43], v[16:19], a[232:235]\n"
    "ds_read_b128 a[172:175], v235 offset:18432\n"
    "v_mfma_f32_16x16x32_bf16 a[248:251], a[44:47], v[16:19], a[248:251]\n"
    "ds_read_b128 a[176:179], v234 offset:18560\n"
    "v_mfma_f32_16x16x32_bf16 v[200:203], v[208:211], v[16:19], v[200:203]\n"
    "ds_read_b128 a[180:183], v235 offset:18560\n"
    "v_mfma_f32_16x16x32_bf16 v[76:79], a[0:3], v[24:27], v[76:79]\n"
    "ds_read_b128 a[184:187], v234 offset:18688\n"
    "v_mfma_f32_16x16x32_bf16 v[92:95], a[4:7], v[24:27], v[92:95]\n"
    "ds_read_b128 a[188:191], v235 offset:18688\n"
    "v_mfma_f32_16x16x32_bf16 v[108:111], a[8:11], v[24:27], v[108:111]\n"
    "v_exp_f32 v47, v47\n"
    "v_mfma_f32_16x16x32_bf16 v[124:127], a[12:15], v[24:27], v[124:127]\n"
    "v_cvt_pk_bf16_f32 v43, v46, v47\n"
    "v_mfma_f32_16x16x32_bf16 v[140:143], a[16:19], v[24:27], v[140:143]\n"
    "v_exp_f32 v48, v48\n"
    "v_mfma_f32_16x16x32_bf16 v[156:159], a[20:23], v[24:27], v[156:159]\n"
    "v_exp_f32 v49, v49\n"
    "v_mfma_f32_16x16x32_bf16 v[172:175], a[24:27], v[24:27], v[172:175]\n"
    "v_exp_f32 v50, v50\n"
    "v_mfma_f32_16x16x32_bf16 v[188:191], a[28:31], v[24:27], v[188:191]\n"
    "v_exp_f32 v51, v51\n"
    "v_mfma_f32_16x16x32_bf16 a[204:207], a[32:35], v[24:27], a[204:207]\n"
    "v_exp_f32 v52, v52\n"
    "v_mfma_f32_16x16x32_bf16 a[220:223], a[36:39], v[24:27], a[220:223]\n"
    "v_exp_f32 v53, v53\n"
    "v_mfma_f32_16x16x32_bf16 a[236:239], a[40:43], v[24:27], a[236:239]\n"
    "v_exp_f32 v54, v54\n"
    "v_mfma_f32_16x16x32_bf16 a[252:255], a[44:47], v[24:27], a[252:255]\n"
    "v_exp_f32 v55, v55\n"
    "v_mfma_f32_16x16x32_bf16 v[204:207], v[208:211], v[24:27], v[204:207]\n"
    "s_waitcnt vmcnt(12) lgkmcnt(0)\n"
    "s_barrier\n"
    "L_ph32:\n"
    "v_mfma_f32_16x16x32_bf16 v[0:3], a[48:51], a[96:99], 0\n"
    "buffer_load_dwordx4 v231, s[24:27], s42 offen nt lds\n"
    "v_mfma_f32_16x16x32_bf16 v[0:3], a[52:55], a[100:103], v[0:3]\n"
    "ds_read_b64_tr_b16 a[0:1], v218 offset:24576\n"
    "v_cvt_pk_bf16_f32 v48, v48, v49\n"
    "v_mfma_f32_16x16x32_bf16 v[0:3], a[56:59], a[104:107], v[0:3]\n"
    "buffer_load_dwordx4 v232, s[24:27], s42 offen offset:1024 nt lds\n"
    "v_mfma_f32_16x16x32_bf16 v[0:3], a[60:63], a[108:111], v[0:3]\n"
    "ds_read_b64_tr_b16 a[2:3], v218 offset:30720\n"
    "v_cvt_pk_bf16_f32 v49, v50, v51\n"
    "v_mfma_f32_16x16x32_bf16 v[0:3], a[64:67], a[112:115], v[0:3]\n"
    "buffer_load_dwordx4 v233, s[24:27], s42 offen offset:2048 nt lds\n"
    "v_mfma_f32_16x16x32_bf16 v[0:3], a[68:71], a[116:119], v[0:3]\n"
    "ds_read_b64_tr_b16 a[4:5], v219 offset:24576\n"
    "s_add_u32 m0, s37, 24576\n"
    "v_mfma_f32_16x16x32_bf16 v[4:7], a[72:75], a[96:99], 0\n"
    "ds_read_b64_tr_b16 a[6:7], v219 offset:30720\n"
    "s_add_u32 s42, s39, 36864\n"
    "v_mfma_f32_16x16x32_bf16 v[4:7], a[76:79], a[100:103], v[4:7]\n"
    "ds_read_b64_tr_b16 a[8:9], v220 offset:24576\n"
    "v_cvt_pk_bf16_f32 v50, v52, v53\n"
    "v_mfma_f32_16x16x32_bf16 v[4:7], a[80:83], a[104:107], v[4:7]\n"
    "ds_read_b64_tr_b16 a[10:11], v220 offset:30720\n"
    "v_cvt_pk_bf16_f32 v51, v54, v55\n"
    "v_mfma_f32_16x16x32_bf16 v[4:7], a[84:87], a[108:111], v[4:7]\n"
    "ds_read_b64_tr_b16 a[12:13], v221 offset:24576\n"
    "v_mfma_f32_16x16x32_bf16 v[4:7], a[88:91], a[112:115], v[4:7]\n"
    "ds_read_b64_tr_b16 a[14:15], v221 offset:30720\n"
    "v_mfma_f32_16x16x32_bf16 v[4:7], a[92:95], a[116:119], v[4:7]\n"
    "ds_read_b64_tr_b16 a[16:17], v218 offset:24704\n"
    "v_mfma_f32_16x16x32_bf16 v[8:11], a[48:51], a[120:123], 0\n"
    "ds_read_b64_tr_b16 a[18:19], v218 offset:30848\n"
    "v_mfma_f32_16x16x32_bf16 v[8:11], a[52:55], a[124:127], v[8:11]\n"
    "ds_read_b64_tr_b16 a[20:21], v219 offset:24704\n"
    "v_max3_f32 v250, v0, v1, v2\n"
    "v_mfma_f32_16x16x32_bf16 v[8:11], a[56:59], a[128:131], v[8:11]\n"
    "ds_read_b64_tr_b16 a[22:23], v219 offset:30848\n"
    "v_mfma_f32_16x16x32_bf16 v[8:11], a[60:63], a[132:135], v[8:11]\n"
    "ds_read_b64_tr_b16 a[24:25], v220 offset:24704\n"
    "v_mfma_f32_16x16x32_bf16 v[8:11], a[64:67], a[136:139], v[8:11]\n"
    "ds_read_b64_tr_b16 a[26:27], v220 offset:30848\n"
    "v_mfma_f32_16x16x32_bf16 v[8:11], a[68:71], a[140:143], v[8:11]\n"
    "ds_read_b64_tr_b16 a[28:29], v221 offset:24704\n"
    "v_mfma_f32_16x16x32_bf16 v[12:15], a[72:75], a[120:123], 0\n"
    "ds_read_b64_tr_b16 a[30:31], v221 offset:30848\n"
    "v_mfma_f32_16x16x32_bf16 v[12:15], a[76:79], a[124:127], v[12:15]\n"
    "ds_read_b64_tr_b16 a[32:33], v218 offset:24832\n"
    "v_max3_f32 v250, v250, v3, v4\n"
    "v_mfma_f32_16x16x32_bf16 v[12:15], a[80:83], a[128:131], v[12:15]\n"
    "ds_read_b64_tr_b16 a[34:35], v218 offset:30976\n"
    "v_max3_f32 v250, v250, v5, v6\n"
    "v_mfma_f32_16x16x32_bf16 v[12:15], a[84:87], a[132:135], v[12:15]\n"
    "ds_read_b64_tr_b16 a[36:37], v219 offset:24832\n"
    "v_max_f32 v250, v250, v7\n"
    "v_mfma_f32_16x16x32_bf16 v[12:15], a[88:91], a[136:139], v[12:15]\n"
    "ds_read_b64_tr_b16 a[38:39], v219 offset:30976\n"
    "v_mov_b32 v251, v250\n"
    "v_mfma_f32_16x16x32_bf16 v[12:15], a[92:95], a[140:143], v[12:15]\n"
    "ds_read_b64_tr_b16 a[40:41], v220 offset:24832\n"
    "v_mfma_f32_16x16x32_bf16 v[16:19], a[48:51], a[144:147], 0\n"
    "ds_read_b64_tr_b16 a[42:43], v220 offset:30976\n"
    "v_permlane16_swap_b32 v250, v251\n"
    "v_mfma_f32_16x16x32_bf16 v[16:19], a[52:55], a[148:151], v[16:19]\n"
    "ds_read_b64_tr_b16 a[44:45], v221 offset:24832\n"
    "v_max_f32 v250, v250, v251\n"
    "v_mfma_f32_16x16x32_bf16 v[16:19], a[56:59], a[152:155], v[16:19]\n"
    "ds_read_b64_tr_b16 a[46:47], v221 offset:30976\n"
    "v_mov_b32 v251, v250\n"
    "v_mfma_f32_16x16x32_bf16 v[16:19], a[60:63], a[156:159], v[16:19]\n"
    "v_exp_f32 v56, v56\n"
    "v_mfma_f32_16x16x32_bf16 v[16:19], a[64:67], a[160:163], v[16:19]\n"
    "v_exp_f32 v57, v57\n"
    "v_mfma_f32_16x16x32_bf16 v[16:19], a[68:71], a[164:167], v[16:19]\n"
    "v_exp_f32 v58, v58\n"
    "v_mfma_f32_16x16x32_bf16 v[20:23], a[72:75], a[144:147], 0\n"
    "v_exp_f32 v59, v59\n"
    "v_mfma_f32_16x16x32_bf16 v[20:23], a[76:79], a[148:151], v[20:23]\n"
    "v_exp_f32 v60, v60\n"
    "v_mfma_f32_16x16x32_bf16 v[20:23], a[80:83], a[152:155], v[20:23]\n"
    "v_exp_f32 v61, v61\n"
    "v_mfma_f32_16x16x32_bf16 v[20:23], a[84:87], a[156:159], v[20:23]\n"
    "v_exp_f32 v62, v62\n"
    "v_mfma_f32_16x16x32_bf16 v[20:23], a[88:91], a[160:163], v[20:23]\n"
    "v_exp_f32 v63, v63\n"
    "v_mfma_f32_16x16x32_bf16 v[20:23], a[92:95], a[164:167], v[20:23]\n"
    "v_cvt_pk_bf16_f32 v56, v56, v57\n"
    "v_cvt_pk_bf16_f32 v57, v58, v59\n"
    "v_mfma_f32_16x16x32_bf16 v[24:27], a[48:51], a[168:171], 0\n"
    "v_cvt_pk_bf16_f32 v58, v60, v61\n"
    "v_cvt_pk_bf16_f32 v59, v62, v63\n"
    "v_mfma_f32_16x16x32_bf16 v[24:27], a[52:55], a[172:175], v[24:27]\n"
    "v_permlane32_swap_b32 v250, v251\n"
    "v_max3_f32 v252, v8, v9, v10\n"
    "v_mfma_f32_16x16x32_bf16 v[24:27], a[56:59], a[176:179], v[24:27]\n"
    "v_max_f32 v250, v250, v251\n"
    "v_max3_f32 v252, v252, v11, v12\n"
    "v_mfma_f32_16x16x32_bf16 v[24:27], a[60:63], a[180:183], v[24:27]\n"
    "v_mul_f32 v212, s49, v250\n"
    "v_max3_f32 v252, v252, v13, v14\n"
    "v_mfma_f32_16x16x32_bf16 v[24:27], a[64:67], a[184:187], v[24:27]\n"
    "v_fma_f32 v0, v0, s36, v212\n"
    "v_fma_f32 v1, v1, s36, v212\n"
    "v_mfma_f32_16x16x32_bf16 v[24:27], a[68:71], a[188:191], v[24:27]\n"
    "v_exp_f32 v0, v0\n"
    "v_mfma_f32_16x16x32_bf16 v[28:31], a[72:75], a[168:171], 0\n"
    "v_exp_f32 v1, v1\n"
    "v_mfma_f32_16x16x32_bf16 v[28:31], a[76:79], a[172:175], v[28:31]\n"
    "v_fma_f32 v2, v2, s36, v212\n"
    "v_fma_f32 v3, v3, s36, v212\n"
    "v_mfma_f32_16x16x32_bf16 v[28:31], a[80:83], a[176:179], v[28:31]\n"
    "v_exp_f32 v2, v2\n"
    "v_mfma_f32_16x16x32_bf16 v[28:31], a[84:87], a[180:183], v[28:31]\n"
    "v_exp_f32 v3, v3\n"
    "v_mfma_f32_16x16x32_bf16 v[28:31], a[88:91], a[184:187], v[28:31]\n"
    "v_fma_f32 v4, v4, s36, v212\n"
    "v_fma_f32 v5, v5, s36, v212\n"
    "v_mfma_f32_16x16x32_bf16 v[28:31], a[92:95], a[188:191], v[28:31]\n"
    "s_waitcnt vmcnt(11) lgkmcnt(0)\n"
    "s_barrier\n"
    "L_ph33:\n"
    "v_mfma_f32_16x16x32_bf16 v[64:67], a[0:3], v[32:35], v[64:67]\n"
    "buffer_load_dwordx4 v222, s[20:23], s42 offen nt lds\n"
    "v_mfma_f32_16x16x32_bf16 v[80:83], a[4:7], v[32:35], v[80:83]\n"
    "ds_read_b128 a[48:51], v216 offset:36864\n"
    "v_fma_f32 v6, v6, s36, v212\n"
    "v_mfma_f32_16x16x32_bf16 v[96:99], a[8:11], v[32:35], v[96:99]\n"
    "buffer_load_dwordx4 v223, s[20:23], s42 offen offset:1024 nt lds\n"
    "v_mfma_f32_16x16x32_bf16 v[112:115], a[12:15], v[32:35], v[112:115]\n"
    "ds_read_b128 a[52:55], v217 offset:36864\n"
    "v_fma_f32 v7, v7, s36, v212\n"
    "v_mfma_f32_16x16x32_bf16 v[128:131], a[16:19], v[32:35], v[128:131]\n"
    "buffer_load_dwordx4 v224, s[20:23], s42 offen offset:2048 nt lds\n"
    "v_mfma_f32_16x16x32_bf16 v[144:147], a[20:23], v[32:35], v[144:147]\n"
    "ds_read_b128 a[56:59], v216 offset:36992\n"
    "v_cvt_pk_bf16_f32 v0, v0, v1\n"
    "v_mfma_f32_16x16x32_bf16 v[160:163], a[24:27], v[32:35], v[160:163]\n"
    "ds_read_b128 a[60:63], v217 offset:36992\n"
    "v_cvt_pk_bf16_f32 v1, v2, v3\n"
    "v_mfma_f32_16x16x32_bf16 v[176:179], a[28:31], v[32:35], v[176:179]\n"
    "ds_read_b128 a[64:67], v216 offset:37120\n"
    "v_max_f32 v252, v252, v15\n"
    "v_mfma_f32_16x16x32_bf16 a[192:195], a[32:35], v[32:35], a[192:195]\n"
    "ds_read_b128 a[68:71], v217 offset:37120\n"
    "v_mov_b32 v253, v252\n"
    "v_mfma_f32_16x16x32_bf16 a[208:211], a[36:39], v[32:35], a[208:211]\n"
    "ds_read_b128 a[72:75], v216 offset:43008\n"
    "v_max3_f32 v250, v16, v17, v18\n"
    "v_mfma_f32_16x16x32_bf16 a[224:227], a[40:43], v[32:35], a[224:227]\n"
    "ds_read_b128 a[76:79], v217 offset:43008\n"
    "v_permlane16_swap_b32 v252, v253\n"
    "v_mfma_f32_16x16x32_bf16 a[240:243], a[44:47], v[32:35], a[240:243]\n"
    "ds_read_b128 a[80:83], v216 offset:43136\n"
    "v_max_f32 v252, v252, v253\n"
    "v_mfma_f32_16x16x32_bf16 v[192:195], v[208:211], v[32:35], v[192:195]\n"
    "ds_read_b128 a[84:87], v217 offset:43136\n"
    "v_mov_b32 v253, v252\n"
    "v_mfma_f32_16x16x32_bf16 v[68:71], a[0:3], v[40:43], v[68:71]\n"
    "ds_read_b128 a[88:91], v216 offset:43264\n"
    "v_max3_f32 v250, v250, v19, v20\n"
    "v_mfma_f32_16x16x32_bf16 v[84:87], a[4:7], v[40:43], v[84:87]\n"
    "ds_read_b128 a[92:95], v217 offset:43264\n"
    "v_permlane32_swap_b32 v252, v253\n"
    "v_mfma_f32_16x16x32_bf16 v[100:103], a[8:11], v[40:43], v[100:103]\n"
    "v_exp_f32 v4, v4\n"
    "v_mfma_f32_16x16x32_bf16 v[116:119], a[12:15], v[40:43], v[116:119]\n"
    "v_exp_f32 v5, v5\n"
    "v_mfma_f32_16x16x32_bf16 v[132:135], a[16:19], v[40:43], v[132:135]\n"
    "v_exp_f32 v6, v6\n"
    "v_mfma_f32_16x16x32_bf16 v[148:151], a[20:23], v[40:43], v[148:151]\n"
    "v_exp_f32 v7, v7\n"
    "v_mfma_f32_16x16x32_bf16 v[164:167], a[24:27], v[40:43], v[164:167]\n"
    "v_cvt_pk_bf16_f32 v2, v4, v5\n"
    "v_cvt_pk_bf16_f32 v3, v6, v7\n"
    "v_mfma_f32_16x16x32_bf16 v[180:183], a[28:31], v[40:43], v[180:183]\n"
    "v_max_f32 v252, v252, v253\n"
    "v_max3_f32 v250, v250, v21, v22\n"
    "v_mfma_f32_16x16x32_bf16 a[196:199], a[32:35], v[40:43], a[196:199]\n"
    "v_mul_f32 v213, s49, v252\n"
    "v_max_f32 v250, v250, v23\n"
    "v_mfma_f32_16x16x32_bf16 a[212:215], a[36:39], v[40:43], a[212:215]\n"
    "v_fma_f32 v8, v8, s36, v213\n"
    "v_fma_f32 v9, v9, s36, v213\n"
    "v_mfma_f32_16x16x32_bf16 a[228:231], a[40:43], v[40:43], a[228:231]\n"
    "v_exp_f32 v8, v8\n"
    "v_mfma_f32_16x16x32_bf16 a[244:247], a[44:47], v[40:43], a[244:247]\n"
    "v_exp_f32 v9, v9\n"
    "v_mfma_f32_16x16x32_bf16 v[196:199], v[208:211], v[40:43], v[196:199]\n"
    "v_fma_f32 v10, v10, s36, v213\n"
    "v_fma_f32 v11, v11, s36, v213\n"
    "v_mfma_f32_16x16x32_bf16 v[72:75], a[0:3], v[48:51], v[72:75]\n"
    "v_exp_f32 v10, v10\n"
    "v_mfma_f32_16x16x32_bf16 v[88:91], a[4:7], v[48:51], v[88:91]\n"
    "v_exp_f32 v11, v11\n"
    "v_mfma_f32_16x16x32_bf16 v[104:107], a[8:11], v[48:51], v[104:107]\n"
    "v_fma_f32 v12, v12, s36, v213\n"
    "v_fma_f32 v13, v13, s36, v213\n"
    "v_mfma_f32_16x16x32_bf16 v[120:123], a[12:15], v[48:51], v[120:123]\n"
    "v_exp_f32 v12, v12\n"
    "v_mfma_f32_16x16x32_bf16 v[136:139], a[16:19], v[48:51], v[136:139]\n"
    "v_exp_f32 v13, v13\n"
    "v_mfma_f32_16x16x32_bf16 v[152:155], a[20:23], v[48:51], v[152:155]\n"
    "v_fma_f32 v14, v14, s36, v213\n"
    "v_fma_f32 v15, v15, s36, v213\n"
    "v_mfma_f32_16x16x32_bf16 v[168:171], a[24:27], v[48:51], v[168:171]\n"
    "v_exp_f32 v14, v14\n"
    "v_mfma_f32_16x16x32_bf16 v[184:187], a[28:31], v[48:51], v[184:187]\n"
    "v_exp_f32 v15, v15\n"
    "v_mfma_f32_16x16x32_bf16 a[200:203], a[32:35], v[48:51], a[200:203]\n"
    "v_cvt_pk_bf16_f32 v8, v8, v9\n"
    "v_cvt_pk_bf16_f32 v9, v10, v11\n"
    "v_mfma_f32_16x16x32_bf16 a[216:219], a[36:39], v[48:51], a[216:219]\n"
    "v_cvt_pk_bf16_f32 v10, v12, v13\n"
    "v_cvt_pk_bf16_f32 v11, v14, v15\n"
    "v_mfma_f32_16x16x32_bf16 a[232:235], a[40:43], v[48:51], a[232:235]\n"
    "v_mov_b32 v251, v250\n"
    "v_max3_f32 v252, v24, v25, v26\n"
    "v_mfma_f32_16x16x32_bf16 a[248:251], a[44:47], v[48:51], a[248:251]\n"
    "v_max3_f32 v252, v252, v27, v28\n"
    "v_mfma_f32_16x16x32_bf16 v[200:203], v[208:211], v[48:51], v[200:203]\n"
    "v_permlane16_swap_b32 v250, v251\n"
    "v_max3_f32 v252, v252, v29, v30\n"
    "v_mfma_f32_16x16x32_bf16 v[76:79], a[0:3], v[56:59], v[76:79]\n"
    "v_max_f32 v250, v250, v251\n"
    "v_max_f32 v252, v252, v31\n"
    "v_mfma_f32_16x16x32_bf16 v[92:95], a[4:7], v[56:59], v[92:95]\n"
    "v_mov_b32 v251, v250\n"
    "v_mov_b32 v253, v252\n"
    "v_mfma_f32_16x16x32_bf16 v[108:111], a[8:11], v[56:59], v[108:111]\n"
    "v_mfma_f32_16x16x32_bf16 v[124:127], a[12:15], v[56:59], v[124:127]\n"
    "v_permlane32_swap_b32 v250, v251\n"
    "v_permlane16_swap_b32 v252, v253\n"
    "v_mfma_f32_16x16x32_bf16 v[140:143], a[16:19], v[56:59], v[140:143]\n"
    "v_max_f32 v250, v250, v251\n"
    "v_max_f32 v252, v252, v253\n"
    "v_mfma_f32_16x16x32_bf16 v[156:159], a[20:23], v[56:59], v[156:159]\n"
    "v_mul_f32 v214, s49, v250\n"
    "v_mov_b32 v253, v252\n"
    "v_mfma_f32_16x16x32_bf16 v[172:175], a[24:27], v[56:59], v[172:175]\n"
    "v_fma_f32 v16, v16, s36, v214\n"
    "v_fma_f32 v17, v17, s36, v214\n"
    "v_mfma_f32_16x16x32_bf16 v[188:191], a[28:31], v[56:59], v[188:191]\n"
    "v_exp_f32 v16, v16\n"
    "v_mfma_f32_16x16x32_bf16 a[204:207], a[32:35], v[56:59], a[204:207]\n"
    "v_exp_f32 v17, v17\n"
    "v_mfma_f32_16x16x32_bf16 a[220:223], a[36:39], v[56:59], a[220:223]\n"
    "v_fma_f32 v18, v18, s36, v214\n"
    "v_fma_f32 v19, v19, s36, v214\n"
    "v_mfma_f32_16x16x32_bf16 a[236:239], a[40:43], v[56:59], a[236:239]\n"
    "v_exp_f32 v18, v18\n"
    "v_mfma_f32_16x16x32_bf16 a[252:255], a[44:47], v[56:59], a[252:255]\n"
    "v_exp_f32 v19, v19\n"
    "v_mfma_f32_16x16x32_bf16 v[204:207], v[208:211], v[56:59], v[204:207]\n"
    "s_waitcnt vmcnt(10) lgkmcnt(0)\n"
    "s_barrier\n"
    "s_add_u32 m0, s37, 36864\n"
    "s_add_u32 s42, s39, 24576\n"
    "s_nop 0\n"
    "buffer_load_dwordx4 v231, s[24:27], s42 offen nt lds\n"
    "buffer_load_dwordx4 v232, s[24:27], s42 offen offset:1024 nt lds\n"
    "buffer_load_dwordx4 v233, s[24:27], s42 offen offset:2048 nt lds\n"
    "v_rcp_f32 v248, v192\n"
    "v_cmp_nge_f32 vcc, s48, v192\n"
    "v_accvgpr_read_b32 v32, a192\n"
    "v_accvgpr_read_b32 v33, a193\n"
    "v_accvgpr_read_b32 v34, a194\n"
    "v_accvgpr_read_b32 v35, a195\n"
    "v_accvgpr_read_b32 v36, a208\n"
    "v_accvgpr_read_b32 v37, a209\n"
    "v_accvgpr_read_b32 v38, a210\n"
    "v_accvgpr_read_b32 v39, a211\n"
    "v_accvgpr_read_b32 v40, a224\n"
    "v_accvgpr_read_b32 v41, a225\n"
    "v_accvgpr_read_b32 v42, a226\n"
    "v_accvgpr_read_b32 v43, a227\n"
    "v_accvgpr_read_b32 v44, a240\n"
    "v_accvgpr_read_b32 v45, a241\n"
    "v_accvgpr_read_b32 v46, a242\n"
    "v_accvgpr_read_b32 v47, a243\n"
    "s_or_b64 s[46:47], s[46:47], vcc\n"
    "v_pk_mul_f32 v[64:65], v[64:65], v[248:249] op_sel_hi:[1,0]\n"
    "v_pk_mul_f32 v[66:67], v[66:67], v[248:249] op_sel_hi:[1,0]\n"
    "v_pk_mul_f32 v[80:81], v[80:81], v[248:249] op_sel_hi:[1,0]\n"
    "v_pk_mul_f32 v[82:83], v[82:83], v[248:249] op_sel_hi:[1,0]\n"
    "v_cvt_pk_bf16_f32 v64, v64, v65\n"
    "v_cvt_pk_bf16_f32 v65, v66, v67\n"
    "v_pk_mul_f32 v[96:97], v[96:97], v[248:249] op_sel_hi:[1,0]\n"
    "v_pk_mul_f32 v[98:99], v[98:99], v[248:249] op_sel_hi:[1,0]\n"
    "v_cvt_pk_bf16_f32 v80, v80, v81\n"
    "v_cvt_pk_bf16_f32 v81, v82, v83\n"
    "ds_write_b64 v237, v[64:65] offset:0\n"
    "v_pk_mul_f32 v[112:113], v[112:113], v[248:249] op_sel_hi:[1,0]\n"
    "v_pk_mul_f32 v[114:115], v[114:115], v[248:249] op_sel_hi:[1,0]\n"
    "v_cvt_pk_bf16_f32 v96, v96, v97\n"
    "v_cvt_pk_bf16_f32 v97, v98, v99\n"
    "ds_write_b64 v238, v[80:81] offset:0\n"
    "v_pk_mul_f32 v[128:129], v[128:129], v[248:249] op_sel_hi:[1,0]\n"
    "v_pk_mul_f32 v[130:131], v[130:131], v[248:249] op_sel_hi:[1,0]\n"
    "v_cvt_pk_bf16_f32 v112, v112, v113\n"
    "v_cvt_pk_bf16_f32 v113, v114, v115\n"
    "ds_write_b64 v239, v[96:97] offset:0\n"
    "v_pk_mul_f32 v[144:145], v[144:145], v[248:249] op_sel_hi:[1,0]\n"
    "v_pk_mul_f32 v[146:147], v[146:147], v[248:249] op_sel_hi:[1,0]\n"
    "v_cvt_pk_bf16_f32 v128, v128, v129\n"
    "v_cvt_pk_bf16_f32 v129, v130, v131\n"
    "ds_write_b64 v240, v[112:113] offset:0\n"
    "v_pk_mul_f32 v[160:161], v[160:161], v[248:249] op_sel_hi:[1,0]\n"
    "v_pk_mul_f32 v[162:163], v[162:163], v[248:249] op_sel_hi:[1,0]\n"
    "v_cvt_pk_bf16_f32 v144, v144, v145\n"
    "v_cvt_pk_bf16_f32 v145, v146, v147\n"
    "ds_write_b64 v237, v[128:129] offset:128\n"
    "v_pk_mul_f32 v[176:177], v[176:177], v[248:249] op_sel_hi:[1,0]\n"
    "v_pk_mul_f32 v[178:179], v[178:179], v[248:249] op_sel_hi:[1,0]\n"
    "v_cvt_pk_bf16_f32 v160, v160, v161\n"
    "v_cvt_pk_bf16_f32 v161, v162, v163\n"
    "ds_write_b64 v238, v[144:145] offset:128\n"
    "v_pk_mul_f32 v[32:33], v[32:33], v[248:249] op_sel_hi:[1,0]\n"
    "v_pk_mul_f32 v[34:35], v[34:35], v[248:249] op_sel_hi:[1,0]\n"
    "v_cvt_pk_bf16_f32 v176, v176, v177\n"
    "v_cvt_pk_bf16_f32 v177, v178, v179\n"
    "ds_write_b64 v239, v[160:161] offset:128\n"
    "v_pk_mul_f32 v[36:37], v[36:37], v[248:249] op_sel_hi:[1,0]\n"
    "v_pk_mul_f32 v[38:39], v[38:39], v[248:249] op_sel_hi:[1,0]\n"
    "v_cvt_pk_bf16_f32 v32, v32, v33\n"
    "v_cvt_pk_bf16_f32 v33, v34, v35\n"
    "ds_write_b64 v240, v[176:177] offset:128\n"
    "v_pk_mul_f32 v[40:41], v[40:41], v[248:249] op_sel_hi:[1,0]\n"
    "v_pk_mul_f32 v[42:43], v[42:43], v[248:249] op_sel_hi:[1,0]\n"
    "v_cvt_pk_bf16_f32 v36, v36, v37\n"
    "v_cvt_pk_bf16_f32 v37, v38, v39\n"
    "ds_write_b64 v237, v[32:33] offset:256\n"
    "v_pk_mul_f32 v[44:45], v[44:45], v[248:249] op_sel_hi:[1,0]\n"
    "v_pk_mul_f32 v[46:47], v[46:47], v[248:249] op_sel_hi:[1,0]\n"
    "v_cvt_pk_bf16_f32 v40, v40, v41\n"
    "v_cvt_pk_bf16_f32 v41, v42, v43\n"
    "ds_write_b64 v238, v[36:37] offset:256\n"
    "v_cvt_pk_bf16_f32 v44, v44, v45\n"
    "v_cvt_pk_bf16_f32 v45, v46, v47\n"
    "ds_write_b64 v239, v[40:41] offset:256\n"
    "ds_write_b64 v240, v[44:45] offset:256\n"
    "v_rcp_f32 v248, v196\n"
    "v_cmp_nge_f32 vcc, s48, v196\n"
    "v_accvgpr_read_b32 v48, a196\n"
    "v_accvgpr_read_b32 v49, a197\n"
    "v_accvgpr_read_b32 v50, a198\n"
    "v_accvgpr_read_b32 v51, a199\n"
    "v_accvgpr_read_b32 v52, a212\n"
    "v_accvgpr_read_b32 v53, a213\n"
    "v_accvgpr_read_b32 v54, a214\n"
    "v_accvgpr_read_b32 v55, a215\n"
    "v_accvgpr_read_b32 v56, a228\n"
    "v_accvgpr_read_b32 v57, a229\n"
    "v_accvgpr_read_b32 v58, a230\n"
    "v_accvgpr_read_b32 v59, a231\n"
    "v_accvgpr_read_b32 v60, a244\n"
    "v_accvgpr_read_b32 v61, a245\n"
    "v_accvgpr_read_b32 v62, a246\n"
    "v_accvgpr_read_b32 v63, a247\n"
    "s_or_b64 s[46:47], s[46:47], vcc\n"
    "v_pk_mul_f32 v[68:69], v[68:69], v[248:249] op_sel_hi:[1,0]\n"
    "v_pk_mul_f32 v[70:71], v[70:71], v[248:249] op_sel_hi:[1,0]\n"
    "v_pk_mul_f32 v[84:85], v[84:85], v[248:249] op_sel_hi:[1,0]\n"
    "v_pk_mul_f32 v[86:87], v[86:87], v[248:249] op_sel_hi:[1,0]\n"
    "v_cvt_pk_bf16_f32 v68, v68, v69\n"
    "v_cvt_pk_bf16_f32 v69, v70, v71\n"
    "v_pk_mul_f32 v[100:101], v[100:101], v[248:249] op_sel_hi:[1,0]\n"
    "v_pk_mul_f32 v[102:103], v[102:103], v[248:249] op_sel_hi:[1,0]\n"
    "v_cvt_pk_bf16_f32 v84, v84, v85\n"
    "v_cvt_pk_bf16_f32 v85, v86, v87\n"
    "ds_write_b64 v237, v[68:69] offset:6144\n"
    "v_pk_mul_f32 v[116:117], v[116:117], v[248:249] op_sel_hi:[1,0]\n"
    "v_pk_mul_f32 v[118:119], v[118:119], v[248:249] op_sel_hi:[1,0]\n"
    "v_cvt_pk_bf16_f32 v100, v100, v101\n"
    "v_cvt_pk_bf16_f32 v101, v102, v103\n"
    "ds_write_b64 v238, v[84:85] offset:6144\n"
    "v_pk_mul_f32 v[132:133], v[132:133], v[248:249] op_sel_hi:[1,0]\n"
    "v_pk_mul_f32 v[134:135], v[134:135], v[248:249] op_sel_hi:[1,0]\n"
    "v_cvt_pk_bf16_f32 v116, v116, v117\n"
    "v_cvt_pk_bf16_f32 v117, v118, v119\n"
    "ds_write_b64 v239, v[100:101] offset:6144\n"
    "v_pk_mul_f32 v[148:149], v[148:149], v[248:249] op_sel_hi:[1,0]\n"
    "v_pk_mul_f32 v[150:151], v[150:151], v[248:249] op_sel_hi:[1,0]\n"
    "v_cvt_pk_bf16_f32 v132, v132, v133\n"
    "v_cvt_pk_bf16_f32 v133, v134, v135\n"
    "ds_write_b64 v240, v[116:117] offset:6144\n"
    "v_pk_mul_f32 v[164:165], v[164:165], v[248:249] op_sel_hi:[1,0]\n"
    "v_pk_mul_f32 v[166:167], v[166:167], v[248:249] op_sel_hi:[1,0]\n"
    "v_cvt_pk_bf16_f32 v148, v148, v149\n"
    "v_cvt_pk_bf16_f32 v149, v150, v151\n"
    "ds_write_b64 v237, v[132:133] offset:6272\n"
    "v_pk_mul_f32 v[180:181], v[180:181], v[248:249] op_sel_hi:[1,0]\n"
    "v_pk_mul_f32 v[182:183], v[182:183], v[248:249] op_sel_hi:[1,0]\n"
    "v_cvt_pk_bf16_f32 v164, v164, v165\n"
    "v_cvt_pk_bf16_f32 v165, v166, v167\n"
    "ds_write_b64 v238, v[148:149] offset:6272\n"
    "v_pk_mul_f32 v[48:49], v[48:49], v[248:249] op_sel_hi:[1,0]\n"
    "v_pk_mul_f32 v[50:51], v[50:51], v[248:249] op_sel_hi:[1,0]\n"
    "v_cvt_pk_bf16_f32 v180, v180, v181\n"
    "v_cvt_pk_bf16_f32 v181, v182, v183\n"
    "ds_write_b64 v239, v[164:165] offset:6272\n"
    "v_pk_mul_f32 v[52:53], v[52:53], v[248:249] op_sel_hi:[1,0]\n"
    "v_pk_mul_f32 v[54:55], v[54:55], v[248:249] op_sel_hi:[1,0]\n"
    "v_cvt_pk_bf16_f32 v48, v48, v49\n"
    "v_cvt_pk_bf16_f32 v49, v50, v51\n"
    "ds_write_b64 v240, v[180:181] offset:6272\n"
    "v_pk_mul_f32 v[56:57], v[56:57], v[248:249] op_sel_hi:[1,0]\n"
    "v_pk_mul_f32 v[58:59], v[58:59], v[248:249] op_sel_hi:[1,0]\n"
    "v_cvt_pk_bf16_f32 v52, v52, v53\n"
    "v_cvt_pk_bf16_f32 v53, v54, v55\n"
    "ds_write_b64 v237, v[48:49] offset:6400\n"
    "v_pk_mul_f32 v[60:61], v[60:61], v[248:249] op_sel_hi:[1,0]\n"
    "v_pk_mul_f32 v[62:63], v[62:63], v[248:249] op_sel_hi:[1,0]\n"
    "v_cvt_pk_bf16_f32 v56, v56, v57\n"
    "v_cvt_pk_bf16_f32 v57, v58, v59\n"
    "ds_write_b64 v238, v[52:53] offset:6400\n"
    "v_cvt_pk_bf16_f32 v60, v60, v61\n"
    "v_cvt_pk_bf16_f32 v61, v62, v63\n"
    "ds_write_b64 v239, v[56:57] offset:6400\n"
    "ds_write_b64 v240, v[60:61] offset:6400\n"
    "v_rcp_f32 v248, v200\n"
    "v_cmp_nge_f32 vcc, s48, v200\n"
    "v_accvgpr_read_b32 v32, a200\n"
    "v_accvgpr_read_b32 v33, a201\n"
    "v_accvgpr_read_b32 v34, a202\n"
    "v_accvgpr_read_b32 v35, a203\n"
    "v_accvgpr_read_b32 v36, a216\n"
    "v_accvgpr_read_b32 v37, a217\n"
    "v_accvgpr_read_b32 v38, a218\n"
    "v_accvgpr_read_b32 v39, a219\n"
    "v_accvgpr_read_b32 v40, a232\n"
    "v_accvgpr_read_b32 v41, a233\n"
    "v_accvgpr_read_b32 v42, a234\n"
    "v_accvgpr_read_b32 v43, a235\n"
    "v_accvgpr_read_b32 v44, a248\n"
    "v_accvgpr_read_b32 v45, a249\n"
    "v_accvgpr_read_b32 v46, a250\n"
    "v_accvgpr_read_b32 v47, a251\n"
    "s_or_b64 s[46:47], s[46:47], vcc\n"
    "v_pk_mul_f32 v[72:73], v[72:73], v[248:249] op_sel_hi:[1,0]\n"
    "v_pk_mul_f32 v[74:75], v[74:75], v[248:249] op_sel_hi:[1,0]\n"
    "v_pk_mul_f32 v[88:89], v[88:89], v[248:249] op_sel_hi:[1,0]\n"
    "v_pk_mul_f32 v[90:91], v[90:91], v[248:249] op_sel_hi:[1,0]\n"
    "v_cvt_pk_bf16_f32 v72, v72, v73\n"
    "v_cvt_pk_bf16_f32 v73, v74, v75\n"
    "v_pk_mul_f32 v[104:105], v[104:105], v[248:249] op_sel_hi:[1,0]\n"
    "v_pk_mul_f32 v[106:107], v[106:107], v[248:249] op_sel_hi:[1,0]\n"
    "v_cvt_pk_bf16_f32 v88, v88, v89\n"
    "v_cvt_pk_bf16_f32 v89, v90, v91\n"
    "ds_write_b64 v237, v[72:73] offset:12288\n"
    "v_pk_mul_f32 v[120:121], v[120:121], v[248:249] op_sel_hi:[1,0]\n"
    "v_pk_mul_f32 v[122:123], v[122:123], v[248:249] op_sel_hi:[1,0]\n"
    "v_cvt_pk_bf16_f32 v104, v104, v105\n"
    "v_cvt_pk_bf16_f32 v105, v106, v107\n"
    "ds_write_b64 v238, v[88:89] offset:12288\n"
    "v_pk_mul_f32 v[136:137], v[136:137], v[248:249] op_sel_hi:[1,0]\n"
    "v_pk_mul_f32 v[138:139], v[138:139], v[248:249] op_sel_hi:[1,0]\n"
    "v_cvt_pk_bf16_f32 v120, v120, v121\n"
    "v_cvt_pk_bf16_f32 v121, v122, v123\n"
    "ds_write_b64 v239, v[104:105] offset:12288\n"
    "v_pk_mul_f32 v[152:153], v[152:153], v[248:249] op_sel_hi:[1,0]\n"
    "v_pk_mul_f32 v[154:155], v[154:155], v[248:249] op_sel_hi:[1,0]\n"
    "v_cvt_pk_bf16_f32 v136, v136, v137\n"
    "v_cvt_pk_bf16_f32 v137, v138, v139\n"
    "ds_write_b64 v240, v[120:121] offset:12288\n"
    "v_pk_mul_f32 v[168:169], v[168:169], v[248:249] op_sel_hi:[1,0]\n"
    "v_pk_mul_f32 v[170:171], v[170:171], v[248:249] op_sel_hi:[1,0]\n"
    "v_cvt_pk_bf16_f32 v152, v152, v153\n"
    "v_cvt_pk_bf16_f32 v153, v154, v155\n"
    "ds_write_b64 v237, v[136:137] offset:12416\n"
    "v_pk_mul_f32 v[184:185], v[184:185], v[248:249] op_sel_hi:[1,0]\n"
    "v_pk_mul_f32 v[186:187], v[186:187], v[248:249] op_sel_hi:[1,0]\n"
    "v_cvt_pk_bf16_f32 v168, v168, v169\n"
    "v_cvt_pk_bf16_f32 v169, v170, v171\n"
    "ds_write_b64 v238, v[152:153] offset:12416\n"
    "v_pk_mul_f32 v[32:33], v[32:33], v[248:249] op_sel_hi:[1,0]\n"
    "v_pk_mul_f32 v[34:35], v[34:35], v[248:249] op_sel_hi:[1,0]\n"
    "v_cvt_pk_bf16_f32 v184, v184, v185\n"
    "v_cvt_pk_bf16_f32 v185, v186, v187\n"
    "ds_write_b64 v239, v[168:169] offset:12416\n"
    "v_pk_mul_f32 v[36:37], v[36:37], v[248:249] op_sel_hi:[1,0]\n"
    "v_pk_mul_f32 v[38:39], v[38:39], v[248:249] op_sel_hi:[1,0]\n"
    "v_cvt_pk_bf16_f32 v32, v32, v33\n"
    "v_cvt_pk_bf16_f32 v33, v34, v35\n"
    "ds_write_b64 v240, v[184:185] offset:12416\n"
    "v_pk_mul_f32 v[40:41], v[40:41], v[248:249] op_sel_hi:[1,0]\n"
    "v_pk_mul_f32 v[42:43], v[42:43], v[248:249] op_sel_hi:[1,0]\n"
    "v_cvt_pk_bf16_f32 v36, v36, v37\n"
    "v_cvt_pk_bf16_f32 v37, v38, v39\n"
    "ds_write_b64 v237, v[32:33] offset:12544\n"
    "v_pk_mul_f32 v[44:45], v[44:45], v[248:249] op_sel_hi:[1,0]\n"
    "v_pk_mul_f32 v[46:47], v[46:47], v[248:249] op_sel_hi:[1,0]\n"
    "v_cvt_pk_bf16_f32 v40, v40, v41\n"
    "v_cvt_pk_bf16_f32 v41, v42, v43\n"
    "ds_write_b64 v238, v[36:37] offset:12544\n"
    "v_cvt_pk_bf16_f32 v44, v44, v45\n"
    "v_cvt_pk_bf16_f32 v45, v46, v47\n"
    "ds_write_b64 v239, v[40:41] offset:12544\n"
    "ds_write_b64 v240, v[44:45] offset:12544\n"
    "v_rcp_f32 v248, v204\n"
    "v_cmp_nge_f32 vcc, s48, v204\n"
    "v_accvgpr_read_b32 v48, a204\n"
    "v_accvgpr_read_b32 v49, a205\n"
    "v_accvgpr_read_b32 v50, a206\n"
    "v_accvgpr_read_b32 v51, a207\n"
    "v_accvgpr_read_b32 v52, a220\n"
    "v_accvgpr_read_b32 v53, a221\n"
    "v_accvgpr_read_b32 v54, a222\n"
    "v_accvgpr_read_b32 v55, a223\n"
    "v_accvgpr_read_b32 v56, a236\n"
    "v_accvgpr_read_b32 v57, a237\n"
    "v_accvgpr_read_b32 v58, a238\n"
    "v_accvgpr_read_b32 v59, a239\n"
    "v_accvgpr_read_b32 v60, a252\n"
    "v_accvgpr_read_b32 v61, a253\n"
    "v_accvgpr_read_b32 v62, a254\n"
    "v_accvgpr_read_b32 v63, a255\n"
    "s_or_b64 s[46:47], s[46:47], vcc\n"
    "v_pk_mul_f32 v[76:77], v[76:77], v[248:249] op_sel_hi:[1,0]\n"
    "v_pk_mul_f32 v[78:79], v[78:79], v[248:249] op_sel_hi:[1,0]\n"
    "v_pk_mul_f32 v[92:93], v[92:93], v[248:249] op_sel_hi:[1,0]\n"
    "v_pk_mul_f32 v[94:95], v[94:95], v[248:249] op_sel_hi:[1,0]\n"
    "v_cvt_pk_bf16_f32 v76, v76, v77\n"
    "v_cvt_pk_bf16_f32 v77, v78, v79\n"
    "v_pk_mul_f32 v[108:109], v[108:109], v[248:249] op_sel_hi:[1,0]\n"
    "v_pk_mul_f32 v[110:111], v[110:111], v[248:249] op_sel_hi:[1,0]\n"
    "v_cvt_pk_bf16_f32 v92, v92, v93\n"
    "v_cvt_pk_bf16_f32 v93, v94, v95\n"
    "ds_write_b64 v237, v[76:77] offset:18432\n"
    "v_pk_mul_f32 v[124:125], v[124:125], v[248:249] op_sel_hi:[1,0]\n"
    "v_pk_mul_f32 v[126:127], v[126:127], v[248:249] op_sel_hi:[1,0]\n"
    "v_cvt_pk_bf16_f32 v108, v108, v109\n"
    "v_cvt_pk_bf16_f32 v109, v110, v111\n"
    "ds_write_b64 v238, v[92:93] offset:18432\n"
    "v_pk_mul_f32 v[140:141], v[140:141], v[248:249] op_sel_hi:[1,0]\n"
    "v_pk_mul_f32 v[142:143], v[142:143], v[248:249] op_sel_hi:[1,0]\n"
    "v_cvt_pk_bf16_f32 v124, v124, v125\n"
    "v_cvt_pk_bf16_f32 v125, v126, v127\n"
    "ds_write_b64 v239, v[108:109] offset:18432\n"
    "v_pk_mul_f32 v[156:157], v[156:157], v[248:249] op_sel_hi:[1,0]\n"
    "v_pk_mul_f32 v[158:159], v[158:159], v[248:249] op_sel_hi:[1,0]\n"
    "v_cvt_pk_bf16_f32 v140, v140, v141\n"
    "v_cvt_pk_bf16_f32 v141, v142, v143\n"
    "ds_write_b64 v240, v[124:125] offset:18432\n"
    "v_pk_mul_f32 v[172:173], v[172:173], v[248:249] op_sel_hi:[1,0]\n"
    "v_pk_mul_f32 v[174:175], v[174:175], v[248:249] op_sel_hi:[1,0]\n"
    "v_cvt_pk_bf16_f32 v156, v156, v157\n"
    "v_cvt_pk_bf16_f32 v157, v158, v159\n"
    "ds_write_b64 v237, v[140:141] offset:18560\n"
    "v_pk_mul_f32 v[188:189], v[188:189], v[248:249] op_sel_hi:[1,0]\n"
    "v_pk_mul_f32 v[190:191], v[190:191], v[248:249] op_sel_hi:[1,0]\n"
    "v_cvt_pk_bf16_f32 v172, v172, v173\n"
    "v_cvt_pk_bf16_f32 v173, v174, v175\n"
    "ds_write_b64 v238, v[156:157] offset:18560\n"
    "v_pk_mul_f32 v[48:49], v[48:49], v[248:249] op_sel_hi:[1,0]\n"
    "v_pk_mul_f32 v[50:51], v[50:51], v[248:249] op_sel_hi:[1,0]\n"
    "v_cvt_pk_bf16_f32 v188, v188, v189\n"
    "v_cvt_pk_bf16_f32 v189, v190, v191\n"
    "ds_write_b64 v239, v[172:173] offset:18560\n"
    "v_pk_mul_f32 v[52:53], v[52:53], v[248:249] op_sel_hi:[1,0]\n"
    "v_pk_mul_f32 v[54:55], v[54:55], v[248:249] op_sel_hi:[1,0]\n"
    "v_cvt_pk_bf16_f32 v48, v48, v49\n"
    "v_cvt_pk_bf16_f32 v49, v50, v51\n"
    "ds_write_b64 v240, v[188:189] offset:18560\n"
    "v_pk_mul_f32 v[56:57], v[56:57], v[248:249] op_sel_hi:[1,0]\n"
    "v_pk_mul_f32 v[58:59], v[58:59], v[248:249] op_sel_hi:[1,0]\n"
    "v_cvt_pk_bf16_f32 v52, v52, v53\n"
    "v_cvt_pk_bf16_f32 v53, v54, v55\n"
    "ds_write_b64 v237, v[48:49] offset:18688\n"
    "v_pk_mul_f32 v[60:61], v[60:61], v[248:249] op_sel_hi:[1,0]\n"
    "v_pk_mul_f32 v[62:63], v[62:63], v[248:249] op_sel_hi:[1,0]\n"
    "v_cvt_pk_bf16_f32 v56, v56, v57\n"
    "v_cvt_pk_bf16_f32 v57, v58, v59\n"
    "ds_write_b64 v238, v[52:53] offset:18688\n"
    "v_cvt_pk_bf16_f32 v60, v60, v61\n"
    "v_cvt_pk_bf16_f32 v61, v62, v63\n"
    "ds_write_b64 v239, v[56:57] offset:18688\n"
    "ds_write_b64 v240, v[60:61] offset:18688\n"
    "s_waitcnt lgkmcnt(0)\n"
    "s_cmp_lg_u64 s[46:47], 0\n"
    "s_cselect_b32 s50, 1, 0\n"
    "s_or_b32 s45, s45, s50\n"
    "s_mov_b64 s[46:47], 0\n"
    "L_ph34:\n"
    "v_mfma_f32_16x16x32_bf16 v[32:35], a[48:51], a[96:99], 0\n"
    "ds_read_b64_tr_b16 a[0:1], v218 offset:49152\n"
    "s_add_u32 m0, s37, 49152\n"
    "v_mfma_f32_16x16x32_bf16 v[32:35], a[52:55], a[100:103], v[32:35]\n"
    "ds_read_b64_tr_b16 a[2:3], v218 offset:55296\n"
    "s_add_u32 s42, s39, 49152\n"
    "v_mfma_f32_16x16x32_bf16 v[32:35], a[56:59], a[104:107], v[32:35]\n"
    "ds_read_b64_tr_b16 a[4:5], v219 offset:49152\n"
    "v_fma_f32 v20, v20, s36, v214\n"
    "v_mfma_f32_16x16x32_bf16 v[32:35], a[60:63], a[108:111], v[32:35]\n"
    "ds_read_b64_tr_b16 a[6:7], v219 offset:55296\n"
    "v_fma_f32 v21, v21, s36, v214\n"
    "v_mfma_f32_16x16x32_bf16 v[32:35], a[64:67], a[112:115], v[32:35]\n"
    "ds_read_b64_tr_b16 a[8:9], v220 offset:49152\n"
    "v_fma_f32 v22, v22, s36, v214\n"
    "v_mfma_f32_16x16x32_bf16 v[32:35], a[68:71], a[116:119], v[32:35]\n"
    "ds_read_b64_tr_b16 a[10:11], v220 offset:55296\n"
    "v_fma_f32 v23, v23, s36, v214\n"
    "v_mfma_f32_16x16x32_bf16 v[36:39], a[72:75], a[96:99], 0\n"
    "ds_read_b64_tr_b16 a[12:13], v221 offset:49152\n"
    "v_cvt_pk_bf16_f32 v16, v16, v17\n"
    "v_mfma_f32_16x16x32_bf16 v[36:39], a[76:79], a[100:103], v[36:39]\n"
    "ds_read_b64_tr_b16 a[14:15], v221 offset:55296\n"
    "v_cvt_pk_bf16_f32 v17, v18, v19\n"
    "v_mfma_f32_16x16x32_bf16 v[36:39], a[80:83], a[104:107], v[36:39]\n"
    "ds_read_b64_tr_b16 a[16:17], v218 offset:49280\n"
    "v_permlane32_swap_b32 v252, v253\n"
    "v_mfma_f32_16x16x32_bf16 v[36:39], a[84:87], a[108:111], v[36:39]\n"
    "ds_read_b64_tr_b16 a[18:19], v218 offset:55424\n"
    "v_max_f32 v252, v252, v253\n"
    "v_mfma_f32_16x16x32_bf16 v[36:39], a[88:91], a[112:115], v[36:39]\n"
    "ds_read_b64_tr_b16 a[20:21], v219 offset:49280\n"
    "v_mul_f32 v215, s49, v252\n"
    "v_mfma_f32_16x16x32_bf16 v[36:39], a[92:95], a[116:119], v[36:39]\n"
    "ds_read_b64_tr_b16 a[22:23], v219 offset:55424\n"
    "v_fma_f32 v24, v24, s36, v215\n"
    "v_mfma_f32_16x16x32_bf16 v[40:43], a[48:51], a[120:123], 0\n"
    "ds_read_b64_tr_b16 a[24:25], v220 offset:49280\n"
    "v_fma_f32 v25, v25, s36, v215\n"
    "v_mfma_f32_16x16x32_bf16 v[40:43], a[52:55], a[124:127], v[40:43]\n"
    "ds_read_b64_tr_b16 a[26:27], v220 offset:55424\n"
    "v_fma_f32 v26, v26, s36, v215\n"
    "v_mfma_f32_16x16x32_bf16 v[40:43], a[56:59], a[128:131], v[40:43]\n"
    "ds_read_b64_tr_b16 a[28:29], v221 offset:49280\n"
    "v_fma_f32 v27, v27, s36, v215\n"
    "v_mfma_f32_16x16x32_bf16 v[40:43], a[60:63], a[132:135], v[40:43]\n"
    "ds_read_b64_tr_b16 a[30:31], v221 offset:55424\n"
    "v_fma_f32 v28, v28, s36, v215\n"
    "v_mfma_f32_16x16x32_bf16 v[40:43], a[64:67], a[136:139], v[40:43]\n"
    "ds_read_b64_tr_b16 a[32:33], v218 offset:49408\n"
    "v_fma_f32 v29, v29, s36, v215\n"
    "v_mfma_f32_16x16x32_bf16 v[40:43], a[68:71], a[140:143], v[40:43]\n"
    "ds_read_b64_tr_b16 a[34:35], v218 offset:55552\n"
    "v_fma_f32 v30, v30, s36, v215\n"
    "v_mfma_f32_16x16x32_bf16 v[44:47], a[72:75], a[120:123], 0\n"
    "ds_read_b64_tr_b16 a[36:37], v219 offset:49408\n"
    "v_fma_f32 v31, v31, s36, v215\n"
    "v_mfma_f32_16x16x32_bf16 v[44:47], a[76:79], a[124:127], v[44:47]\n"
    "ds_read_b64_tr_b16 a[38:39], v219 offset:55552\n"
    "v_fma_f32 v32, v32, s36, v212\n"
    "v_mfma_f32_16x16x32_bf16 v[44:47], a[80:83], a[128:131], v[44:47]\n"
    "ds_read_b64_tr_b16 a[40:41], v220 offset:49408\n"
    "v_fma_f32 v33, v33, s36, v212\n"
    "v_mfma_f32_16x16x32_bf16 v[44:47], a[84:87], a[132:135], v[44:47]\n"
    "ds_read_b64_tr_b16 a[42:43], v220 offset:55552\n"
    "v_fma_f32 v34, v34, s36, v212\n"
    "v_mfma_f32_16x16x32_bf16 v[44:47], a[88:91], a[136:139], v[44:47]\n"
    "ds_read_b64_tr_b16 a[44:45], v221 offset:49408\n"
    "v_fma_f32 v35, v35, s36, v212\n"
    "v_mfma_f32_16x16x32_bf16 v[44:47], a[92:95], a[140:143], v[44:47]\n"
    "ds_read_b64_tr_b16 a[46:47], v221 offset:55552\n"
    "v_fma_f32 v36, v36, s36, v212\n"
    "v_mfma_f32_16x16x32_bf16 v[48:51], a[48:51], a[144:147], 0\n"
    "v_exp_f32 v20, v20\n"
    "v_mfma_f32_16x16x32_bf16 v[48:51], a[52:55], a[148:151], v[48:51]\n"
    "v_exp_f32 v21, v21\n"
    "v_mfma_f32_16x16x32_bf16 v[48:51], a[56:59], a[152:155], v[48:51]\n"
    "v_exp_f32 v22, v22\n"
    "v_mfma_f32_16x16x32_bf16 v[48:51], a[60:63], a[156:159], v[48:51]\n"
    "v_exp_f32 v23, v23\n"
    "v_mfma_f32_16x16x32_bf16 v[48:51], a[64:67], a[160:163], v[48:51]\n"
    "v_cvt_pk_bf16_f32 v18, v20, v21\n"
    "v_cvt_pk_bf16_f32 v19, v22, v23\n"
    "v_mfma_f32_16x16x32_bf16 v[48:51], a[68:71], a[164:167], v[48:51]\n"
    "v_exp_f32 v24, v24\n"
    "v_mfma_f32_16x16x32_bf16 v[52:55], a[72:75], a[144:147], 0\n"
    "v_exp_f32 v25, v25\n"
    "v_mfma_f32_16x16x32_bf16 v[52:55], a[76:79], a[148:151], v[52:55]\n"
    "v_exp_f32 v26, v26\n"
    "v_mfma_f32_16x16x32_bf16 v[52:55], a[80:83], a[152:155], v[52:55]\n"
    "v_exp_f32 v27, v27\n"
    "v_mfma_f32_16x16x32_bf16 v[52:55], a[84:87], a[156:159], v[52:55]\n"
    "v_exp_f32 v28, v28\n"
    "v_mfma_f32_16x16x32_bf16 v[52:55], a[88:91], a[160:163], v[52:55]\n"
    "v_exp_f32 v29, v29\n"
    "v_mfma_f32_16x16x32_bf16 v[52:55], a[92:95], a[164:167], v[52:55]\n"
    "v_exp_f32 v30, v30\n"
    "v_mfma_f32_16x16x32_bf16 v[56:59], a[48:51], a[168:171], 0\n"
    "v_exp_f32 v31, v31\n"
    "v_mfma_f32_16x16x32_bf16 v[56:59], a[52:55], a[172:175], v[56:59]\n"
    "v_cvt_pk_bf16_f32 v24, v24, v25\n"
    "v_cvt_pk_bf16_f32 v25, v26, v27\n"
    "v_mfma_f32_16x16x32_bf16 v[56:59], a[56:59], a[176:179], v[56:59]\n"
    "v_cvt_pk_bf16_f32 v26, v28, v29\n"
    "v_cvt_pk_bf16_f32 v27, v30, v31\n"
    "v_mfma_f32_16x16x32_bf16 v[56:59], a[60:63], a[180:183], v[56:59]\n"
    "v_exp_f32 v32, v32\n"
    "v_mfma_f32_16x16x32_bf16 v[56:59], a[64:67], a[184:187], v[56:59]\n"
    "v_exp_f32 v33, v33\n"
    "v_mfma_f32_16x16x32_bf16 v[56:59], a[68:71], a[188:191], v[56:59]\n"
    "v_exp_f32 v34, v34\n"
    "v_mfma_f32_16x16x32_bf16 v[60:63], a[72:75], a[168:171], 0\n"
    "v_exp_f32 v35, v35\n"
    "v_mfma_f32_16x16x32_bf16 v[60:63], a[76:79], a[172:175], v[60:63]\n"
    "v_exp_f32 v36, v36\n"
    "v_mfma_f32_16x16x32_bf16 v[60:63], a[80:83], a[176:179], v[60:63]\n"
    "v_fma_f32 v37, v37, s36, v212\n"
    "v_fma_f32 v38, v38, s36, v212\n"
    "v_mfma_f32_16x16x32_bf16 v[60:63], a[84:87], a[180:183], v[60:63]\n"
    "v_exp_f32 v37, v37\n"
    "v_mfma_f32_16x16x32_bf16 v[60:63], a[88:91], a[184:187], v[60:63]\n"
    "v_exp_f32 v38, v38\n"
    "v_mfma_f32_16x16x32_bf16 v[60:63], a[92:95], a[188:191], v[60:63]\n"
    "s_waitcnt vmcnt(10) lgkmcnt(0)\n"
    "s_barrier\n"
    "L_ph35:\n"
    "v_mfma_f32_16x16x32_bf16 v[64:67], a[0:3], v[0:3], 0\n"
    "buffer_load_dwordx4 v222, s[20:23], s42 offen nt lds\n"
    "v_mfma_f32_16x16x32_bf16 v[80:83], a[4:7], v[0:3], 0\n"
    "ds_read_b128 a[48:51], v216 offset:0\n"
    "v_fma_f32 v39, v39, s36, v212\n"
    "v_mfma_f32_16x16x32_bf16 v[96:99], a[8:11], v[0:3], 0\n"
    "buffer_load_dwordx4 v223, s[20:23], s42 offen offset:1024 nt lds\n"
    "v_mfma_f32_16x16x32_bf16 v[112:115], a[12:15], v[0:3], 0\n"
    "ds_read_b128 a[52:55], v217 offset:0\n"
    "v_cvt_pk_bf16_f32 v32, v32, v33\n"
    "v_mfma_f32_16x16x32_bf16 v[128:131], a[16:19], v[0:3], 0\n"
    "buffer_load_dwordx4 v224, s[20:23], s42 offen offset:2048 nt lds\n"
    "v_mfma_f32_16x16x32_bf16 v[144:147], a[20:23], v[0:3], 0\n"
    "s_mov_b32 m0, 159744\n"
    "s_mov_b32 s51, L_ph39-L_code_start\n"
    "v_mfma_f32_16x16x32_bf16 v[160:163], a[24:27], v[0:3], 0\n"
    "buffer_load_dword v249, s[52:55], s51 offen lds\n"
    "v_mfma_f32_16x16x32_bf16 v[176:179], a[28:31], v[0:3], 0\n"
    "ds_read_b128 a[56:59], v216 offset:128\n"
    "s_add_u32 m0, s37, 0\n"
    "v_mfma_f32_16x16x32_bf16 a[192:195], a[32:35], v[0:3], 0\n"
    "ds_read_b128 a[60:63], v217 offset:128\n"
    "s_add_u32 s42, s39, 36864\n"
    "v_mfma_f32_16x16x32_bf16 a[208:211], a[36:39], v[0:3], 0\n"
    "ds_read_b128 a[64:67], v216 offset:256\n"
    "v_cvt_pk_bf16_f32 v33, v34, v35\n"
    "v_mfma_f32_16x16x32_bf16 a[224:227], a[40:43], v[0:3], 0\n"
    "ds_read_b128 a[68:71], v217 offset:256\n"
    "v_cvt_pk_bf16_f32 v34, v36, v37\n"
    "v_mfma_f32_16x16x32_bf16 a[240:243], a[44:47], v[0:3], 0\n"
    "ds_read_b128 a[72:75], v216 offset:6144\n"
    "v_fma_f32 v40, v40, s36, v213\n"
    "v_mfma_f32_16x16x32_bf16 v[192:195], v[208:211], v[0:3], 0\n"
    "ds_read_b128 a[76:79], v217 offset:6144\n"
    "v_fma_f32 v41, v41, s36, v213\n"
    "v_mfma_f32_16x16x32_bf16 v[68:71], a[0:3], v[8:11], 0\n"
    "ds_read_b128 a[80:83], v216 offset:6272\n"
    "v_fma_f32 v42, v42, s36, v213\n"
    "v_mfma_f32_16x16x32_bf16 v[84:87], a[4:7], v[8:11], 0\n"
    "ds_read_b128 a[84:87], v217 offset:6272\n"
    "v_fma_f32 v43, v43, s36, v213\n"
    "v_mfma_f32_16x16x32_bf16 v[100:103], a[8:11], v[8:11], 0\n"
    "ds_read_b128 a[88:91], v216 offset:6400\n"
    "v_fma_f32 v44, v44, s36, v213\n"
    "v_mfma_f32_16x16x32_bf16 v[116:119], a[12:15], v[8:11], 0\n"
    "ds_read_b128 a[92:95], v217 offset:6400\n"
    "v_fma_f32 v45, v45, s36, v213\n"
    "v_mfma_f32_16x16x32_bf16 v[132:135], a[16:19], v[8:11], 0\n"
    "v_exp_f32 v39, v39\n"
    "v_mfma_f32_16x16x32_bf16 v[148:151], a[20:23], v[8:11], 0\n"
    "v_cvt_pk_bf16_f32 v35, v38, v39\n"
    "v_fma_f32 v46, v46, s36, v213\n"
    "v_mfma_f32_16x16x32_bf16 v[164:167], a[24:27], v[8:11], 0\n"
    "v_exp_f32 v40, v40\n"
    "v_mfma_f32_16x16x32_bf16 v[180:183], a[28:31], v[8:11], 0\n"
    "v_exp_f32 v41, v41\n"
    "v_mfma_f32_16x16x32_bf16 a[196:199], a[32:35], v[8:11], 0\n"
    "v_exp_f32 v42, v42\n"
    "v_mfma_f32_16x16x32_bf16 a[212:215], a[36:39], v[8:11], 0\n"
    "v_exp_f32 v43, v43\n"
    "v_mfma_f32_16x16x32_bf16 a[228:231], a[40:43], v[8:11], 0\n"
    "v_exp_f32 v44, v44\n"
    "v_mfma_f32_16x16x32_bf16 a[244:247], a[44:47], v[8:11], 0\n"
    "v_exp_f32 v45, v45\n"
    "v_mfma_f32_16x16x32_bf16 v[196:199], v[208:211], v[8:11], 0\n"
    "v_exp_f32 v46, v46\n"
    "v_mfma_f32_16x16x32_bf16 v[72:75], a[0:3], v[16:19], 0\n"
    "v_fma_f32 v47, v47, s36, v213\n"
    "v_cvt_pk_bf16_f32 v40, v40, v41\n"
    "v_mfma_f32_16x16x32_bf16 v[88:91], a[4:7], v[16:19], 0\n"
    "v_exp_f32 v47, v47\n"
    "v_mfma_f32_16x16x32_bf16 v[104:107], a[8:11], v[16:19], 0\n"
    "v_cvt_pk_bf16_f32 v41, v42, v43\n"
    "v_cvt_pk_bf16_f32 v42, v44, v45\n"
    "v_mfma_f32_16x16x32_bf16 v[120:123], a[12:15], v[16:19], 0\n"
    "v_cvt_pk_bf16_f32 v43, v46, v47\n"
    "v_fma_f32 v48, v48, s36, v214\n"
    "v_mfma_f32_16x16x32_bf16 v[136:139], a[16:19], v[16:19], 0\n"
    "v_exp_f32 v48, v48\n"
    "v_mfma_f32_16x16x32_bf16 v[152:155], a[20:23], v[16:19], 0\n"
    "v_fma_f32 v49, v49, s36, v214\n"
    "v_fma_f32 v50, v50, s36, v214\n"
    "v_mfma_f32_16x16x32_bf16 v[168:171], a[24:27], v[16:19], 0\n"
    "v_exp_f32 v49, v49\n"
    "v_mfma_f32_16x16x32_bf16 v[184:187], a[28:31], v[16:19], 0\n"
    "v_exp_f32 v50, v50\n"
    "v_mfma_f32_16x16x32_bf16 a[200:203], a[32:35], v[16:19], 0\n"
    "v_fma_f32 v51, v51, s36, v214\n"
    "v_fma_f32 v52, v52, s36, v214\n"
    "v_mfma_f32_16x16x32_bf16 a[216:219], a[36:39], v[16:19], 0\n"
    "v_exp_f32 v51, v51\n"
    "v_mfma_f32_16x16x32_bf16 a[232:235], a[40:43], v[16:19], 0\n"
    "v_exp_f32 v52, v52\n"
    "v_mfma_f32_16x16x32_bf16 a[248:251], a[44:47], v[16:19], 0\n"
    "v_fma_f32 v53, v53, s36, v214\n"
    "v_fma_f32 v54, v54, s36, v214\n"
    "v_mfma_f32_16x16x32_bf16 v[200:203], v[208:211], v[16:19], 0\n"
    "v_exp_f32 v53, v53\n"
    "v_mfma_f32_16x16x32_bf16 v[76:79], a[0:3], v[24:27], 0\n"
    "v_exp_f32 v54, v54\n"
    "v_mfma_f32_16x16x32_bf16 v[92:95], a[4:7], v[24:27], 0\n"
    "v_fma_f32 v55, v55, s36, v214\n"
    "v_cvt_pk_bf16_f32 v48, v48, v49\n"
    "v_mfma_f32_16x16x32_bf16 v[108:111], a[8:11], v[24:27], 0\n"
    "v_exp_f32 v55, v55\n"
    "v_mfma_f32_16x16x32_bf16 v[124:127], a[12:15], v[24:27], 0\n"
    "v_cvt_pk_bf16_f32 v49, v50, v51\n"
    "v_cvt_pk_bf16_f32 v50, v52, v53\n"
    "v_mfma_f32_16x16x32_bf16 v[140:143], a[16:19], v[24:27], 0\n"
    "v_cvt_pk_bf16_f32 v51, v54, v55\n"
    "v_fma_f32 v56, v56, s36, v215\n"
    "v_mfma_f32_16x16x32_bf16 v[156:159], a[20:23], v[24:27], 0\n"
    "v_exp_f32 v56, v56\n"
    "v_mfma_f32_16x16x32_bf16 v[172:175], a[24:27], v[24:27], 0\n"
    "v_fma_f32 v57, v57, s36, v215\n"
    "v_fma_f32 v58, v58, s36, v215\n"
    "v_mfma_f32_16x16x32_bf16 v[188:191], a[28:31], v[24:27], 0\n"
    "v_exp_f32 v57, v57\n"
    "v_mfma_f32_16x16x32_bf16 a[204:207], a[32:35], v[24:27], 0\n"
    "v_exp_f32 v58, v58\n"
    "v_mfma_f32_16x16x32_bf16 a[220:223], a[36:39], v[24:27], 0\n"
    "v_fma_f32 v59, v59, s36, v215\n"
    "v_fma_f32 v60, v60, s36, v215\n"
    "v_mfma_f32_16x16x32_bf16 a[236:239], a[40:43], v[24:27], 0\n"
    "v_exp_f32 v59, v59\n"
    "v_mfma_f32_16x16x32_bf16 a[252:255], a[44:47], v[24:27], 0\n"
    "v_exp_f32 v60, v60\n"
    "v_mfma_f32_16x16x32_bf16 v[204:207], v[208:211], v[24:27], 0\n"
    "s_waitcnt vmcnt(10) lgkmcnt(0)\n"
    "s_barrier\n"
    "L_ph36:\n"
    "v_mfma_f32_16x16x32_bf16 v[0:3], a[48:51], a[96:99], 0\n"
    "buffer_load_dwordx4 v231, s[24:27], s42 offen nt lds\n"
    "v_mfma_f32_16x16x32_bf16 v[0:3], a[52:55], a[100:103], v[0:3]\n"
    "ds_read_b64_tr_b16 a[0:1], v218 offset:12288\n"
    "v_fma_f32 v61, v61, s36, v215\n"
    "v_mfma_f32_16x16x32_bf16 v[0:3], a[56:59], a[104:107], v[0:3]\n"
    "buffer_load_dwordx4 v232, s[24:27], s42 offen offset:1024 nt lds\n"
    "v_mfma_f32_16x16x32_bf16 v[0:3], a[60:63], a[108:111], v[0:3]\n"
    "ds_read_b64_tr_b16 a[2:3], v218 offset:18432\n"
    "v_fma_f32 v62, v62, s36, v215\n"
    "v_mfma_f32_16x16x32_bf16 v[0:3], a[64:67], a[112:115], v[0:3]\n"
    "buffer_load_dwordx4 v233, s[24:27], s42 offen offset:2048 nt lds\n"
    "v_mfma_f32_16x16x32_bf16 v[0:3], a[68:71], a[116:119], v[0:3]\n"
    "ds_read_b64_tr_b16 a[4:5], v219 offset:12288\n"
    "s_add_u32 m0, s37, 12288\n"
    "v_mfma_f32_16x16x32_bf16 v[4:7], a[72:75], a[96:99], 0\n"
    "ds_read_b64_tr_b16 a[6:7], v219 offset:18432\n"
    "s_add_u32 s42, s39, 61440\n"
    "v_mfma_f32_16x16x32_bf16 v[4:7], a[76:79], a[100:103], v[4:7]\n"
    "ds_read_b64_tr_b16 a[8:9], v220 offset:12288\n"
    "v_fma_f32 v63, v63, s36, v215\n"
    "v_mfma_f32_16x16x32_bf16 v[4:7], a[80:83], a[104:107], v[4:7]\n"
    "ds_read_b64_tr_b16 a[10:11], v220 offset:18432\n"
    "v_cvt_pk_bf16_f32 v56, v56, v57\n"
    "v_mfma_f32_16x16x32_bf16 v[4:7], a[84:87], a[108:111], v[4:7]\n"
    "ds_read_b64_tr_b16 a[12:13], v221 offset:12288\n"
    "v_cvt_pk_bf16_f32 v57, v58, v59\n"
    "v_mfma_f32_16x16x32_bf16 v[4:7], a[88:91], a[112:115], v[4:7]\n"
    "ds_read_b64_tr_b16 a[14:15], v221 offset:18432\n"
    "v_mfma_f32_16x16x32_bf16 v[4:7], a[92:95], a[116:119], v[4:7]\n"
    "ds_read_b64_tr_b16 a[16:17], v218 offset:12416\n"
    "v_mfma_f32_16x16x32_bf16 v[8:11], a[48:51], a[120:123], 0\n"
    "ds_read_b64_tr_b16 a[18:19], v218 offset:18560\n"
    "v_mfma_f32_16x16x32_bf16 v[8:11], a[52:55], a[124:127], v[8:11]\n"
    "ds_read_b64_tr_b16 a[20:21], v219 offset:12416\n"
    "v_fma_f32 v0, v0, s36, v212\n"
    "v_mfma_f32_16x16x32_bf16 v[8:11], a[56:59], a[128:131], v[8:11]\n"
    "ds_read_b64_tr_b16 a[22:23], v219 offset:18560\n"
    "v_fma_f32 v1, v1, s36, v212\n"
    "v_mfma_f32_16x16x32_bf16 v[8:11], a[60:63], a[132:135], v[8:11]\n"
    "ds_read_b64_tr_b16 a[24:25], v220 offset:12416\n"
    "v_fma_f32 v2, v2, s36, v212\n"
    "v_mfma_f32_16x16x32_bf16 v[8:11], a[64:67], a[136:139], v[8:11]\n"
    "ds_read_b64_tr_b16 a[26:27], v220 offset:18560\n"
    "v_fma_f32 v3, v3, s36, v212\n"
    "v_mfma_f32_16x16x32_bf16 v[8:11], a[68:71], a[140:143], v[8:11]\n"
    "ds_read_b64_tr_b16 a[28:29], v221 offset:12416\n"
    "v_mfma_f32_16x16x32_bf16 v[12:15], a[72:75], a[120:123], 0\n"
    "ds_read_b64_tr_b16 a[30:31], v221 offset:18560\n"
    "v_mfma_f32_16x16x32_bf16 v[12:15], a[76:79], a[124:127], v[12:15]\n"
    "ds_read_b64_tr_b16 a[32:33], v218 offset:12544\n"
    "v_fma_f32 v4, v4, s36, v212\n"
    "v_mfma_f32_16x16x32_bf16 v[12:15], a[80:83], a[128:131], v[12:15]\n"
    "ds_read_b64_tr_b16 a[34:35], v218 offset:18688\n"
    "v_fma_f32 v5, v5, s36, v212\n"
    "v_mfma_f32_16x16x32_bf16 v[12:15], a[84:87], a[132:135], v[12:15]\n"
    "ds_read_b64_tr_b16 a[36:37], v219 offset:12544\n"
    "v_fma_f32 v6, v6, s36, v212\n"
    "v_mfma_f32_16x16x32_bf16 v[12:15], a[88:91], a[136:139], v[12:15]\n"
    "ds_read_b64_tr_b16 a[38:39], v219 offset:18688\n"
    "v_fma_f32 v7, v7, s36, v212\n"
    "v_mfma_f32_16x16x32_bf16 v[12:15], a[92:95], a[140:143], v[12:15]\n"
    "ds_read_b64_tr_b16 a[40:41], v220 offset:12544\n"
    "v_mfma_f32_16x16x32_bf16 v[16:19], a[48:51], a[144:147], 0\n"
    "ds_read_b64_tr_b16 a[42:43], v220 offset:18688\n"
    "v_mfma_f32_16x16x32_bf16 v[16:19], a[52:55], a[148:151], v[16:19]\n"
    "ds_read_b64_tr_b16 a[44:45], v221 offset:12544\n"
    "v_fma_f32 v8, v8, s36, v213\n"
    "v_mfma_f32_16x16x32_bf16 v[16:19], a[56:59], a[152:155], v[16:19]\n"
    "ds_read_b64_tr_b16 a[46:47], v221 offset:18688\n"
    "v_fma_f32 v9, v9, s36, v213\n"
    "v_mfma_f32_16x16x32_bf16 v[16:19], a[60:63], a[156:159], v[16:19]\n"
    "ds_read_b128 v[250:253], v241 offset:0\n"
    "v_fma_f32 v10, v10, s36, v213\n"
    "v_mfma_f32_16x16x32_bf16 v[16:19], a[64:67], a[160:163], v[16:19]\n"
    "v_exp_f32 v61, v61\n"
    "v_mfma_f32_16x16x32_bf16 v[16:19], a[68:71], a[164:167], v[16:19]\n"
    "v_exp_f32 v62, v62\n"
    "v_mfma_f32_16x16x32_bf16 v[20:23], a[72:75], a[144:147], 0\n"
    "v_exp_f32 v63, v63\n"
    "v_mfma_f32_16x16x32_bf16 v[20:23], a[76:79], a[148:151], v[20:23]\n"
    "v_cvt_pk_bf16_f32 v58, v60, v61\n"
    "v_cvt_pk_bf16_f32 v59, v62, v63\n"
    "v_mfma_f32_16x16x32_bf16 v[20:23], a[80:83], a[152:155], v[20:23]\n"
    "v_exp_f32 v0, v0\n"
    "v_mfma_f32_16x16x32_bf16 v[20:23], a[84:87], a[156:159], v[20:23]\n"
    "v_exp_f32 v1, v1\n"
    "v_mfma_f32_16x16x32_bf16 v[20:23], a[88:91], a[160:163], v[20:23]\n"
    "v_exp_f32 v2, v2\n"
    "v_mfma_f32_16x16x32_bf16 v[20:23], a[92:95], a[164:167], v[20:23]\n"
    "v_exp_f32 v3, v3\n"
    "v_mfma_f32_16x16x32_bf16 v[24:27], a[48:51], a[168:171], 0\n"
    "v_exp_f32 v4, v4\n"
    "v_mfma_f32_16x16x32_bf16 v[24:27], a[52:55], a[172:175], v[24:27]\n"
    "v_exp_f32 v5, v5\n"
    "v_mfma_f32_16x16x32_bf16 v[24:27], a[56:59], a[176:179], v[24:27]\n"
    "v_exp_f32 v6, v6\n"
    "v_mfma_f32_16x16x32_bf16 v[24:27], a[60:63], a[180:183], v[24:27]\n"
    "v_exp_f32 v7, v7\n"
    "v_mfma_f32_16x16x32_bf16 v[24:27], a[64:67], a[184:187], v[24:27]\n"
    "v_cvt_pk_bf16_f32 v0, v0, v1\n"
    "v_cvt_pk_bf16_f32 v1, v2, v3\n"
    "v_mfma_f32_16x16x32_bf16 v[24:27], a[68:71], a[188:191], v[24:27]\n"
    "v_cvt_pk_bf16_f32 v2, v4, v5\n"
    "v_cvt_pk_bf16_f32 v3, v6, v7\n"
    "v_mfma_f32_16x16x32_bf16 v[28:31], a[72:75], a[168:171], 0\n"
    "v_exp_f32 v8, v8\n"
    "v_mfma_f32_16x16x32_bf16 v[28:31], a[76:79], a[172:175], v[28:31]\n"
    "v_exp_f32 v9, v9\n"
    "v_mfma_f32_16x16x32_bf16 v[28:31], a[80:83], a[176:179], v[28:31]\n"
    "v_exp_f32 v10, v10\n"
    "v_mfma_f32_16x16x32_bf16 v[28:31], a[84:87], a[180:183], v[28:31]\n"
    "v_fma_f32 v11, v11, s36, v213\n"
    "v_fma_f32 v12, v12, s36, v213\n"
    "v_mfma_f32_16x16x32_bf16 v[28:31], a[88:91], a[184:187], v[28:31]\n"
    "v_exp_f32 v11, v11\n"
    "v_mfma_f32_16x16x32_bf16 v[28:31], a[92:95], a[188:191], v[28:31]\n"
    "s_waitcnt vmcnt(10) lgkmcnt(0)\n"
    "s_barrier\n"
    "L_ph37:\n"
    "v_mfma_f32_16x16x32_bf16 v[64:67], a[0:3], v[32:35], v[64:67]\n"
    "buffer_load_dwordx4 v222, s[20:23], s42 offen nt lds\n"
    "v_mfma_f32_16x16x32_bf16 v[80:83], a[4:7], v[32:35], v[80:83]\n"
    "ds_read_b128 a[48:51], v216 offset:24576\n"
    "v_fma_f32 v13, v13, s36, v213\n"
    "v_mfma_f32_16x16x32_bf16 v[96:99], a[8:11], v[32:35], v[96:99]\n"
    "buffer_load_dwordx4 v223, s[20:23], s42 offen offset:1024 nt lds\n"
    "v_mfma_f32_16x16x32_bf16 v[112:115], a[12:15], v[32:35], v[112:115]\n"
    "ds_read_b128 a[52:55], v217 offset:24576\n"
    "v_fma_f32 v14, v14, s36, v213\n"
    "v_mfma_f32_16x16x32_bf16 v[128:131], a[16:19], v[32:35], v[128:131]\n"
    "buffer_load_dwordx4 v224, s[20:23], s42 offen offset:2048 nt lds\n"
    "v_mfma_f32_16x16x32_bf16 v[144:147], a[20:23], v[32:35], v[144:147]\n"
    "ds_read_b128 a[56:59], v216 offset:24704\n"
    "s_add_u32 s43, s40, 0\n"
    "v_mfma_f32_16x16x32_bf16 v[160:163], a[24:27], v[32:35], v[160:163]\n"
    "buffer_store_dwordx4 v[250:253], v236, s[28:31], s43 offen sc1\n"
    "v_mfma_f32_16x16x32_bf16 v[176:179], a[28:31], v[32:35], v[176:179]\n"
    "ds_read_b128 a[60:63], v217 offset:24704\n"
    "s_add_u32 m0, s37, 24576\n"
    "v_mfma_f32_16x16x32_bf16 a[192:195], a[32:35], v[32:35], a[192:195]\n"
    "ds_read_b128 a[64:67], v216 offset:24832\n"
    "s_add_u32 s42, s39, 49152\n"
    "v_mfma_f32_16x16x32_bf16 a[208:211], a[36:39], v[32:35], a[208:211]\n"
    "ds_read_b128 a[68:71], v217 offset:24832\n"
    "v_fma_f32 v15, v15, s36, v213\n"
    "v_mfma_f32_16x16x32_bf16 a[224:227], a[40:43], v[32:35], a[224:227]\n"
    "ds_read_b128 a[72:75], v216 offset:30720\n"
    "v_cvt_pk_bf16_f32 v8, v8, v9\n"
    "v_mfma_f32_16x16x32_bf16 a[240:243], a[44:47], v[32:35], a[240:243]\n"
    "ds_read_b128 a[76:79], v217 offset:30720\n"
    "v_cvt_pk_bf16_f32 v9, v10, v11\n"
    "v_mfma_f32_16x16x32_bf16 v[192:195], v[208:211], v[32:35], v[192:195]\n"
    "ds_read_b128 a[80:83], v216 offset:30848\n"
    "v_fma_f32 v16, v16, s36, v214\n"
    "v_mfma_f32_16x16x32_bf16 v[68:71], a[0:3], v[40:43], v[68:71]\n"
    "ds_read_b128 a[84:87], v217 offset:30848\n"
    "v_fma_f32 v17, v17, s36, v214\n"
    "v_mfma_f32_16x16x32_bf16 v[84:87], a[4:7], v[40:43], v[84:87]\n"
    "ds_read_b128 a[88:91], v216 offset:30976\n"
    "v_fma_f32 v18, v18, s36, v214\n"
    "v_mfma_f32_16x16x32_bf16 v[100:103], a[8:11], v[40:43], v[100:103]\n"
    "ds_read_b128 a[92:95], v217 offset:30976\n"
    "v_fma_f32 v19, v19, s36, v214\n"
    "v_mfma_f32_16x16x32_bf16 v[116:119], a[12:15], v[40:43], v[116:119]\n"
    "ds_read_b128 v[250:253], v242 offset:1024\n"
    "v_fma_f32 v20, v20, s36, v214\n"
    "v_mfma_f32_16x16x32_bf16 v[132:135], a[16:19], v[40:43], v[132:135]\n"
    "v_exp_f32 v12, v12\n"
    "v_mfma_f32_16x16x32_bf16 v[148:151], a[20:23], v[40:43], v[148:151]\n"
    "v_exp_f32 v13, v13\n"
    "v_mfma_f32_16x16x32_bf16 v[164:167], a[24:27], v[40:43], v[164:167]\n"
    "v_exp_f32 v14, v14\n"
    "v_mfma_f32_16x16x32_bf16 v[180:183], a[28:31], v[40:43], v[180:183]\n"
    "v_exp_f32 v15, v15\n"
    "v_mfma_f32_16x16x32_bf16 a[196:199], a[32:35], v[40:43], a[196:199]\n"
    "v_cvt_pk_bf16_f32 v10, v12, v13\n"
    "v_cvt_pk_bf16_f32 v11, v14, v15\n"
    "v_mfma_f32_16x16x32_bf16 a[212:215], a[36:39], v[40:43], a[212:215]\n"
    "v_exp_f32 v16, v16\n"
    "v_mfma_f32_16x16x32_bf16 a[228:231], a[40:43], v[40:43], a[228:231]\n"
    "v_exp_f32 v17, v17\n"
    "v_mfma_f32_16x16x32_bf16 a[244:247], a[44:47], v[40:43], a[244:247]\n"
    "v_exp_f32 v18, v18\n"
    "v_mfma_f32_16x16x32_bf16 v[196:199], v[208:211], v[40:43], v[196:199]\n"
    "v_exp_f32 v19, v19\n"
    "v_mfma_f32_16x16x32_bf16 v[72:75], a[0:3], v[48:51], v[72:75]\n"
    "v_exp_f32 v20, v20\n"
    "v_mfma_f32_16x16x32_bf16 v[88:91], a[4:7], v[48:51], v[88:91]\n"
    "v_fma_f32 v21, v21, s36, v214\n"
    "v_fma_f32 v22, v22, s36, v214\n"
    "v_mfma_f32_16x16x32_bf16 v[104:107], a[8:11], v[48:51], v[104:107]\n"
    "v_exp_f32 v21, v21\n"
    "v_mfma_f32_16x16x32_bf16 v[120:123], a[12:15], v[48:51], v[120:123]\n"
    "v_exp_f32 v22, v22\n"
    "v_mfma_f32_16x16x32_bf16 v[136:139], a[16:19], v[48:51], v[136:139]\n"
    "v_fma_f32 v23, v23, s36, v214\n"
    "v_cvt_pk_bf16_f32 v16, v16, v17\n"
    "v_mfma_f32_16x16x32_bf16 v[152:155], a[20:23], v[48:51], v[152:155]\n"
    "v_exp_f32 v23, v23\n"
    "v_mfma_f32_16x16x32_bf16 v[168:171], a[24:27], v[48:51], v[168:171]\n"
    "v_cvt_pk_bf16_f32 v17, v18, v19\n"
    "v_cvt_pk_bf16_f32 v18, v20, v21\n"
    "v_mfma_f32_16x16x32_bf16 v[184:187], a[28:31], v[48:51], v[184:187]\n"
    "v_cvt_pk_bf16_f32 v19, v22, v23\n"
    "v_fma_f32 v24, v24, s36, v215\n"
    "v_mfma_f32_16x16x32_bf16 a[200:203], a[32:35], v[48:51], a[200:203]\n"
    "v_exp_f32 v24, v24\n"
    "v_mfma_f32_16x16x32_bf16 a[216:219], a[36:39], v[48:51], a[216:219]\n"
    "v_fma_f32 v25, v25, s36, v215\n"
    "v_fma_f32 v26, v26, s36, v215\n"
    "v_mfma_f32_16x16x32_bf16 a[232:235], a[40:43], v[48:51], a[232:235]\n"
    "v_exp_f32 v25, v25\n"
    "v_mfma_f32_16x16x32_bf16 a[248:251], a[44:47], v[48:51], a[248:251]\n"
    "v_exp_f32 v26, v26\n"
    "v_mfma_f32_16x16x32_bf16 v[200:203], v[208:211], v[48:51], v[200:203]\n"
    "v_fma_f32 v27, v27, s36, v215\n"
    "v_fma_f32 v28, v28, s36, v215\n"
    "v_mfma_f32_16x16x32_bf16 v[76:79], a[0:3], v[56:59], v[76:79]\n"
    "v_exp_f32 v27, v27\n"
    "v_mfma_f32_16x16x32_bf16 v[92:95], a[4:7], v[56:59], v[92:95]\n"
    "v_exp_f32 v28, v28\n"
    "v_mfma_f32_16x16x32_bf16 v[108:111], a[8:11], v[56:59], v[108:111]\n"
    "v_fma_f32 v29, v29, s36, v215\n"
    "v_fma_f32 v30, v30, s36, v215\n"
    "v_mfma_f32_16x16x32_bf16 v[124:127], a[12:15], v[56:59], v[124:127]\n"
    "v_exp_f32 v29, v29\n"
    "v_mfma_f32_16x16x32_bf16 v[140:143], a[16:19], v[56:59], v[140:143]\n"
    "v_exp_f32 v30, v30\n"
    "v_mfma_f32_16x16x32_bf16 v[156:159], a[20:23], v[56:59], v[156:159]\n"
    "v_fma_f32 v31, v31, s36, v215\n"
    "v_cvt_pk_bf16_f32 v24, v24, v25\n"
    "v_mfma_f32_16x16x32_bf16 v[172:175], a[24:27], v[56:59], v[172:175]\n"
    "v_exp_f32 v31, v31\n"
    "v_mfma_f32_16x16x32_bf16 v[188:191], a[28:31], v[56:59], v[188:191]\n"
    "v_cvt_pk_bf16_f32 v25, v26, v27\n"
    "v_cvt_pk_bf16_f32 v26, v28, v29\n"
    "v_mfma_f32_16x16x32_bf16 a[204:207], a[32:35], v[56:59], a[204:207]\n"
    "v_cvt_pk_bf16_f32 v27, v30, v31\n"
    "v_mfma_f32_16x16x32_bf16 a[220:223], a[36:39], v[56:59], a[220:223]\n"
    "v_mfma_f32_16x16x32_bf16 a[236:239], a[40:43], v[56:59], a[236:239]\n"
    "v_mfma_f32_16x16x32_bf16 a[252:255], a[44:47], v[56:59], a[252:255]\n"
    "v_mfma_f32_16x16x32_bf16 v[204:207], v[208:211], v[56:59], v[204:207]\n"
    "s_waitcnt vmcnt(11) lgkmcnt(0)\n"
    "s_barrier\n"
    "L_ph38:\n"
    "v_mfma_f32_16x16x32_bf16 v[32:35], a[48:51], a[96:99], 0\n"
    "buffer_load_dwordx4 v231, s[24:27], s42 offen nt lds\n"
    "v_mfma_f32_16x16x32_bf16 v[32:35], a[52:55], a[100:103], v[32:35]\n"
    "ds_read_b64_tr_b16 a[0:1], v218 offset:36864\n"
    "v_mfma_f32_16x16x32_bf16 v[32:35], a[56:59], a[104:107], v[32:35]\n"
    "buffer_load_dwordx4 v232, s[24:27], s42 offen offset:1024 nt lds\n"
    "v_mfma_f32_16x16x32_bf16 v[32:35], a[60:63], a[108:111], v[32:35]\n"
    "ds_read_b64_tr_b16 a[2:3], v218 offset:43008\n"
    "v_mfma_f32_16x16x32_bf16 v[32:35], a[64:67], a[112:115], v[32:35]\n"
    "buffer_load_dwordx4 v233, s[24:27], s42 offen offset:2048 nt lds\n"
    "v_mfma_f32_16x16x32_bf16 v[32:35], a[68:71], a[116:119], v[32:35]\n"
    "ds_read_b64_tr_b16 a[4:5], v219 offset:36864\n"
    "s_add_u32 s43, s40, 0\n"
    "v_mfma_f32_16x16x32_bf16 v[36:39], a[72:75], a[96:99], 0\n"
    "buffer_store_dwordx4 v[250:253], v236, s[28:31], s43 offen offset:1024 sc1\n"
    "v_mfma_f32_16x16x32_bf16 v[36:39], a[76:79], a[100:103], v[36:39]\n"
    "ds_read_b64_tr_b16 a[6:7], v219 offset:43008\n"
    "s_add_u32 m0, s37, 36864\n"
    "v_mfma_f32_16x16x32_bf16 v[36:39], a[80:83], a[104:107], v[36:39]\n"
    "ds_read_b64_tr_b16 a[8:9], v220 offset:36864\n"
    "s_add_u32 s42, s39, 73728\n"
    "v_mfma_f32_16x16x32_bf16 v[36:39], a[84:87], a[108:111], v[36:39]\n"
    "ds_read_b64_tr_b16 a[10:11], v220 offset:43008\n"
    "v_mfma_f32_16x16x32_bf16 v[36:39], a[88:91], a[112:115], v[36:39]\n"
    "ds_read_b64_tr_b16 a[12:13], v221 offset:36864\n"
    "v_mfma_f32_16x16x32_bf16 v[36:39], a[92:95], a[116:119], v[36:39]\n"
    "ds_read_b64_tr_b16 a[14:15], v221 offset:43008\n"
    "v_mfma_f32_16x16x32_bf16 v[40:43], a[48:51], a[120:123], 0\n"
    "ds_read_b64_tr_b16 a[16:17], v218 offset:36992\n"
    "v_mfma_f32_16x16x32_bf16 v[40:43], a[52:55], a[124:127], v[40:43]\n"
    "ds_read_b64_tr_b16 a[18:19], v218 offset:43136\n"
    "v_fma_f32 v32, v32, s36, v212\n"
    "v_mfma_f32_16x16x32_bf16 v[40:43], a[56:59], a[128:131], v[40:43]\n"
    "ds_read_b64_tr_b16 a[20:21], v219 offset:36992\n"
    "v_fma_f32 v33, v33, s36, v212\n"
    "v_mfma_f32_16x16x32_bf16 v[40:43], a[60:63], a[132:135], v[40:43]\n"
    "ds_read_b64_tr_b16 a[22:23], v219 offset:43136\n"
    "v_fma_f32 v34, v34, s36, v212\n"
    "v_mfma_f32_16x16x32_bf16 v[40:43], a[64:67], a[136:139], v[40:43]\n"
    "ds_read_b64_tr_b16 a[24:25], v220 offset:36992\n"
    "v_fma_f32 v35, v35, s36, v212\n"
    "v_mfma_f32_16x16x32_bf16 v[40:43], a[68:71], a[140:143], v[40:43]\n"
    "ds_read_b64_tr_b16 a[26:27], v220 offset:43136\n"
    "v_mfma_f32_16x16x32_bf16 v[44:47], a[72:75], a[120:123], 0\n"
    "ds_read_b64_tr_b16 a[28:29], v221 offset:36992\n"
    "v_mfma_f32_16x16x32_bf16 v[44:47], a[76:79], a[124:127], v[44:47]\n"
    "ds_read_b64_tr_b16 a[30:31], v221 offset:43136\n"
    "v_fma_f32 v36, v36, s36, v212\n"
    "v_mfma_f32_16x16x32_bf16 v[44:47], a[80:83], a[128:131], v[44:47]\n"
    "ds_read_b64_tr_b16 a[32:33], v218 offset:37120\n"
    "v_fma_f32 v37, v37, s36, v212\n"
    "v_mfma_f32_16x16x32_bf16 v[44:47], a[84:87], a[132:135], v[44:47]\n"
    "ds_read_b64_tr_b16 a[34:35], v218 offset:43264\n"
    "v_fma_f32 v38, v38, s36, v212\n"
    "v_mfma_f32_16x16x32_bf16 v[44:47], a[88:91], a[136:139], v[44:47]\n"
    "ds_read_b64_tr_b16 a[36:37], v219 offset:37120\n"
    "v_fma_f32 v39, v39, s36, v212\n"
    "v_mfma_f32_16x16x32_bf16 v[44:47], a[92:95], a[140:143], v[44:47]\n"
    "ds_read_b64_tr_b16 a[38:39], v219 offset:43264\n"
    "v_mfma_f32_16x16x32_bf16 v[48:51], a[48:51], a[144:147], 0\n"
    "ds_read_b64_tr_b16 a[40:41], v220 offset:37120\n"
    "v_mfma_f32_16x16x32_bf16 v[48:51], a[52:55], a[148:151], v[48:51]\n"
    "ds_read_b64_tr_b16 a[42:43], v220 offset:43264\n"
    "v_fma_f32 v40, v40, s36, v213\n"
    "v_mfma_f32_16x16x32_bf16 v[48:51], a[56:59], a[152:155], v[48:51]\n"
    "ds_read_b64_tr_b16 a[44:45], v221 offset:37120\n"
    "v_fma_f32 v41, v41, s36, v213\n"
    "v_mfma_f32_16x16x32_bf16 v[48:51], a[60:63], a[156:159], v[48:51]\n"
    "ds_read_b64_tr_b16 a[46:47], v221 offset:43264\n"
    "v_fma_f32 v42, v42, s36, v213\n"
    "v_mfma_f32_16x16x32_bf16 v[48:51], a[64:67], a[160:163], v[48:51]\n"
    "ds_read_b128 v[250:253], v243 offset:2048\n"
    "v_fma_f32 v43, v43, s36, v213\n"
    "v_mfma_f32_16x16x32_bf16 v[48:51], a[68:71], a[164:167], v[48:51]\n"
    "v_exp_f32 v32, v32\n"
    "v_mfma_f32_16x16x32_bf16 v[52:55], a[72:75], a[144:147], 0\n"
    "v_exp_f32 v33, v33\n"
    "v_mfma_f32_16x16x32_bf16 v[52:55], a[76:79], a[148:151], v[52:55]\n"
    "v_exp_f32 v34, v34\n"
    "v_mfma_f32_16x16x32_bf16 v[52:55], a[80:83], a[152:155], v[52:55]\n"
    "v_exp_f32 v35, v35\n"
    "v_mfma_f32_16x16x32_bf16 v[52:55], a[84:87], a[156:159], v[52:55]\n"
    "v_exp_f32 v36, v36\n"
    "v_mfma_f32_16x16x32_bf16 v[52:55], a[88:91], a[160:163], v[52:55]\n"
    "v_exp_f32 v37, v37\n"
    "v_mfma_f32_16x16x32_bf16 v[52:55], a[92:95], a[164:167], v[52:55]\n"
    "v_exp_f32 v38, v38\n"
    "v_mfma_f32_16x16x32_bf16 v[56:59], a[48:51], a[168:171], 0\n"
    "v_exp_f32 v39, v39\n"
    "v_mfma_f32_16x16x32_bf16 v[56:59], a[52:55], a[172:175], v[56:59]\n"
    "v_cvt_pk_bf16_f32 v32, v32, v33\n"
    "v_cvt_pk_bf16_f32 v33, v34, v35\n"
    "v_mfma_f32_16x16x32_bf16 v[56:59], a[56:59], a[176:179], v[56:59]\n"
    "v_cvt_pk_bf16_f32 v34, v36, v37\n"
    "v_cvt_pk_bf16_f32 v35, v38, v39\n"
    "v_mfma_f32_16x16x32_bf16 v[56:59], a[60:63], a[180:183], v[56:59]\n"
    "v_exp_f32 v40, v40\n"
    "v_mfma_f32_16x16x32_bf16 v[56:59], a[64:67], a[184:187], v[56:59]\n"
    "v_exp_f32 v41, v41\n"
    "v_mfma_f32_16x16x32_bf16 v[56:59], a[68:71], a[188:191], v[56:59]\n"
    "v_exp_f32 v42, v42\n"
    "v_mfma_f32_16x16x32_bf16 v[60:63], a[72:75], a[168:171], 0\n"
    "v_exp_f32 v43, v43\n"
    "v_mfma_f32_16x16x32_bf16 v[60:63], a[76:79], a[172:175], v[60:63]\n"
    "v_fma_f32 v44, v44, s36, v213\n"
    "v_fma_f32 v45, v45, s36, v213\n"
    "v_mfma_f32_16x16x32_bf16 v[60:63], a[80:83], a[176:179], v[60:63]\n"
    "v_exp_f32 v44, v44\n"
    "v_mfma_f32_16x16x32_bf16 v[60:63], a[84:87], a[180:183], v[60:63]\n"
    "v_exp_f32 v45, v45\n"
    "v_mfma_f32_16x16x32_bf16 v[60:63], a[88:91], a[184:187], v[60:63]\n"
    "v_fma_f32 v46, v46, s36, v213\n"
    "v_fma_f32 v47, v47, s36, v213\n"
    "v_mfma_f32_16x16x32_bf16 v[60:63], a[92:95], a[188:191], v[60:63]\n"
    "s_waitcnt vmcnt(12) lgkmcnt(0)\n"
    "s_barrier\n"
    "L_ph39:\n"
    "v_mfma_f32_16x16x32_bf16 v[64:67], a[0:3], v[0:3], v[64:67]\n"
    "buffer_load_dwordx4 v222, s[20:23], s42 offen nt lds\n"
    "v_mfma_f32_16x16x32_bf16 v[80:83], a[4:7], v[0:3], v[80:83]\n"
    "ds_read_b128 a[48:51], v216 offset:49152\n"
    "v_cvt_pk_bf16_f32 v40, v40, v41\n"
    "v_mfma_f32_16x16x32_bf16 v[96:99], a[8:11], v[0:3], v[96:99]\n"
    "buffer_load_dwordx4 v223, s[20:23], s42 offen offset:1024 nt lds\n"
    "v_mfma_f32_16x16x32_bf16 v[112:115], a[12:15], v[0:3], v[112:115]\n"
    "ds_read_b128 a[52:55], v217 offset:49152\n"
    "v_cvt_pk_bf16_f32 v41, v42, v43\n"
    "v_mfma_f32_16x16x32_bf16 v[128:131], a[16:19], v[0:3], v[128:131]\n"
    "buffer_load_dwordx4 v224, s[20:23], s42 offen offset:2048 nt lds\n"
    "v_mfma_f32_16x16x32_bf16 v[144:147], a[20:23], v[0:3], v[144:147]\n"
    "ds_read_b128 a[56:59], v216 offset:49280\n"
    "s_add_u32 s43, s40, 0\n"
    "v_mfma_f32_16x16x32_bf16 v[160:163], a[24:27], v[0:3], v[160:163]\n"
    "buffer_store_dwordx4 v[250:253], v236, s[28:31], s43 offen offset:2048 sc1\n"
    "v_mfma_f32_16x16x32_bf16 v[176:179], a[28:31], v[0:3], v[176:179]\n"
    "s_mov_b32 m0, 159744\n"
    "s_mov_b32 s51, L_ph43-L_code_start\n"
    "v_mfma_f32_16x16x32_bf16 a[192:195], a[32:35], v[0:3], a[192:195]\n"
    "buffer_load_dword v249, s[52:55], s51 offen lds\n"
    "v_mfma_f32_16x16x32_bf16 a[208:211], a[36:39], v[0:3], a[208:211]\n"
    "ds_read_b128 a[60:63], v217 offset:49280\n"
    "s_add_u32 m0, s37, 49152\n"
    "v_mfma_f32_16x16x32_bf16 a[224:227], a[40:43], v[0:3], a[224:227]\n"
    "ds_read_b128 a[64:67], v216 offset:49408\n"
    "s_add_u32 s42, s39, 61440\n"
    "v_mfma_f32_16x16x32_bf16 a[240:243], a[44:47], v[0:3], a[240:243]\n"
    "ds_read_b128 a[68:71], v217 offset:49408\n"
    "v_cvt_pk_bf16_f32 v42, v44, v45\n"
    "v_mfma_f32_16x16x32_bf16 v[192:195], v[208:211], v[0:3], v[192:195]\n"
    "ds_read_b128 a[72:75], v216 offset:55296\n"
    "v_fma_f32 v48, v48, s36, v214\n"
    "v_mfma_f32_16x16x32_bf16 v[68:71], a[0:3], v[8:11], v[68:71]\n"
    "ds_read_b128 a[76:79], v217 offset:55296\n"
    "v_fma_f32 v49, v49, s36, v214\n"
    "v_mfma_f32_16x16x32_bf16 v[84:87], a[4:7], v[8:11], v[84:87]\n"
    "ds_read_b128 a[80:83], v216 offset:55424\n"
    "v_fma_f32 v50, v50, s36, v214\n"
    "v_mfma_f32_16x16x32_bf16 v[100:103], a[8:11], v[8:11], v[100:103]\n"
    "ds_read_b128 a[84:87], v217 offset:55424\n"
    "v_fma_f32 v51, v51, s36, v214\n"
    "v_mfma_f32_16x16x32_bf16 v[116:119], a[12:15], v[8:11], v[116:119]\n"
    "ds_read_b128 a[88:91], v216 offset:55552\n"
    "v_fma_f32 v52, v52, s36, v214\n"
    "v_mfma_f32_16x16x32_bf16 v[132:135], a[16:19], v[8:11], v[132:135]\n"
    "ds_read_b128 a[92:95], v217 offset:55552\n"
    "v_fma_f32 v53, v53, s36, v214\n"
    "v_mfma_f32_16x16x32_bf16 v[148:151], a[20:23], v[8:11], v[148:151]\n"
    "ds_read_b128 v[250:253], v244 offset:3072\n"
    "v_fma_f32 v54, v54, s36, v214\n"
    "v_mfma_f32_16x16x32_bf16 v[164:167], a[24:27], v[8:11], v[164:167]\n"
    "v_exp_f32 v46, v46\n"
    "v_mfma_f32_16x16x32_bf16 v[180:183], a[28:31], v[8:11], v[180:183]\n"
    "v_exp_f32 v47, v47\n"
    "v_mfma_f32_16x16x32_bf16 a[196:199], a[32:35], v[8:11], a[196:199]\n"
    "v_cvt_pk_bf16_f32 v43, v46, v47\n"
    "v_fma_f32 v55, v55, s36, v214\n"
    "v_mfma_f32_16x16x32_bf16 a[212:215], a[36:39], v[8:11], a[212:215]\n"
    "v_exp_f32 v48, v48\n"
    "v_mfma_f32_16x16x32_bf16 a[228:231], a[40:43], v[8:11], a[228:231]\n"
    "v_exp_f32 v49, v49\n"
    "v_mfma_f32_16x16x32_bf16 a[244:247], a[44:47], v[8:11], a[244:247]\n"
    "v_exp_f32 v50, v50\n"
    "v_mfma_f32_16x16x32_bf16 v[196:199], v[208:211], v[8:11], v[196:199]\n"
    "v_exp_f32 v51, v51\n"
    "v_mfma_f32_16x16x32_bf16 v[72:75], a[0:3], v[16:19], v[72:75]\n"
    "v_exp_f32 v52, v52\n"
    "v_mfma_f32_16x16x32_bf16 v[88:91], a[4:7], v[16:19], v[88:91]\n"
    "v_exp_f32 v53, v53\n"
    "v_mfma_f32_16x16x32_bf16 v[104:107], a[8:11], v[16:19], v[104:107]\n"
    "v_exp_f32 v54, v54\n"
    "v_mfma_f32_16x16x32_bf16 v[120:123], a[12:15], v[16:19], v[120:123]\n"
    "v_exp_f32 v55, v55\n"
    "v_mfma_f32_16x16x32_bf16 v[136:139], a[16:19], v[16:19], v[136:139]\n"
    "v_cvt_pk_bf16_f32 v48, v48, v49\n"
    "v_cvt_pk_bf16_f32 v49, v50, v51\n"
    "v_mfma_f32_16x16x32_bf16 v[152:155], a[20:23], v[16:19], v[152:155]\n"
    "v_cvt_pk_bf16_f32 v50, v52, v53\n"
    "v_cvt_pk_bf16_f32 v51, v54, v55\n"
    "v_mfma_f32_16x16x32_bf16 v[168:171], a[24:27], v[16:19], v[168:171]\n"
    "v_fma_f32 v56, v56, s36, v215\n"
    "v_fma_f32 v57, v57, s36, v215\n"
    "v_mfma_f32_16x16x32_bf16 v[184:187], a[28:31], v[16:19], v[184:187]\n"
    "v_exp_f32 v56, v56\n"
    "v_mfma_f32_16x16x32_bf16 a[200:203], a[32:35], v[16:19], a[200:203]\n"
    "v_exp_f32 v57, v57\n"
    "v_mfma_f32_16x16x32_bf16 a[216:219], a[36:39], v[16:19], a[216:219]\n"
    "v_fma_f32 v58, v58, s36, v215\n"
    "v_fma_f32 v59, v59, s36, v215\n"
    "v_mfma_f32_16x16x32_bf16 a[232:235], a[40:43], v[16:19], a[232:235]\n"
    "v_exp_f32 v58, v58\n"
    "v_mfma_f32_16x16x32_bf16 a[248:251], a[44:47], v[16:19], a[248:251]\n"
    "v_exp_f32 v59, v59\n"
    "v_mfma_f32_16x16x32_bf16 v[200:203], v[208:211], v[16:19], v[200:203]\n"
    "v_fma_f32 v60, v60, s36, v215\n"
    "v_fma_f32 v61, v61, s36, v215\n"
    "v_mfma_f32_16x16x32_bf16 v[76:79], a[0:3], v[24:27], v[76:79]\n"
    "v_exp_f32 v60, v60\n"
    "v_mfma_f32_16x16x32_bf16 v[92:95], a[4:7], v[24:27], v[92:95]\n"
    "v_exp_f32 v61, v61\n"
    "v_mfma_f32_16x16x32_bf16 v[108:111], a[8:11], v[24:27], v[108:111]\n"
    "v_fma_f32 v62, v62, s36, v215\n"
    "v_fma_f32 v63, v63, s36, v215\n"
    "v_mfma_f32_16x16x32_bf16 v[124:127], a[12:15], v[24:27], v[124:127]\n"
    "v_exp_f32 v62, v62\n"
    "v_mfma_f32_16x16x32_bf16 v[140:143], a[16:19], v[24:27], v[140:143]\n"
    "v_exp_f32 v63, v63\n"
    "v_mfma_f32_16x16x32_bf16 v[156:159], a[20:23], v[24:27], v[156:159]\n"
    "v_cvt_pk_bf16_f32 v56, v56, v57\n"
    "v_cvt_pk_bf16_f32 v57, v58, v59\n"
    "v_mfma_f32_16x16x32_bf16 v[172:175], a[24:27], v[24:27], v[172:175]\n"
    "v_cvt_pk_bf16_f32 v58, v60, v61\n"
    "v_cvt_pk_bf16_f32 v59, v62, v63\n"
    "v_mfma_f32_16x16x32_bf16 v[188:191], a[28:31], v[24:27], v[188:191]\n"
    "v_mfma_f32_16x16x32_bf16 a[204:207], a[32:35], v[24:27], a[204:207]\n"
    "v_mfma_f32_16x16x32_bf16 a[220:223], a[36:39], v[24:27], a[220:223]\n"
    "v_mfma_f32_16x16x32_bf16 a[236:239], a[40:43], v[24:27], a[236:239]\n"
    "v_mfma_f32_16x16x32_bf16 a[252:255], a[44:47], v[24:27], a[252:255]\n"
    "v_mfma_f32_16x16x32_bf16 v[204:207], v[208:211], v[24:27], v[204:207]\n"
    "s_waitcnt vmcnt(13) lgkmcnt(0)\n"
    "s_barrier\n"
    "L_ph40:\n"
    "v_mfma_f32_16x16x32_bf16 v[0:3], a[48:51], a[96:99], 0\n"
    "buffer_load_dwordx4 v231, s[24:27], s42 offen nt lds\n"
    "v_mfma_f32_16x16x32_bf16 v[0:3], a[52:55], a[100:103], v[0:3]\n"
    "ds_read_b64_tr_b16 a[0:1], v218 offset:0\n"
    "v_mfma_f32_16x16x32_bf16 v[0:3], a[56:59], a[104:107], v[0:3]\n"
    "buffer_load_dwordx4 v232, s[24:27], s42 offen offset:1024 nt lds\n"
    "v_mfma_f32_16x16x32_bf16 v[0:3], a[60:63], a[108:111], v[0:3]\n"
    "ds_read_b64_tr_b16 a[2:3], v218 offset:6144\n"
    "v_mfma_f32_16x16x32_bf16 v[0:3], a[64:67], a[112:115], v[0:3]\n"
    "buffer_load_dwordx4 v233, s[24:27], s42 offen offset:2048 nt lds\n"
    "v_mfma_f32_16x16x32_bf16 v[0:3], a[68:71], a[116:119], v[0:3]\n"
    "ds_read_b64_tr_b16 a[4:5], v219 offset:0\n"
    "s_add_u32 s43, s40, 3072\n"
    "v_mfma_f32_16x16x32_bf16 v[4:7], a[72:75], a[96:99], 0\n"
    "buffer_store_dwordx4 v[250:253], v236, s[28:31], s43 offen sc1\n"
    "v_mfma_f32_16x16x32_bf16 v[4:7], a[76:79], a[100:103], v[4:7]\n"
    "ds_read_b64_tr_b16 a[6:7], v219 offset:6144\n"
    "s_add_u32 m0, s37, 0\n"
    "v_mfma_f32_16x16x32_bf16 v[4:7], a[80:83], a[104:107], v[4:7]\n"
    "ds_read_b64_tr_b16 a[8:9], v220 offset:0\n"
    "s_add_u32 s42, s39, 86016\n"
    "v_mfma_f32_16x16x32_bf16 v[4:7], a[84:87], a[108:111], v[4:7]\n"
    "ds_read_b64_tr_b16 a[10:11], v220 offset:6144\n"
    "v_mfma_f32_16x16x32_bf16 v[4:7], a[88:91], a[112:115], v[4:7]\n"
    "ds_read_b64_tr_b16 a[12:13], v221 offset:0\n"
    "v_mfma_f32_16x16x32_bf16 v[4:7], a[92:95], a[116:119], v[4:7]\n"
    "ds_read_b64_tr_b16 a[14:15], v221 offset:6144\n"
    "v_mfma_f32_16x16x32_bf16 v[8:11], a[48:51], a[120:123], 0\n"
    "ds_read_b64_tr_b16 a[16:17], v218 offset:128\n"
    "v_mfma_f32_16x16x32_bf16 v[8:11], a[52:55], a[124:127], v[8:11]\n"
    "ds_read_b64_tr_b16 a[18:19], v218 offset:6272\n"
    "v_fma_f32 v0, v0, s36, v212\n"
    "v_mfma_f32_16x16x32_bf16 v[8:11], a[56:59], a[128:131], v[8:11]\n"
    "ds_read_b64_tr_b16 a[20:21], v219 offset:128\n"
    "v_fma_f32 v1, v1, s36, v212\n"
    "v_mfma_f32_16x16x32_bf16 v[8:11], a[60:63], a[132:135], v[8:11]\n"
    "ds_read_b64_tr_b16 a[22:23], v219 offset:6272\n"
    "v_fma_f32 v2, v2, s36, v212\n"
    "v_mfma_f32_16x16x32_bf16 v[8:11], a[64:67], a[136:139], v[8:11]\n"
    "ds_read_b64_tr_b16 a[24:25], v220 offset:128\n"
    "v_fma_f32 v3, v3, s36, v212\n"
    "v_mfma_f32_16x16x32_bf16 v[8:11], a[68:71], a[140:143], v[8:11]\n"
    "ds_read_b64_tr_b16 a[26:27], v220 offset:6272\n"
    "v_mfma_f32_16x16x32_bf16 v[12:15], a[72:75], a[120:123], 0\n"
    "ds_read_b64_tr_b16 a[28:29], v221 offset:128\n"
    "v_mfma_f32_16x16x32_bf16 v[12:15], a[76:79], a[124:127], v[12:15]\n"
    "ds_read_b64_tr_b16 a[30:31], v221 offset:6272\n"
    "v_fma_f32 v4, v4, s36, v212\n"
    "v_mfma_f32_16x16x32_bf16 v[12:15], a[80:83], a[128:131], v[12:15]\n"
    "ds_read_b64_tr_b16 a[32:33], v218 offset:256\n"
    "v_fma_f32 v5, v5, s36, v212\n"
    "v_mfma_f32_16x16x32_bf16 v[12:15], a[84:87], a[132:135], v[12:15]\n"
    "ds_read_b64_tr_b16 a[34:35], v218 offset:6400\n"
    "v_fma_f32 v6, v6, s36, v212\n"
    "v_mfma_f32_16x16x32_bf16 v[12:15], a[88:91], a[136:139], v[12:15]\n"
    "ds_read_b64_tr_b16 a[36:37], v219 offset:256\n"
    "v_fma_f32 v7, v7, s36, v212\n"
    "v_mfma_f32_16x16x32_bf16 v[12:15], a[92:95], a[140:143], v[12:15]\n"
    "ds_read_b64_tr_b16 a[38:39], v219 offset:6400\n"
    "v_mfma_f32_16x16x32_bf16 v[16:19], a[48:51], a[144:147], 0\n"
    "ds_read_b64_tr_b16 a[40:41], v220 offset:256\n"
    "v_mfma_f32_16x16x32_bf16 v[16:19], a[52:55], a[148:151], v[16:19]\n"
    "ds_read_b64_tr_b16 a[42:43], v220 offset:6400\n"
    "v_fma_f32 v8, v8, s36, v213\n"
    "v_mfma_f32_16x16x32_bf16 v[16:19], a[56:59], a[152:155], v[16:19]\n"
    "ds_read_b64_tr_b16 a[44:45], v221 offset:256\n"
    "v_fma_f32 v9, v9, s36, v213\n"
    "v_mfma_f32_16x16x32_bf16 v[16:19], a[60:63], a[156:159], v[16:19]\n"
    "ds_read_b64_tr_b16 a[46:47], v221 offset:6400\n"
    "v_fma_f32 v10, v10, s36, v213\n"
    "v_mfma_f32_16x16x32_bf16 v[16:19], a[64:67], a[160:163], v[16:19]\n"
    "ds_read_b128 v[250:253], v245 offset:4096\n"
    "v_fma_f32 v11, v11, s36, v213\n"
    "v_mfma_f32_16x16x32_bf16 v[16:19], a[68:71], a[164:167], v[16:19]\n"
    "v_exp_f32 v0, v0\n"
    "v_mfma_f32_16x16x32_bf16 v[20:23], a[72:75], a[144:147], 0\n"
    "v_exp_f32 v1, v1\n"
    "v_mfma_f32_16x16x32_bf16 v[20:23], a[76:79], a[148:151], v[20:23]\n"
    "v_exp_f32 v2, v2\n"
    "v_mfma_f32_16x16x32_bf16 v[20:23], a[80:83], a[152:155], v[20:23]\n"
    "v_exp_f32 v3, v3\n"
    "v_mfma_f32_16x16x32_bf16 v[20:23], a[84:87], a[156:159], v[20:23]\n"
    "v_exp_f32 v4, v4\n"
    "v_mfma_f32_16x16x32_bf16 v[20:23], a[88:91], a[160:163], v[20:23]\n"
    "v_exp_f32 v5, v5\n"
    "v_mfma_f32_16x16x32_bf16 v[20:23], a[92:95], a[164:167], v[20:23]\n"
    "v_exp_f32 v6, v6\n"
    "v_mfma_f32_16x16x32_bf16 v[24:27], a[48:51], a[168:171], 0\n"
    "v_exp_f32 v7, v7\n"
    "v_mfma_f32_16x16x32_bf16 v[24:27], a[52:55], a[172:175], v[24:27]\n"
    "v_cvt_pk_bf16_f32 v0, v0, v1\n"
    "v_cvt_pk_bf16_f32 v1, v2, v3\n"
    "v_mfma_f32_16x16x32_bf16 v[24:27], a[56:59], a[176:179], v[24:27]\n"
    "v_cvt_pk_bf16_f32 v2, v4, v5\n"
    "v_cvt_pk_bf16_f32 v3, v6, v7\n"
    "v_mfma_f32_16x16x32_bf16 v[24:27], a[60:63], a[180:183], v[24:27]\n"
    "v_exp_f32 v8, v8\n"
    "v_mfma_f32_16x16x32_bf16 v[24:27], a[64:67], a[184:187], v[24:27]\n"
    "v_exp_f32 v9, v9\n"
    "v_mfma_f32_16x16x32_bf16 v[24:27], a[68:71], a[188:191], v[24:27]\n"
    "v_exp_f32 v10, v10\n"
    "v_mfma_f32_16x16x32_bf16 v[28:31], a[72:75], a[168:171], 0\n"
    "v_exp_f32 v11, v11\n"
    "v_mfma_f32_16x16x32_bf16 v[28:31], a[76:79], a[172:175], v[28:31]\n"
    "v_fma_f32 v12, v12, s36, v213\n"
    "v_fma_f32 v13, v13, s36, v213\n"
    "v_mfma_f32_16x16x32_bf16 v[28:31], a[80:83], a[176:179], v[28:31]\n"
    "v_exp_f32 v12, v12\n"
    "v_mfma_f32_16x16x32_bf16 v[28:31], a[84:87], a[180:183], v[28:31]\n"
    "v_exp_f32 v13, v13\n"
    "v_mfma_f32_16x16x32_bf16 v[28:31], a[88:91], a[184:187], v[28:31]\n"
    "v_fma_f32 v14, v14, s36, v213\n"
    "v_fma_f32 v15, v15, s36, v213\n"
    "v_mfma_f32_16x16x32_bf16 v[28:31], a[92:95], a[188:191], v[28:31]\n"
    "s_waitcnt vmcnt(14) lgkmcnt(0)\n"
    "s_barrier\n"
    "L_ph41:\n"
    "v_mfma_f32_16x16x32_bf16 v[64:67], a[0:3], v[32:35], v[64:67]\n"
    "buffer_load_dwordx4 v222, s[20:23], s42 offen nt lds\n"
    "v_mfma_f32_16x16x32_bf16 v[80:83], a[4:7], v[32:35], v[80:83]\n"
    "ds_read_b128 a[48:51], v216 offset:12288\n"
    "v_cvt_pk_bf16_f32 v8, v8, v9\n"
    "v_mfma_f32_16x16x32_bf16 v[96:99], a[8:11], v[32:35], v[96:99]\n"
    "buffer_load_dwordx4 v223, s[20:23], s42 offen offset:1024 nt lds\n"
    "v_mfma_f32_16x16x32_bf16 v[112:115], a[12:15], v[32:35], v[112:115]\n"
    "ds_read_b128 a[52:55], v217 offset:12288\n"
    "v_cvt_pk_bf16_f32 v9, v10, v11\n"
    "v_mfma_f32_16x16x32_bf16 v[128:131], a[16:19], v[32:35], v[128:131]\n"
    "buffer_load_dwordx4 v224, s[20:23], s42 offen offset:2048 nt lds\n"
    "v_mfma_f32_16x16x32_bf16 v[144:147], a[20:23], v[32:35], v[144:147]\n"
    "ds_read_b128 a[56:59], v216 offset:12416\n"
    "s_add_u32 s43, s40, 3072\n"
    "v_mfma_f32_16x16x32_bf16 v[160:163], a[24:27], v[32:35], v[160:163]\n"
    "buffer_store_dwordx4 v[250:253], v236, s[28:31], s43 offen offset:1024 sc1\n"
    "v_mfma_f32_16x16x32_bf16 v[176:179], a[28:31], v[32:35], v[176:179]\n"
    "ds_read_b128 a[60:63], v217 offset:12416\n"
    "s_add_u32 m0, s37, 12288\n"
    "v_mfma_f32_16x16x32_bf16 a[192:195], a[32:35], v[32:35], a[192:195]\n"
    "ds_read_b128 a[64:67], v216 offset:12544\n"
    "s_add_u32 s42, s39, 73728\n"
    "v_mfma_f32_16x16x32_bf16 a[208:211], a[36:39], v[32:35], a[208:211]\n"
    "ds_read_b128 a[68:71], v217 offset:12544\n"
    "v_cvt_pk_bf16_f32 v10, v12, v13\n"
    "v_mfma_f32_16x16x32_bf16 a[224:227], a[40:43], v[32:35], a[224:227]\n"
    "ds_read_b128 a[72:75], v216 offset:18432\n"
    "v_fma_f32 v16, v16, s36, v214\n"
    "v_mfma_f32_16x16x32_bf16 a[240:243], a[44:47], v[32:35], a[240:243]\n"
    "ds_read_b128 a[76:79], v217 offset:18432\n"
    "v_fma_f32 v17, v17, s36, v214\n"
    "v_mfma_f32_16x16x32_bf16 v[192:195], v[208:211], v[32:35], v[192:195]\n"
    "ds_read_b128 a[80:83], v216 offset:18560\n"
    "v_fma_f32 v18, v18, s36, v214\n"
    "v_mfma_f32_16x16x32_bf16 v[68:71], a[0:3], v[40:43], v[68:71]\n"
    "ds_read_b128 a[84:87], v217 offset:18560\n"
    "v_fma_f32 v19, v19, s36, v214\n"
    "v_mfma_f32_16x16x32_bf16 v[84:87], a[4:7], v[40:43], v[84:87]\n"
    "ds_read_b128 a[88:91], v216 offset:18688\n"
    "v_fma_f32 v20, v20, s36, v214\n"
    "v_mfma_f32_16x16x32_bf16 v[100:103], a[8:11], v[40:43], v[100:103]\n"
    "ds_read_b128 a[92:95], v217 offset:18688\n"
    "v_fma_f32 v21, v21, s36, v214\n"
    "v_mfma_f32_16x16x32_bf16 v[116:119], a[12:15], v[40:43], v[116:119]\n"
    "ds_read_b128 v[250:253], v246 offset:5120\n"
    "v_fma_f32 v22, v22, s36, v214\n"
    "v_mfma_f32_16x16x32_bf16 v[132:135], a[16:19], v[40:43], v[132:135]\n"
    "v_exp_f32 v14, v14\n"
    "v_mfma_f32_16x16x32_bf16 v[148:151], a[20:23], v[40:43], v[148:151]\n"
    "v_exp_f32 v15, v15\n"
    "v_mfma_f32_16x16x32_bf16 v[164:167], a[24:27], v[40:43], v[164:167]\n"
    "v_cvt_pk_bf16_f32 v11, v14, v15\n"
    "v_fma_f32 v23, v23, s36, v214\n"
    "v_mfma_f32_16x16x32_bf16 v[180:183], a[28:31], v[40:43], v[180:183]\n"
    "v_exp_f32 v16, v16\n"
    "v_mfma_f32_16x16x32_bf16 a[196:199], a[32:35], v[40:43], a[196:199]\n"
    "v_exp_f32 v17, v17\n"
    "v_mfma_f32_16x16x32_bf16 a[212:215], a[36:39], v[40:43], a[212:215]\n"
    "v_exp_f32 v18, v18\n"
    "v_mfma_f32_16x16x32_bf16 a[228:231], a[40:43], v[40:43], a[228:231]\n"
    "v_exp_f32 v19, v19\n"
    "v_mfma_f32_16x16x32_bf16 a[244:247], a[44:47], v[40:43], a[244:247]\n"
    "v_exp_f32 v20, v20\n"
    "v_mfma_f32_16x16x32_bf16 v[196:199], v[208:211], v[40:43], v[196:199]\n"
    "v_exp_f32 v21, v21\n"
    "v_mfma_f32_16x16x32_bf16 v[72:75], a[0:3], v[48:51], v[72:75]\n"
    "v_exp_f32 v22, v22\n"
    "v_mfma_f32_16x16x32_bf16 v[88:91], a[4:7], v[48:51], v[88:91]\n"
    "v_exp_f32 v23, v23\n"
    "v_mfma_f32_16x16x32_bf16 v[104:107], a[8:11], v[48:51], v[104:107]\n"
    "v_cvt_pk_bf16_f32 v16, v16, v17\n"
    "v_cvt_pk_bf16_f32 v17, v18, v19\n"
    "v_mfma_f32_16x16x32_bf16 v[120:123], a[12:15], v[48:51], v[120:123]\n"
    "v_cvt_pk_bf16_f32 v18, v20, v21\n"
    "v_cvt_pk_bf16_f32 v19, v22, v23\n"
    "v_mfma_f32_16x16x32_bf16 v[136:139], a[16:19], v[48:51], v[136:139]\n"
    "v_fma_f32 v24, v24, s36, v215\n"
    "v_fma_f32 v25, v25, s36, v215\n"
    "v_mfma_f32_16x16x32_bf16 v[152:155], a[20:23], v[48:51], v[152:155]\n"
    "v_exp_f32 v24, v24\n"
    "v_mfma_f32_16x16x32_bf16 v[168:171], a[24:27], v[48:51], v[168:171]\n"
    "v_exp_f32 v25, v25\n"
    "v_mfma_f32_16x16x32_bf16 v[184:187], a[28:31], v[48:51], v[184:187]\n"
    "v_fma_f32 v26, v26, s36, v215\n"
    "v_fma_f32 v27, v27, s36, v215\n"
    "v_mfma_f32_16x16x32_bf16 a[200:203], a[32:35], v[48:51], a[200:203]\n"
    "v_exp_f32 v26, v26\n"
    "v_mfma_f32_16x16x32_bf16 a[216:219], a[36:39], v[48:51], a[216:219]\n"
    "v_exp_f32 v27, v27\n"
    "v_mfma_f32_16x16x32_bf16 a[232:235], a[40:43], v[48:51], a[232:235]\n"
    "v_fma_f32 v28, v28, s36, v215\n"
    "v_fma_f32 v29, v29, s36, v215\n"
    "v_mfma_f32_16x16x32_bf16 a[248:251], a[44:47], v[48:51], a[248:251]\n"
    "v_exp_f32 v28, v28\n"
    "v_mfma_f32_16x16x32_bf16 v[200:203], v[208:211], v[48:51], v[200:203]\n"
    "v_exp_f32 v29, v29\n"
    "v_mfma_f32_16x16x32_bf16 v[76:79], a[0:3], v[56:59], v[76:79]\n"
    "v_fma_f32 v30, v30, s36, v215\n"
    "v_fma_f32 v31, v31, s36, v215\n"
    "v_mfma_f32_16x16x32_bf16 v[92:95], a[4:7], v[56:59], v[92:95]\n"
    "v_exp_f32 v30, v30\n"
    "v_mfma_f32_16x16x32_bf16 v[108:111], a[8:11], v[56:59], v[108:111]\n"
    "v_exp_f32 v31, v31\n"
    "v_mfma_f32_16x16x32_bf16 v[124:127], a[12:15], v[56:59], v[124:127]\n"
    "v_cvt_pk_bf16_f32 v24, v24, v25\n"
    "v_cvt_pk_bf16_f32 v25, v26, v27\n"
    "v_mfma_f32_16x16x32_bf16 v[140:143], a[16:19], v[56:59], v[140:143]\n"
    "v_cvt_pk_bf16_f32 v26, v28, v29\n"
    "v_cvt_pk_bf16_f32 v27, v30, v31\n"
    "v_mfma_f32_16x16x32_bf16 v[156:159], a[20:23], v[56:59], v[156:159]\n"
    "v_mfma_f32_16x16x32_bf16 v[172:175], a[24:27], v[56:59], v[172:175]\n"
    "v_mfma_f32_16x16x32_bf16 v[188:191], a[28:31], v[56:59], v[188:191]\n"
    "v_mfma_f32_16x16x32_bf16 a[204:207], a[32:35], v[56:59], a[204:207]\n"
    "v_mfma_f32_16x16x32_bf16 a[220:223], a[36:39], v[56:59], a[220:223]\n"
    "v_mfma_f32_16x16x32_bf16 a[236:239], a[40:43], v[56:59], a[236:239]\n"
    "v_mfma_f32_16x16x32_bf16 a[252:255], a[44:47], v[56:59], a[252:255]\n"
    "v_mfma_f32_16x16x32_bf16 v[204:207], v[208:211], v[56:59], v[204:207]\n"
    "s_waitcnt vmcnt(14) lgkmcnt(0)\n"
    "s_barrier\n"
    "L_ph42:\n"
    "v_mfma_f32_16x16x32_bf16 v[32:35], a[48:51], a[96:99], 0\n"
    "buffer_load_dwordx4 v231, s[24:27], s42 offen nt lds\n"
    "v_mfma_f32_16x16x32_bf16 v[32:35], a[52:55], a[100:103], v[32:35]\n"
    "ds_read_b64_tr_b16 a[0:1], v218 offset:24576\n"
    "v_mfma_f32_16x16x32_bf16 v[32:35], a[56:59], a[104:107], v[32:35]\n"
    "buffer_load_dwordx4 v232, s[24:27], s42 offen offset:1024 nt lds\n"
    "v_mfma_f32_16x16x32_bf16 v[32:35], a[60:63], a[108:111], v[32:35]\n"
    "ds_read_b64_tr_b16 a[2:3], v218 offset:30720\n"
    "v_mfma_f32_16x16x32_bf16 v[32:35], a[64:67], a[112:115], v[32:35]\n"
    "buffer_load_dwordx4 v233, s[24:27], s42 offen offset:2048 nt lds\n"
    "v_mfma_f32_16x16x32_bf16 v[32:35], a[68:71], a[116:119], v[32:35]\n"
    "ds_read_b64_tr_b16 a[4:5], v219 offset:24576\n"
    "s_add_u32 s43, s40, 3072\n"
    "v_mfma_f32_16x16x32_bf16 v[36:39], a[72:75], a[96:99], 0\n"
    "buffer_store_dwordx4 v[250:253], v236, s[28:31], s43 offen offset:2048 sc1\n"
    "v_mfma_f32_16x16x32_bf16 v[36:39], a[76:79], a[100:103], v[36:39]\n"
    "ds_read_b64_tr_b16 a[6:7], v219 offset:30720\n"
    "s_add_u32 m0, s37, 24576\n"
    "v_mfma_f32_16x16x32_bf16 v[36:39], a[80:83], a[104:107], v[36:39]\n"
    "ds_read_b64_tr_b16 a[8:9], v220 offset:24576\n"
    "s_add_u32 s42, s39, 98304\n"
    "v_mfma_f32_16x16x32_bf16 v[36:39], a[84:87], a[108:111], v[36:39]\n"
    "ds_read_b64_tr_b16 a[10:11], v220 offset:30720\n"
    "v_mfma_f32_16x16x32_bf16 v[36:39], a[88:91], a[112:115], v[36:39]\n"
    "ds_read_b64_tr_b16 a[12:13], v221 offset:24576\n"
    "v_mfma_f32_16x16x32_bf16 v[36:39], a[92:95], a[116:119], v[36:39]\n"
    "ds_read_b64_tr_b16 a[14:15], v221 offset:30720\n"
    "v_mfma_f32_16x16x32_bf16 v[40:43], a[48:51], a[120:123], 0\n"
    "ds_read_b64_tr_b16 a[16:17], v218 offset:24704\n"
    "v_mfma_f32_16x16x32_bf16 v[40:43], a[52:55], a[124:127], v[40:43]\n"
    "ds_read_b64_tr_b16 a[18:19], v218 offset:30848\n"
    "v_fma_f32 v32, v32, s36, v212\n"
    "v_mfma_f32_16x16x32_bf16 v[40:43], a[56:59], a[128:131], v[40:43]\n"
    "ds_read_b64_tr_b16 a[20:21], v219 offset:24704\n"
    "v_fma_f32 v33, v33, s36, v212\n"
    "v_mfma_f32_16x16x32_bf16 v[40:43], a[60:63], a[132:135], v[40:43]\n"
    "ds_read_b64_tr_b16 a[22:23], v219 offset:30848\n"
    "v_fma_f32 v34, v34, s36, v212\n"
    "v_mfma_f32_16x16x32_bf16 v[40:43], a[64:67], a[136:139], v[40:43]\n"
    "ds_read_b64_tr_b16 a[24:25], v220 offset:24704\n"
    "v_fma_f32 v35, v35, s36, v212\n"
    "v_mfma_f32_16x16x32_bf16 v[40:43], a[68:71], a[140:143], v[40:43]\n"
    "ds_read_b64_tr_b16 a[26:27], v220 offset:30848\n"
    "v_mfma_f32_16x16x32_bf16 v[44:47], a[72:75], a[120:123], 0\n"
    "ds_read_b64_tr_b16 a[28:29], v221 offset:24704\n"
    "v_mfma_f32_16x16x32_bf16 v[44:47], a[76:79], a[124:127], v[44:47]\n"
    "ds_read_b64_tr_b16 a[30:31], v221 offset:30848\n"
    "v_fma_f32 v36, v36, s36, v212\n"
    "v_mfma_f32_16x16x32_bf16 v[44:47], a[80:83], a[128:131], v[44:47]\n"
    "ds_read_b64_tr_b16 a[32:33], v218 offset:24832\n"
    "v_fma_f32 v37, v37, s36, v212\n"
    "v_mfma_f32_16x16x32_bf16 v[44:47], a[84:87], a[132:135], v[44:47]\n"
    "ds_read_b64_tr_b16 a[34:35], v218 offset:30976\n"
    "v_fma_f32 v38, v38, s36, v212\n"
    "v_mfma_f32_16x16x32_bf16 v[44:47], a[88:91], a[136:139], v[44:47]\n"
    "ds_read_b64_tr_b16 a[36:37], v219 offset:24832\n"
    "v_fma_f32 v39, v39, s36, v212\n"
    "v_mfma_f32_16x16x32_bf16 v[44:47], a[92:95], a[140:143], v[44:47]\n"
    "ds_read_b64_tr_b16 a[38:39], v219 offset:30976\n"
    "v_mfma_f32_16x16x32_bf16 v[48:51], a[48:51], a[144:147], 0\n"
    "ds_read_b64_tr_b16 a[40:41], v220 offset:24832\n"
    "v_mfma_f32_16x16x32_bf16 v[48:51], a[52:55], a[148:151], v[48:51]\n"
    "ds_read_b64_tr_b16 a[42:43], v220 offset:30976\n"
    "v_fma_f32 v40, v40, s36, v213\n"
    "v_mfma_f32_16x16x32_bf16 v[48:51], a[56:59], a[152:155], v[48:51]\n"
    "ds_read_b64_tr_b16 a[44:45], v221 offset:24832\n"
    "v_fma_f32 v41, v41, s36, v213\n"
    "v_mfma_f32_16x16x32_bf16 v[48:51], a[60:63], a[156:159], v[48:51]\n"
    "ds_read_b64_tr_b16 a[46:47], v221 offset:30976\n"
    "v_fma_f32 v42, v42, s36, v213\n"
    "v_mfma_f32_16x16x32_bf16 v[48:51], a[64:67], a[160:163], v[48:51]\n"
    "ds_read_b128 v[250:253], v241 offset:6144\n"
    "v_fma_f32 v43, v43, s36, v213\n"
    "v_mfma_f32_16x16x32_bf16 v[48:51], a[68:71], a[164:167], v[48:51]\n"
    "v_exp_f32 v32, v32\n"
    "v_mfma_f32_16x16x32_bf16 v[52:55], a[72:75], a[144:147], 0\n"
    "v_exp_f32 v33, v33\n"
    "v_mfma_f32_16x16x32_bf16 v[52:55], a[76:79], a[148:151], v[52:55]\n"
    "v_exp_f32 v34, v34\n"
    "v_mfma_f32_16x16x32_bf16 v[52:55], a[80:83], a[152:155], v[52:55]\n"
    "v_exp_f32 v35, v35\n"
    "v_mfma_f32_16x16x32_bf16 v[52:55], a[84:87], a[156:159], v[52:55]\n"
    "v_exp_f32 v36, v36\n"
    "v_mfma_f32_16x16x32_bf16 v[52:55], a[88:91], a[160:163], v[52:55]\n"
    "v_exp_f32 v37, v37\n"
    "v_mfma_f32_16x16x32_bf16 v[52:55], a[92:95], a[164:167], v[52:55]\n"
    "v_exp_f32 v38, v38\n"
    "v_mfma_f32_16x16x32_bf16 v[56:59], a[48:51], a[168:171], 0\n"
    "v_exp_f32 v39, v39\n"
    "v_mfma_f32_16x16x32_bf16 v[56:59], a[52:55], a[172:175], v[56:59]\n"
    "v_cvt_pk_bf16_f32 v32, v32, v33\n"
    "v_cvt_pk_bf16_f32 v33, v34, v35\n"
    "v_mfma_f32_16x16x32_bf16 v[56:59], a[56:59], a[176:179], v[56:59]\n"
    "v_cvt_pk_bf16_f32 v34, v36, v37\n"
    "v_cvt_pk_bf16_f32 v35, v38, v39\n"
    "v_mfma_f32_16x16x32_bf16 v[56:59], a[60:63], a[180:183], v[56:59]\n"
    "v_exp_f32 v40, v40\n"
    "v_mfma_f32_16x16x32_bf16 v[56:59], a[64:67], a[184:187], v[56:59]\n"
    "v_exp_f32 v41, v41\n"
    "v_mfma_f32_16x16x32_bf16 v[56:59], a[68:71], a[188:191], v[56:59]\n"
    "v_exp_f32 v42, v42\n"
    "v_mfma_f32_16x16x32_bf16 v[60:63], a[72:75], a[168:171], 0\n"
    "v_exp_f32 v43, v43\n"
    "v_mfma_f32_16x16x32_bf16 v[60:63], a[76:79], a[172:175], v[60:63]\n"
    "v_fma_f32 v44, v44, s36, v213\n"
    "v_fma_f32 v45, v45, s36, v213\n"
    "v_mfma_f32_16x16x32_bf16 v[60:63], a[80:83], a[176:179], v[60:63]\n"
    "v_exp_f32 v44, v44\n"
    "v_mfma_f32_16x16x32_bf16 v[60:63], a[84:87], a[180:183], v[60:63]\n"
    "v_exp_f32 v45, v45\n"
    "v_mfma_f32_16x16x32_bf16 v[60:63], a[88:91], a[184:187], v[60:63]\n"
    "v_fma_f32 v46, v46, s36, v213\n"
    "v_fma_f32 v47, v47, s36, v213\n"
    "v_mfma_f32_16x16x32_bf16 v[60:63], a[92:95], a[188:191], v[60:63]\n"
    "s_waitcnt vmcnt(14) lgkmcnt(0)\n"
    "s_barrier\n"
    "L_ph43:\n"
    "v_mfma_f32_16x16x32_bf16 v[64:67], a[0:3], v[0:3], v[64:67]\n"
    "buffer_load_dwordx4 v222, s[20:23], s42 offen nt lds\n"
    "v_mfma_f32_16x16x32_bf16 v[80:83], a[4:7], v[0:3], v[80:83]\n"
    "ds_read_b128 a[48:51], v216 offset:36864\n"
    "v_cvt_pk_bf16_f32 v40, v40, v41\n"
    "v_mfma_f32_16x16x32_bf16 v[96:99], a[8:11], v[0:3], v[96:99]\n"
    "buffer_load_dwordx4 v223, s[20:23], s42 offen offset:1024 nt lds\n"
    "v_mfma_f32_16x16x32_bf16 v[112:115], a[12:15], v[0:3], v[112:115]\n"
    "ds_read_b128 a[52:55], v217 offset:36864\n"
    "v_cvt_pk_bf16_f32 v41, v42, v43\n"
    "v_mfma_f32_16x16x32_bf16 v[128:131], a[16:19], v[0:3], v[128:131]\n"
    "buffer_load_dwordx4 v224, s[20:23], s42 offen offset:2048 nt lds\n"
    "v_mfma_f32_16x16x32_bf16 v[144:147], a[20:23], v[0:3], v[144:147]\n"
    "ds_read_b128 a[56:59], v216 offset:36992\n"
    "s_add_u32 s43, s40, 6144\n"
    "v_mfma_f32_16x16x32_bf16 v[160:163], a[24:27], v[0:3], v[160:163]\n"
    "buffer_store_dwordx4 v[250:253], v236, s[28:31], s43 offen sc1\n"
    "v_mfma_f32_16x16x32_bf16 v[176:179], a[28:31], v[0:3], v[176:179]\n"
    "s_mov_b32 m0, 159744\n"
    "s_mov_b32 s51, L_ph47-L_code_start\n"
    "v_mfma_f32_16x16x32_bf16 a[192:195], a[32:35], v[0:3], a[192:195]\n"
    "buffer_load_dword v249, s[52:55], s51 offen lds\n"
    "v_mfma_f32_16x16x32_bf16 a[208:211], a[36:39], v[0:3], a[208:211]\n"
    "ds_read_b128 a[60:63], v217 offset:36992\n"
    "s_add_u32 m0, s37, 36864\n"
    "v_mfma_f32_16x16x32_bf16 a[224:227], a[40:43], v[0:3], a[224:227]\n"
    "ds_read_b128 a[64:67], v216 offset:37120\n"
    "s_add_u32 s42, s39, 86016\n"
    "v_mfma_f32_16x16x32_bf16 a[240:243], a[44:47], v[0:3], a[240:243]\n"
    "ds_read_b128 a[68:71], v217 offset:37120\n"
    "v_cvt_pk_bf16_f32 v42, v44, v45\n"
    "v_mfma_f32_16x16x32_bf16 v[192:195], v[208:211], v[0:3], v[192:195]\n"
    "ds_read_b128 a[72:75], v216 offset:43008\n"
    "v_fma_f32 v48, v48, s36, v214\n"
    "v_mfma_f32_16x16x32_bf16 v[68:71], a[0:3], v[8:11], v[68:71]\n"
    "ds_read_b128 a[76:79], v217 offset:43008\n"
    "v_fma_f32 v49, v49, s36, v214\n"
    "v_mfma_f32_16x16x32_bf16 v[84:87], a[4:7], v[8:11], v[84:87]\n"
    "ds_read_b128 a[80:83], v216 offset:43136\n"
    "v_fma_f32 v50, v50, s36, v214\n"
    "v_mfma_f32_16x16x32_bf16 v[100:103], a[8:11], v[8:11], v[100:103]\n"
    "ds_read_b128 a[84:87], v217 offset:43136\n"
    "v_fma_f32 v51, v51, s36, v214\n"
    "v_mfma_f32_16x16x32_bf16 v[116:119], a[12:15], v[8:11], v[116:119]\n"
    "ds_read_b128 a[88:91], v216 offset:43264\n"
    "v_fma_f32 v52, v52, s36, v214\n"
    "v_mfma_f32_16x16x32_bf16 v[132:135], a[16:19], v[8:11], v[132:135]\n"
    "ds_read_b128 a[92:95], v217 offset:43264\n"
    "v_fma_f32 v53, v53, s36, v214\n"
    "v_mfma_f32_16x16x32_bf16 v[148:151], a[20:23], v[8:11], v[148:151]\n"
    "ds_read_b128 v[250:253], v242 offset:7168\n"
    "v_fma_f32 v54, v54, s36, v214\n"
    "v_mfma_f32_16x16x32_bf16 v[164:167], a[24:27], v[8:11], v[164:167]\n"
    "v_exp_f32 v46, v46\n"
    "v_mfma_f32_16x16x32_bf16 v[180:183], a[28:31], v[8:11], v[180:183]\n"
    "v_exp_f32 v47, v47\n"
    "v_mfma_f32_16x16x32_bf16 a[196:199], a[32:35], v[8:11], a[196:199]\n"
    "v_cvt_pk_bf16_f32 v43, v46, v47\n"
    "v_fma_f32 v55, v55, s36, v214\n"
    "v_mfma_f32_16x16x32_bf16 a[212:215], a[36:39], v[8:11], a[212:215]\n"
    "v_exp_f32 v48, v48\n"
    "v_mfma_f32_16x16x32_bf16 a[228:231], a[40:43], v[8:11], a[228:231]\n"
    "v_exp_f32 v49, v49\n"
    "v_mfma_f32_16x16x32_bf16 a[244:247], a[44:47], v[8:11], a[244:247]\n"
    "v_exp_f32 v50, v50\n"
    "v_mfma_f32_16x16x32_bf16 v[196:199], v[208:211], v[8:11], v[196:199]\n"
    "v_exp_f32 v51, v51\n"
    "v_mfma_f32_16x16x32_bf16 v[72:75], a[0:3], v[16:19], v[72:75]\n"
    "v_exp_f32 v52, v52\n"
    "v_mfma_f32_16x16x32_bf16 v[88:91], a[4:7], v[16:19], v[88:91]\n"
    "v_exp_f32 v53, v53\n"
    "v_mfma_f32_16x16x32_bf16 v[104:107], a[8:11], v[16:19], v[104:107]\n"
    "v_exp_f32 v54, v54\n"
    "v_mfma_f32_16x16x32_bf16 v[120:123], a[12:15], v[16:19], v[120:123]\n"
    "v_exp_f32 v55, v55\n"
    "v_mfma_f32_16x16x32_bf16 v[136:139], a[16:19], v[16:19], v[136:139]\n"
    "v_cvt_pk_bf16_f32 v48, v48, v49\n"
    "v_cvt_pk_bf16_f32 v49, v50, v51\n"
    "v_mfma_f32_16x16x32_bf16 v[152:155], a[20:23], v[16:19], v[152:155]\n"
    "v_cvt_pk_bf16_f32 v50, v52, v53\n"
    "v_cvt_pk_bf16_f32 v51, v54, v55\n"
    "v_mfma_f32_16x16x32_bf16 v[168:171], a[24:27], v[16:19], v[168:171]\n"
    "v_fma_f32 v56, v56, s36, v215\n"
    "v_fma_f32 v57, v57, s36, v215\n"
    "v_mfma_f32_16x16x32_bf16 v[184:187], a[28:31], v[16:19], v[184:187]\n"
    "v_exp_f32 v56, v56\n"
    "v_mfma_f32_16x16x32_bf16 a[200:203], a[32:35], v[16:19], a[200:203]\n"
    "v_exp_f32 v57, v57\n"
    "v_mfma_f32_16x16x32_bf16 a[216:219], a[36:39], v[16:19], a[216:219]\n"
    "v_fma_f32 v58, v58, s36, v215\n"
    "v_fma_f32 v59, v59, s36, v215\n"
    "v_mfma_f32_16x16x32_bf16 a[232:235], a[40:43], v[16:19], a[232:235]\n"
    "v_exp_f32 v58, v58\n"
    "v_mfma_f32_16x16x32_bf16 a[248:251], a[44:47], v[16:19], a[248:251]\n"
    "v_exp_f32 v59, v59\n"
    "v_mfma_f32_16x16x32_bf16 v[200:203], v[208:211], v[16:19], v[200:203]\n"
    "v_fma_f32 v60, v60, s36, v215\n"
    "v_fma_f32 v61, v61, s36, v215\n"
    "v_mfma_f32_16x16x32_bf16 v[76:79], a[0:3], v[24:27], v[76:79]\n"
    "v_exp_f32 v60, v60\n"
    "v_mfma_f32_16x16x32_bf16 v[92:95], a[4:7], v[24:27], v[92:95]\n"
    "v_exp_f32 v61, v61\n"
    "v_mfma_f32_16x16x32_bf16 v[108:111], a[8:11], v[24:27], v[108:111]\n"
    "v_fma_f32 v62, v62, s36, v215\n"
    "v_fma_f32 v63, v63, s36, v215\n"
    "v_mfma_f32_16x16x32_bf16 v[124:127], a[12:15], v[24:27], v[124:127]\n"
    "v_exp_f32 v62, v62\n"
    "v_mfma_f32_16x16x32_bf16 v[140:143], a[16:19], v[24:27], v[140:143]\n"
    "v_exp_f32 v63, v63\n"
    "v_mfma_f32_16x16x32_bf16 v[156:159], a[20:23], v[24:27], v[156:159]\n"
    "v_cvt_pk_bf16_f32 v56, v56, v57\n"
    "v_cvt_pk_bf16_f32 v57, v58, v59\n"
    "v_mfma_f32_16x16x32_bf16 v[172:175], a[24:27], v[24:27], v[172:175]\n"
    "v_cvt_pk_bf16_f32 v58, v60, v61\n"
    "v_cvt_pk_bf16_f32 v59, v62, v63\n"
    "v_mfma_f32_16x16x32_bf16 v[188:191], a[28:31], v[24:27], v[188:191]\n"
    "v_mfma_f32_16x16x32_bf16 a[204:207], a[32:35], v[24:27], a[204:207]\n"
    "v_mfma_f32_16x16x32_bf16 a[220:223], a[36:39], v[24:27], a[220:223]\n"
    "v_mfma_f32_16x16x32_bf16 a[236:239], a[40:43], v[24:27], a[236:239]\n"
    "v_mfma_f32_16x16x32_bf16 a[252:255], a[44:47], v[24:27], a[252:255]\n"
    "v_mfma_f32_16x16x32_bf16 v[204:207], v[208:211], v[24:27], v[204:207]\n"
    "s_waitcnt vmcnt(14) lgkmcnt(0)\n"
    "s_barrier\n"
    "L_ph44:\n"
    "v_mfma_f32_16x16x32_bf16 v[0:3], a[48:51], a[96:99], 0\n"
    "buffer_load_dwordx4 v231, s[24:27], s42 offen nt lds\n"
    "v_mfma_f32_16x16x32_bf16 v[0:3], a[52:55], a[100:103], v[0:3]\n"
    "ds_read_b64_tr_b16 a[0:1], v218 offset:49152\n"
    "v_mfma_f32_16x16x32_bf16 v[0:3], a[56:59], a[104:107], v[0:3]\n"
    "buffer_load_dwordx4 v232, s[24:27], s42 offen offset:1024 nt lds\n"
    "v_mfma_f32_16x16x32_bf16 v[0:3], a[60:63], a[108:111], v[0:3]\n"
    "ds_read_b64_tr_b16 a[2:3], v218 offset:55296\n"
    "v_mfma_f32_16x16x32_bf16 v[0:3], a[64:67], a[112:115], v[0:3]\n"
    "buffer_load_dwordx4 v233, s[24:27], s42 offen offset:2048 nt lds\n"
    "v_mfma_f32_16x16x32_bf16 v[0:3], a[68:71], a[116:119], v[0:3]\n"
    "ds_read_b64_tr_b16 a[4:5], v219 offset:49152\n"
    "s_add_u32 s43, s40, 6144\n"
    "v_mfma_f32_16x16x32_bf16 v[4:7], a[72:75], a[96:99], 0\n"
    "buffer_store_dwordx4 v[250:253], v236, s[28:31], s43 offen offset:1024 sc1\n"
    "v_mfma_f32_16x16x32_bf16 v[4:7], a[76:79], a[100:103], v[4:7]\n"
    "ds_read_b64_tr_b16 a[6:7], v219 offset:55296\n"
    "s_add_u32 m0, s37, 49152\n"
    "v_mfma_f32_16x16x32_bf16 v[4:7], a[80:83], a[104:107], v[4:7]\n"
    "ds_read_b64_tr_b16 a[8:9], v220 offset:49152\n"
    "s_add_u32 s42, s39, 110592\n"
    "v_mfma_f32_16x16x32_bf16 v[4:7], a[84:87], a[108:111], v[4:7]\n"
    "ds_read_b64_tr_b16 a[10:11], v220 offset:55296\n"
    "v_mfma_f32_16x16x32_bf16 v[4:7], a[88:91], a[112:115], v[4:7]\n"
    "ds_read_b64_tr_b16 a[12:13], v221 offset:49152\n"
    "v_mfma_f32_16x16x32_bf16 v[4:7], a[92:95], a[116:119], v[4:7]\n"
    "ds_read_b64_tr_b16 a[14:15], v221 offset:55296\n"
    "v_mfma_f32_16x16x32_bf16 v[8:11], a[48:51], a[120:123], 0\n"
    "ds_read_b64_tr_b16 a[16:17], v218 offset:49280\n"
    "v_mfma_f32_16x16x32_bf16 v[8:11], a[52:55], a[124:127], v[8:11]\n"
    "ds_read_b64_tr_b16 a[18:19], v218 offset:55424\n"
    "v_fma_f32 v0, v0, s36, v212\n"
    "v_mfma_f32_16x16x32_bf16 v[8:11], a[56:59], a[128:131], v[8:11]\n"
    "ds_read_b64_tr_b16 a[20:21], v219 offset:49280\n"
    "v_fma_f32 v1, v1, s36, v212\n"
    "v_mfma_f32_16x16x32_bf16 v[8:11], a[60:63], a[132:135], v[8:11]\n"
    "ds_read_b64_tr_b16 a[22:23], v219 offset:55424\n"
    "v_fma_f32 v2, v2, s36, v212\n"
    "v_mfma_f32_16x16x32_bf16 v[8:11], a[64:67], a[136:139], v[8:11]\n"
    "ds_read_b64_tr_b16 a[24:25], v220 offset:49280\n"
    "v_fma_f32 v3, v3, s36, v212\n"
    "v_mfma_f32_16x16x32_bf16 v[8:11], a[68:71], a[140:143], v[8:11]\n"
    "ds_read_b64_tr_b16 a[26:27], v220 offset:55424\n"
    "v_mfma_f32_16x16x32_bf16 v[12:15], a[72:75], a[120:123], 0\n"
    "ds_read_b64_tr_b16 a[28:29], v221 offset:49280\n"
    "v_mfma_f32_16x16x32_bf16 v[12:15], a[76:79], a[124:127], v[12:15]\n"
    "ds_read_b64_tr_b16 a[30:31], v221 offset:55424\n"
    "v_fma_f32 v4, v4, s36, v212\n"
    "v_mfma_f32_16x16x32_bf16 v[12:15], a[80:83], a[128:131], v[12:15]\n"
    "ds_read_b64_tr_b16 a[32:33], v218 offset:49408\n"
    "v_fma_f32 v5, v5, s36, v212\n"
    "v_mfma_f32_16x16x32_bf16 v[12:15], a[84:87], a[132:135], v[12:15]\n"
    "ds_read_b64_tr_b16 a[34:35], v218 offset:55552\n"
    "v_fma_f32 v6, v6, s36, v212\n"
    "v_mfma_f32_16x16x32_bf16 v[12:15], a[88:91], a[136:139], v[12:15]\n"
    "ds_read_b64_tr_b16 a[36:37], v219 offset:49408\n"
    "v_fma_f32 v7, v7, s36, v212\n"
    "v_mfma_f32_16x16x32_bf16 v[12:15], a[92:95], a[140:143], v[12:15]\n"
    "ds_read_b64_tr_b16 a[38:39], v219 offset:55552\n"
    "v_mfma_f32_16x16x32_bf16 v[16:19], a[48:51], a[144:147], 0\n"
    "ds_read_b64_tr_b16 a[40:41], v220 offset:49408\n"
    "v_mfma_f32_16x16x32_bf16 v[16:19], a[52:55], a[148:151], v[16:19]\n"
    "ds_read_b64_tr_b16 a[42:43], v220 offset:55552\n"
    "v_fma_f32 v8, v8, s36, v213\n"
    "v_mfma_f32_16x16x32_bf16 v[16:19], a[56:59], a[152:155], v[16:19]\n"
    "ds_read_b64_tr_b16 a[44:45], v221 offset:49408\n"
    "v_fma_f32 v9, v9, s36, v213\n"
    "v_mfma_f32_16x16x32_bf16 v[16:19], a[60:63], a[156:159], v[16:19]\n"
    "ds_read_b64_tr_b16 a[46:47], v221 offset:55552\n"
    "v_fma_f32 v10, v10, s36, v213\n"
    "v_mfma_f32_16x16x32_bf16 v[16:19], a[64:67], a[160:163], v[16:19]\n"
    "ds_read_b128 v[250:253], v243 offset:8192\n"
    "v_fma_f32 v11, v11, s36, v213\n"
    "v_mfma_f32_16x16x32_bf16 v[16:19], a[68:71], a[164:167], v[16:19]\n"
    "v_exp_f32 v0, v0\n"
    "v_mfma_f32_16x16x32_bf16 v[20:23], a[72:75], a[144:147], 0\n"
    "v_exp_f32 v1, v1\n"
    "v_mfma_f32_16x16x32_bf16 v[20:23], a[76:79], a[148:151], v[20:23]\n"
    "v_exp_f32 v2, v2\n"
    "v_mfma_f32_16x16x32_bf16 v[20:23], a[80:83], a[152:155], v[20:23]\n"
    "v_exp_f32 v3, v3\n"
    "v_mfma_f32_16x16x32_bf16 v[20:23], a[84:87], a[156:159], v[20:23]\n"
    "v_exp_f32 v4, v4\n"
    "v_mfma_f32_16x16x32_bf16 v[20:23], a[88:91], a[160:163], v[20:23]\n"
    "v_exp_f32 v5, v5\n"
    "v_mfma_f32_16x16x32_bf16 v[20:23], a[92:95], a[164:167], v[20:23]\n"
    "v_exp_f32 v6, v6\n"
    "v_mfma_f32_16x16x32_bf16 v[24:27], a[48:51], a[168:171], 0\n"
    "v_exp_f32 v7, v7\n"
    "v_mfma_f32_16x16x32_bf16 v[24:27], a[52:55], a[172:175], v[24:27]\n"
    "v_cvt_pk_bf16_f32 v0, v0, v1\n"
    "v_cvt_pk_bf16_f32 v1, v2, v3\n"
    "v_mfma_f32_16x16x32_bf16 v[24:27], a[56:59], a[176:179], v[24:27]\n"
    "v_cvt_pk_bf16_f32 v2, v4, v5\n"
    "v_cvt_pk_bf16_f32 v3, v6, v7\n"
    "v_mfma_f32_16x16x32_bf16 v[24:27], a[60:63], a[180:183], v[24:27]\n"
    "v_exp_f32 v8, v8\n"
    "v_mfma_f32_16x16x32_bf16 v[24:27], a[64:67], a[184:187], v[24:27]\n"
    "v_exp_f32 v9, v9\n"
    "v_mfma_f32_16x16x32_bf16 v[24:27], a[68:71], a[188:191], v[24:27]\n"
    "v_exp_f32 v10, v10\n"
    "v_mfma_f32_16x16x32_bf16 v[28:31], a[72:75], a[168:171], 0\n"
    "v_exp_f32 v11, v11\n"
    "v_mfma_f32_16x16x32_bf16 v[28:31], a[76:79], a[172:175], v[28:31]\n"
    "v_fma_f32 v12, v12, s36, v213\n"
    "v_fma_f32 v13, v13, s36, v213\n"
    "v_mfma_f32_16x16x32_bf16 v[28:31], a[80:83], a[176:179], v[28:31]\n"
    "v_exp_f32 v12, v12\n"
    "v_mfma_f32_16x16x32_bf16 v[28:31], a[84:87], a[180:183], v[28:31]\n"
    "v_exp_f32 v13, v13\n"
    "v_mfma_f32_16x16x32_bf16 v[28:31], a[88:91], a[184:187], v[28:31]\n"
    "v_fma_f32 v14, v14, s36, v213\n"
    "v_fma_f32 v15, v15, s36, v213\n"
    "v_mfma_f32_16x16x32_bf16 v[28:31], a[92:95], a[188:191], v[28:31]\n"
    "s_waitcnt vmcnt(14) lgkmcnt(0)\n"
    "s_barrier\n"
    "L_ph45:\n"
    "v_mfma_f32_16x16x32_bf16 v[64:67], a[0:3], v[32:35], v[64:67]\n"
    "buffer_load_dwordx4 v222, s[20:23], s42 offen nt lds\n"
    "v_mfma_f32_16x16x32_bf16 v[80:83], a[4:7], v[32:35], v[80:83]\n"
    "ds_read_b128 a[48:51], v216 offset:0\n"
    "v_cvt_pk_bf16_f32 v8, v8, v9\n"
    "v_mfma_f32_16x16x32_bf16 v[96:99], a[8:11], v[32:35], v[96:99]\n"
    "buffer_load_dwordx4 v223, s[20:23], s42 offen offset:1024 nt lds\n"
    "v_mfma_f32_16x16x32_bf16 v[112:115], a[12:15], v[32:35], v[112:115]\n"
    "ds_read_b128 a[52:55], v217 offset:0\n"
    "v_cvt_pk_bf16_f32 v9, v10, v11\n"
    "v_mfma_f32_16x16x32_bf16 v[128:131], a[16:19], v[32:35], v[128:131]\n"
    "buffer_load_dwordx4 v224, s[20:23], s42 offen offset:2048 nt lds\n"
    "v_mfma_f32_16x16x32_bf16 v[144:147], a[20:23], v[32:35], v[144:147]\n"
    "ds_read_b128 a[56:59], v216 offset:128\n"
    "s_add_u32 s43, s40, 6144\n"
    "v_mfma_f32_16x16x32_bf16 v[160:163], a[24:27], v[32:35], v[160:163]\n"
    "buffer_store_dwordx4 v[250:253], v236, s[28:31], s43 offen offset:2048 sc1\n"
    "v_mfma_f32_16x16x32_bf16 v[176:179], a[28:31], v[32:35], v[176:179]\n"
    "ds_read_b128 a[60:63], v217 offset:128\n"
    "s_add_u32 m0, s37, 0\n"
    "v_mfma_f32_16x16x32_bf16 a[192:195], a[32:35], v[32:35], a[192:195]\n"
    "ds_read_b128 a[64:67], v216 offset:256\n"
    "s_add_u32 s42, s39, 98304\n"
    "v_mfma_f32_16x16x32_bf16 a[208:211], a[36:39], v[32:35], a[208:211]\n"
    "ds_read_b128 a[68:71], v217 offset:256\n"
    "v_cvt_pk_bf16_f32 v10, v12, v13\n"
    "v_mfma_f32_16x16x32_bf16 a[224:227], a[40:43], v[32:35], a[224:227]\n"
    "ds_read_b128 a[72:75], v216 offset:6144\n"
    "v_fma_f32 v16, v16, s36, v214\n"
    "v_mfma_f32_16x16x32_bf16 a[240:243], a[44:47], v[32:35], a[240:243]\n"
    "ds_read_b128 a[76:79], v217 offset:6144\n"
    "v_fma_f32 v17, v17, s36, v214\n"
    "v_mfma_f32_16x16x32_bf16 v[192:195], v[208:211], v[32:35], v[192:195]\n"
    "ds_read_b128 a[80:83], v216 offset:6272\n"
    "v_fma_f32 v18, v18, s36, v214\n"
    "v_mfma_f32_16x16x32_bf16 v[68:71], a[0:3], v[40:43], v[68:71]\n"
    "ds_read_b128 a[84:87], v217 offset:6272\n"
    "v_fma_f32 v19, v19, s36, v214\n"
    "v_mfma_f32_16x16x32_bf16 v[84:87], a[4:7], v[40:43], v[84:87]\n"
    "ds_read_b128 a[88:91], v216 offset:6400\n"
    "v_fma_f32 v20, v20, s36, v214\n"
    "v_mfma_f32_16x16x32_bf16 v[100:103], a[8:11], v[40:43], v[100:103]\n"
    "ds_read_b128 a[92:95], v217 offset:6400\n"
    "v_fma_f32 v21, v21, s36, v214\n"
    "v_mfma_f32_16x16x32_bf16 v[116:119], a[12:15], v[40:43], v[116:119]\n"
    "ds_read_b128 v[250:253], v244 offset:9216\n"
    "v_fma_f32 v22, v22, s36, v214\n"
    "v_mfma_f32_16x16x32_bf16 v[132:135], a[16:19], v[40:43], v[132:135]\n"
    "v_exp_f32 v14, v14\n"
    "v_mfma_f32_16x16x32_bf16 v[148:151], a[20:23], v[40:43], v[148:151]\n"
    "v_exp_f32 v15, v15\n"
    "v_mfma_f32_16x16x32_bf16 v[164:167], a[24:27], v[40:43], v[164:167]\n"
    "v_cvt_pk_bf16_f32 v11, v14, v15\n"
    "v_fma_f32 v23, v23, s36, v214\n"
    "v_mfma_f32_16x16x32_bf16 v[180:183], a[28:31], v[40:43], v[180:183]\n"
    "v_exp_f32 v16, v16\n"
    "v_mfma_f32_16x16x32_bf16 a[196:199], a[32:35], v[40:43], a[196:199]\n"
    "v_exp_f32 v17, v17\n"
    "v_mfma_f32_16x16x32_bf16 a[212:215], a[36:39], v[40:43], a[212:215]\n"
    "v_exp_f32 v18, v18\n"
    "v_mfma_f32_16x16x32_bf16 a[228:231], a[40:43], v[40:43], a[228:231]\n"
    "v_exp_f32 v19, v19\n"
    "v_mfma_f32_16x16x32_bf16 a[244:247], a[44:47], v[40:43], a[244:247]\n"
    "v_exp_f32 v20, v20\n"
    "v_mfma_f32_16x16x32_bf16 v[196:199], v[208:211], v[40:43], v[196:199]\n"
    "v_exp_f32 v21, v21\n"
    "v_mfma_f32_16x16x32_bf16 v[72:75], a[0:3], v[48:51], v[72:75]\n"
    "v_exp_f32 v22, v22\n"
    "v_mfma_f32_16x16x32_bf16 v[88:91], a[4:7], v[48:51], v[88:91]\n"
    "v_exp_f32 v23, v23\n"
    "v_mfma_f32_16x16x32_bf16 v[104:107], a[8:11], v[48:51], v[104:107]\n"
    "v_cvt_pk_bf16_f32 v16, v16, v17\n"
    "v_cvt_pk_bf16_f32 v17, v18, v19\n"
    "v_mfma_f32_16x16x32_bf16 v[120:123], a[12:15], v[48:51], v[120:123]\n"
    "v_cvt_pk_bf16_f32 v18, v20, v21\n"
    "v_cvt_pk_bf16_f32 v19, v22, v23\n"
    "v_mfma_f32_16x16x32_bf16 v[136:139], a[16:19], v[48:51], v[136:139]\n"
    "v_fma_f32 v24, v24, s36, v215\n"
    "v_fma_f32 v25, v25, s36, v215\n"
    "v_mfma_f32_16x16x32_bf16 v[152:155], a[20:23], v[48:51], v[152:155]\n"
    "v_exp_f32 v24, v24\n"
    "v_mfma_f32_16x16x32_bf16 v[168:171], a[24:27], v[48:51], v[168:171]\n"
    "v_exp_f32 v25, v25\n"
    "v_mfma_f32_16x16x32_bf16 v[184:187], a[28:31], v[48:51], v[184:187]\n"
    "v_fma_f32 v26, v26, s36, v215\n"
    "v_fma_f32 v27, v27, s36, v215\n"
    "v_mfma_f32_16x16x32_bf16 a[200:203], a[32:35], v[48:51], a[200:203]\n"
    "v_exp_f32 v26, v26\n"
    "v_mfma_f32_16x16x32_bf16 a[216:219], a[36:39], v[48:51], a[216:219]\n"
    "v_exp_f32 v27, v27\n"
    "v_mfma_f32_16x16x32_bf16 a[232:235], a[40:43], v[48:51], a[232:235]\n"
    "v_fma_f32 v28, v28, s36, v215\n"
    "v_fma_f32 v29, v29, s36, v215\n"
    "v_mfma_f32_16x16x32_bf16 a[248:251], a[44:47], v[48:51], a[248:251]\n"
    "v_exp_f32 v28, v28\n"
    "v_mfma_f32_16x16x32_bf16 v[200:203], v[208:211], v[48:51], v[200:203]\n"
    "v_exp_f32 v29, v29\n"
    "v_mfma_f32_16x16x32_bf16 v[76:79], a[0:3], v[56:59], v[76:79]\n"
    "v_fma_f32 v30, v30, s36, v215\n"
    "v_fma_f32 v31, v31, s36, v215\n"
    "v_mfma_f32_16x16x32_bf16 v[92:95], a[4:7], v[56:59], v[92:95]\n"
    "v_exp_f32 v30, v30\n"
    "v_mfma_f32_16x16x32_bf16 v[108:111], a[8:11], v[56:59], v[108:111]\n"
    "v_exp_f32 v31, v31\n"
    "v_mfma_f32_16x16x32_bf16 v[124:127], a[12:15], v[56:59], v[124:127]\n"
    "v_cvt_pk_bf16_f32 v24, v24, v25\n"
    "v_cvt_pk_bf16_f32 v25, v26, v27\n"
    "v_mfma_f32_16x16x32_bf16 v[140:143], a[16:19], v[56:59], v[140:143]\n"
    "v_cvt_pk_bf16_f32 v26, v28, v29\n"
    "v_cvt_pk_bf16_f32 v27, v30, v31\n"
    "v_mfma_f32_16x16x32_bf16 v[156:159], a[20:23], v[56:59], v[156:159]\n"
    "v_mfma_f32_16x16x32_bf16 v[172:175], a[24:27], v[56:59], v[172:175]\n"
    "v_mfma_f32_16x16x32_bf16 v[188:191], a[28:31], v[56:59], v[188:191]\n"
    "v_mfma_f32_16x16x32_bf16 a[204:207], a[32:35], v[56:59], a[204:207]\n"
    "v_mfma_f32_16x16x32_bf16 a[220:223], a[36:39], v[56:59], a[220:223]\n"
    "v_mfma_f32_16x16x32_bf16 a[236:239], a[40:43], v[56:59], a[236:239]\n"
    "v_mfma_f32_16x16x32_bf16 a[252:255], a[44:47], v[56:59], a[252:255]\n"
    "v_mfma_f32_16x16x32_bf16 v[204:207], v[208:211], v[56:59], v[204:207]\n"
    "s_waitcnt vmcnt(14) lgkmcnt(0)\n"
    "s_barrier\n"
    "L_ph46:\n"
    "v_mfma_f32_16x16x32_bf16 v[32:35], a[48:51], a[96:99], 0\n"
    "buffer_load_dwordx4 v231, s[24:27], s42 offen nt lds\n"
    "v_mfma_f32_16x16x32_bf16 v[32:35], a[52:55], a[100:103], v[32:35]\n"
    "ds_read_b64_tr_b16 a[0:1], v218 offset:12288\n"
    "v_mfma_f32_16x16x32_bf16 v[32:35], a[56:59], a[104:107], v[32:35]\n"
    "buffer_load_dwordx4 v232, s[24:27], s42 offen offset:1024 nt lds\n"
    "v_mfma_f32_16x16x32_bf16 v[32:35], a[60:63], a[108:111], v[32:35]\n"
    "ds_read_b64_tr_b16 a[2:3], v218 offset:18432\n"
    "v_mfma_f32_16x16x32_bf16 v[32:35], a[64:67], a[112:115], v[32:35]\n"
    "buffer_load_dwordx4 v233, s[24:27], s42 offen offset:2048 nt lds\n"
    "v_mfma_f32_16x16x32_bf16 v[32:35], a[68:71], a[116:119], v[32:35]\n"
    "ds_read_b64_tr_b16 a[4:5], v219 offset:12288\n"
    "s_add_u32 s43, s40, 9216\n"
    "v_mfma_f32_16x16x32_bf16 v[36:39], a[72:75], a[96:99], 0\n"
    "buffer_store_dwordx4 v[250:253], v236, s[28:31], s43 offen sc1\n"
    "v_mfma_f32_16x16x32_bf16 v[36:39], a[76:79], a[100:103], v[36:39]\n"
    "ds_read_b64_tr_b16 a[6:7], v219 offset:18432\n"
    "s_add_u32 m0, s37, 12288\n"
    "v_mfma_f32_16x16x32_bf16 v[36:39], a[80:83], a[104:107], v[36:39]\n"
    "ds_read_b64_tr_b16 a[8:9], v220 offset:12288\n"
    "s_add_u32 s42, s39, 122880\n"
    "v_mfma_f32_16x16x32_bf16 v[36:39], a[84:87], a[108:111], v[36:39]\n"
    "ds_read_b64_tr_b16 a[10:11], v220 offset:18432\n"
    "v_mfma_f32_16x16x32_bf16 v[36:39], a[88:91], a[112:115], v[36:39]\n"
    "ds_read_b64_tr_b16 a[12:13], v221 offset:12288\n"
    "v_mfma_f32_16x16x32_bf16 v[36:39], a[92:95], a[116:119], v[36:39]\n"
    "ds_read_b64_tr_b16 a[14:15], v221 offset:18432\n"
    "v_mfma_f32_16x16x32_bf16 v[40:43], a[48:51], a[120:123], 0\n"
    "ds_read_b64_tr_b16 a[16:17], v218 offset:12416\n"
    "v_mfma_f32_16x16x32_bf16 v[40:43], a[52:55], a[124:127], v[40:43]\n"
    "ds_read_b64_tr_b16 a[18:19], v218 offset:18560\n"
    "v_fma_f32 v32, v32, s36, v212\n"
    "v_mfma_f32_16x16x32_bf16 v[40:43], a[56:59], a[128:131], v[40:43]\n"
    "ds_read_b64_tr_b16 a[20:21], v219 offset:12416\n"
    "v_fma_f32 v33, v33, s36, v212\n"
    "v_mfma_f32_16x16x32_bf16 v[40:43], a[60:63], a[132:135], v[40:43]\n"
    "ds_read_b64_tr_b16 a[22:23], v219 offset:18560\n"
    "v_fma_f32 v34, v34, s36, v212\n"
    "v_mfma_f32_16x16x32_bf16 v[40:43], a[64:67], a[136:139], v[40:43]\n"
    "ds_read_b64_tr_b16 a[24:25], v220 offset:12416\n"
    "v_fma_f32 v35, v35, s36, v212\n"
    "v_mfma_f32_16x16x32_bf16 v[40:43], a[68:71], a[140:143], v[40:43]\n"
    "ds_read_b64_tr_b16 a[26:27], v220 offset:18560\n"
    "v_mfma_f32_16x16x32_bf16 v[44:47], a[72:75], a[120:123], 0\n"
    "ds_read_b64_tr_b16 a[28:29], v221 offset:12416\n"
    "v_mfma_f32_16x16x32_bf16 v[44:47], a[76:79], a[124:127], v[44:47]\n"
    "ds_read_b64_tr_b16 a[30:31], v221 offset:18560\n"
    "v_fma_f32 v36, v36, s36, v212\n"
    "v_mfma_f32_16x16x32_bf16 v[44:47], a[80:83], a[128:131], v[44:47]\n"
    "ds_read_b64_tr_b16 a[32:33], v218 offset:12544\n"
    "v_fma_f32 v37, v37, s36, v212\n"
    "v_mfma_f32_16x16x32_bf16 v[44:47], a[84:87], a[132:135], v[44:47]\n"
    "ds_read_b64_tr_b16 a[34:35], v218 offset:18688\n"
    "v_fma_f32 v38, v38, s36, v212\n"
    "v_mfma_f32_16x16x32_bf16 v[44:47], a[88:91], a[136:139], v[44:47]\n"
    "ds_read_b64_tr_b16 a[36:37], v219 offset:12544\n"
    "v_fma_f32 v39, v39, s36, v212\n"
    "v_mfma_f32_16x16x32_bf16 v[44:47], a[92:95], a[140:143], v[44:47]\n"
    "ds_read_b64_tr_b16 a[38:39], v219 offset:18688\n"
    "v_mfma_f32_16x16x32_bf16 v[48:51], a[48:51], a[144:147], 0\n"
    "ds_read_b64_tr_b16 a[40:41], v220 offset:12544\n"
    "v_mfma_f32_16x16x32_bf16 v[48:51], a[52:55], a[148:151], v[48:51]\n"
    "ds_read_b64_tr_b16 a[42:43], v220 offset:18688\n"
    "v_fma_f32 v40, v40, s36, v213\n"
    "v_mfma_f32_16x16x32_bf16 v[48:51], a[56:59], a[152:155], v[48:51]\n"
    "ds_read_b64_tr_b16 a[44:45], v221 offset:12544\n"
    "v_fma_f32 v41, v41, s36, v213\n"
    "v_mfma_f32_16x16x32_bf16 v[48:51], a[60:63], a[156:159], v[48:51]\n"
    "ds_read_b64_tr_b16 a[46:47], v221 offset:18688\n"
    "v_fma_f32 v42, v42, s36, v213\n"
    "v_mfma_f32_16x16x32_bf16 v[48:51], a[64:67], a[160:163], v[48:51]\n"
    "ds_read_b128 v[250:253], v245 offset:10240\n"
    "v_fma_f32 v43, v43, s36, v213\n"
    "v_mfma_f32_16x16x32_bf16 v[48:51], a[68:71], a[164:167], v[48:51]\n"
    "v_exp_f32 v32, v32\n"
    "v_mfma_f32_16x16x32_bf16 v[52:55], a[72:75], a[144:147], 0\n"
    "v_exp_f32 v33, v33\n"
    "v_mfma_f32_16x16x32_bf16 v[52:55], a[76:79], a[148:151], v[52:55]\n"
    "v_exp_f32 v34, v34\n"
    "v_mfma_f32_16x16x32_bf16 v[52:55], a[80:83], a[152:155], v[52:55]\n"
    "v_exp_f32 v35, v35\n"
    "v_mfma_f32_16x16x32_bf16 v[52:55], a[84:87], a[156:159], v[52:55]\n"
    "v_exp_f32 v36, v36\n"
    "v_mfma_f32_16x16x32_bf16 v[52:55], a[88:91], a[160:163], v[52:55]\n"
    "v_exp_f32 v37, v37\n"
    "v_mfma_f32_16x16x32_bf16 v[52:55], a[92:95], a[164:167], v[52:55]\n"
    "v_exp_f32 v38, v38\n"
    "v_mfma_f32_16x16x32_bf16 v[56:59], a[48:51], a[168:171], 0\n"
    "v_exp_f32 v39, v39\n"
    "v_mfma_f32_16x16x32_bf16 v[56:59], a[52:55], a[172:175], v[56:59]\n"
    "v_cvt_pk_bf16_f32 v32, v32, v33\n"
    "v_cvt_pk_bf16_f32 v33, v34, v35\n"
    "v_mfma_f32_16x16x32_bf16 v[56:59], a[56:59], a[176:179], v[56:59]\n"
    "v_cvt_pk_bf16_f32 v34, v36, v37\n"
    "v_cvt_pk_bf16_f32 v35, v38, v39\n"
    "v_mfma_f32_16x16x32_bf16 v[56:59], a[60:63], a[180:183], v[56:59]\n"
    "v_exp_f32 v40, v40\n"
    "v_mfma_f32_16x16x32_bf16 v[56:59], a[64:67], a[184:187], v[56:59]\n"
    "v_exp_f32 v41, v41\n"
    "v_mfma_f32_16x16x32_bf16 v[56:59], a[68:71], a[188:191], v[56:59]\n"
    "v_exp_f32 v42, v42\n"
    "v_mfma_f32_16x16x32_bf16 v[60:63], a[72:75], a[168:171], 0\n"
    "v_exp_f32 v43, v43\n"
    "v_mfma_f32_16x16x32_bf16 v[60:63], a[76:79], a[172:175], v[60:63]\n"
    "v_fma_f32 v44, v44, s36, v213\n"
    "v_fma_f32 v45, v45, s36, v213\n"
    "v_mfma_f32_16x16x32_bf16 v[60:63], a[80:83], a[176:179], v[60:63]\n"
    "v_exp_f32 v44, v44\n"
    "v_mfma_f32_16x16x32_bf16 v[60:63], a[84:87], a[180:183], v[60:63]\n"
    "v_exp_f32 v45, v45\n"
    "v_mfma_f32_16x16x32_bf16 v[60:63], a[88:91], a[184:187], v[60:63]\n"
    "v_fma_f32 v46, v46, s36, v213\n"
    "v_fma_f32 v47, v47, s36, v213\n"
    "v_mfma_f32_16x16x32_bf16 v[60:63], a[92:95], a[188:191], v[60:63]\n"
    "s_waitcnt vmcnt(14) lgkmcnt(0)\n"
    "s_barrier\n"
    "L_ph47:\n"
    "v_mfma_f32_16x16x32_bf16 v[64:67], a[0:3], v[0:3], v[64:67]\n"
    "buffer_load_dwordx4 v222, s[20:23], s42 offen nt lds\n"
    "v_mfma_f32_16x16x32_bf16 v[80:83], a[4:7], v[0:3], v[80:83]\n"
    "ds_read_b128 a[48:51], v216 offset:24576\n"
    "v_cvt_pk_bf16_f32 v40, v40, v41\n"
    "v_mfma_f32_16x16x32_bf16 v[96:99], a[8:11], v[0:3], v[96:99]\n"
    "buffer_load_dwordx4 v223, s[20:23], s42 offen offset:1024 nt lds\n"
    "v_mfma_f32_16x16x32_bf16 v[112:115], a[12:15], v[0:3], v[112:115]\n"
    "ds_read_b128 a[52:55], v217 offset:24576\n"
    "v_cvt_pk_bf16_f32 v41, v42, v43\n"
    "v_mfma_f32_16x16x32_bf16 v[128:131], a[16:19], v[0:3], v[128:131]\n"
    "buffer_load_dwordx4 v224, s[20:23], s42 offen offset:2048 nt lds\n"
    "v_mfma_f32_16x16x32_bf16 v[144:147], a[20:23], v[0:3], v[144:147]\n"
    "ds_read_b128 a[56:59], v216 offset:24704\n"
    "s_add_u32 s43, s40, 9216\n"
    "v_mfma_f32_16x16x32_bf16 v[160:163], a[24:27], v[0:3], v[160:163]\n"
    "buffer_store_dwordx4 v[250:253], v236, s[28:31], s43 offen offset:1024 sc1\n"
    "v_mfma_f32_16x16x32_bf16 v[176:179], a[28:31], v[0:3], v[176:179]\n"
    "s_mov_b32 m0, 159744\n"
    "s_mov_b32 s51, L_ph51-L_code_start\n"
    "v_mfma_f32_16x16x32_bf16 a[192:195], a[32:35], v[0:3], a[192:195]\n"
    "buffer_load_dword v249, s[52:55], s51 offen lds\n"
    "v_mfma_f32_16x16x32_bf16 a[208:211], a[36:39], v[0:3], a[208:211]\n"
    "ds_read_b128 a[60:63], v217 offset:24704\n"
    "s_add_u32 m0, s37, 24576\n"
    "v_mfma_f32_16x16x32_bf16 a[224:227], a[40:43], v[0:3], a[224:227]\n"
    "ds_read_b128 a[64:67], v216 offset:24832\n"
    "s_add_u32 s42, s39, 110592\n"
    "v_mfma_f32_16x16x32_bf16 a[240:243], a[44:47], v[0:3], a[240:243]\n"
    "ds_read_b128 a[68:71], v217 offset:24832\n"
    "v_cvt_pk_bf16_f32 v42, v44, v45\n"
    "v_mfma_f32_16x16x32_bf16 v[192:195], v[208:211], v[0:3], v[192:195]\n"
    "ds_read_b128 a[72:75], v216 offset:30720\n"
    "v_fma_f32 v48, v48, s36, v214\n"
    "v_mfma_f32_16x16x32_bf16 v[68:71], a[0:3], v[8:11], v[68:71]\n"
    "ds_read_b128 a[76:79], v217 offset:30720\n"
    "v_fma_f32 v49, v49, s36, v214\n"
    "v_mfma_f32_16x16x32_bf16 v[84:87], a[4:7], v[8:11], v[84:87]\n"
    "ds_read_b128 a[80:83], v216 offset:30848\n"
    "v_fma_f32 v50, v50, s36, v214\n"
    "v_mfma_f32_16x16x32_bf16 v[100:103], a[8:11], v[8:11], v[100:103]\n"
    "ds_read_b128 a[84:87], v217 offset:30848\n"
    "v_fma_f32 v51, v51, s36, v214\n"
    "v_mfma_f32_16x16x32_bf16 v[116:119], a[12:15], v[8:11], v[116:119]\n"
    "ds_read_b128 a[88:91], v216 offset:30976\n"
    "v_fma_f32 v52, v52, s36, v214\n"
    "v_mfma_f32_16x16x32_bf16 v[132:135], a[16:19], v[8:11], v[132:135]\n"
    "ds_read_b128 a[92:95], v217 offset:30976\n"
    "v_fma_f32 v53, v53, s36, v214\n"
    "v_mfma_f32_16x16x32_bf16 v[148:151], a[20:23], v[8:11], v[148:151]\n"
    "ds_read_b128 v[250:253], v246 offset:11264\n"
    "v_fma_f32 v54, v54, s36, v214\n"
    "v_mfma_f32_16x16x32_bf16 v[164:167], a[24:27], v[8:11], v[164:167]\n"
    "v_exp_f32 v46, v46\n"
    "v_mfma_f32_16x16x32_bf16 v[180:183], a[28:31], v[8:11], v[180:183]\n"
    "v_exp_f32 v47, v47\n"
    "v_mfma_f32_16x16x32_bf16 a[196:199], a[32:35], v[8:11], a[196:199]\n"
    "v_cvt_pk_bf16_f32 v43, v46, v47\n"
    "v_fma_f32 v55, v55, s36, v214\n"
    "v_mfma_f32_16x16x32_bf16 a[212:215], a[36:39], v[8:11], a[212:215]\n"
    "v_exp_f32 v48, v48\n"
    "v_mfma_f32_16x16x32_bf16 a[228:231], a[40:43], v[8:11], a[228:231]\n"
    "v_exp_f32 v49, v49\n"
    "v_mfma_f32_16x16x32_bf16 a[244:247], a[44:47], v[8:11], a[244:247]\n"
    "v_exp_f32 v50, v50\n"
    "v_mfma_f32_16x16x32_bf16 v[196:199], v[208:211], v[8:11], v[196:199]\n"
    "v_exp_f32 v51, v51\n"
    "v_mfma_f32_16x16x32_bf16 v[72:75], a[0:3], v[16:19], v[72:75]\n"
    "v_exp_f32 v52, v52\n"
    "v_mfma_f32_16x16x32_bf16 v[88:91], a[4:7], v[16:19], v[88:91]\n"
    "v_exp_f32 v53, v53\n"
    "v_mfma_f32_16x16x32_bf16 v[104:107], a[8:11], v[16:19], v[104:107]\n"
    "v_exp_f32 v54, v54\n"
    "v_mfma_f32_16x16x32_bf16 v[120:123], a[12:15], v[16:19], v[120:123]\n"
    "v_exp_f32 v55, v55\n"
    "v_mfma_f32_16x16x32_bf16 v[136:139], a[16:19], v[16:19], v[136:139]\n"
    "v_cvt_pk_bf16_f32 v48, v48, v49\n"
    "v_cvt_pk_bf16_f32 v49, v50, v51\n"
    "v_mfma_f32_16x16x32_bf16 v[152:155], a[20:23], v[16:19], v[152:155]\n"
    "v_cvt_pk_bf16_f32 v50, v52, v53\n"
    "v_cvt_pk_bf16_f32 v51, v54, v55\n"
    "v_mfma_f32_16x16x32_bf16 v[168:171], a[24:27], v[16:19], v[168:171]\n"
    "v_fma_f32 v56, v56, s36, v215\n"
    "v_fma_f32 v57, v57, s36, v215\n"
    "v_mfma_f32_16x16x32_bf16 v[184:187], a[28:31], v[16:19], v[184:187]\n"
    "v_exp_f32 v56, v56\n"
    "v_mfma_f32_16x16x32_bf16 a[200:203], a[32:35], v[16:19], a[200:203]\n"
    "v_exp_f32 v57, v57\n"
    "v_mfma_f32_16x16x32_bf16 a[216:219], a[36:39], v[16:19], a[216:219]\n"
    "v_fma_f32 v58, v58, s36, v215\n"
    "v_fma_f32 v59, v59, s36, v215\n"
    "v_mfma_f32_16x16x32_bf16 a[232:235], a[40:43], v[16:19], a[232:235]\n"
    "v_exp_f32 v58, v58\n"
    "v_mfma_f32_16x16x32_bf16 a[248:251], a[44:47], v[16:19], a[248:251]\n"
    "v_exp_f32 v59, v59\n"
    "v_mfma_f32_16x16x32_bf16 v[200:203], v[208:211], v[16:19], v[200:203]\n"
    "v_fma_f32 v60, v60, s36, v215\n"
    "v_fma_f32 v61, v61, s36, v215\n"
    "v_mfma_f32_16x16x32_bf16 v[76:79], a[0:3], v[24:27], v[76:79]\n"
    "v_exp_f32 v60, v60\n"
    "v_mfma_f32_16x16x32_bf16 v[92:95], a[4:7], v[24:27], v[92:95]\n"
    "v_exp_f32 v61, v61\n"
    "v_mfma_f32_16x16x32_bf16 v[108:111], a[8:11], v[24:27], v[108:111]\n"
    "v_fma_f32 v62, v62, s36, v215\n"
    "v_fma_f32 v63, v63, s36, v215\n"
    "v_mfma_f32_16x16x32_bf16 v[124:127], a[12:15], v[24:27], v[124:127]\n"
    "v_exp_f32 v62, v62\n"
    "v_mfma_f32_16x16x32_bf16 v[140:143], a[16:19], v[24:27], v[140:143]\n"
    "v_exp_f32 v63, v63\n"
    "v_mfma_f32_16x16x32_bf16 v[156:159], a[20:23], v[24:27], v[156:159]\n"
    "v_cvt_pk_bf16_f32 v56, v56, v57\n"
    "v_cvt_pk_bf16_f32 v57, v58, v59\n"
    "v_mfma_f32_16x16x32_bf16 v[172:175], a[24:27], v[24:27], v[172:175]\n"
    "v_cvt_pk_bf16_f32 v58, v60, v61\n"
    "v_cvt_pk_bf16_f32 v59, v62, v63\n"
    "v_mfma_f32_16x16x32_bf16 v[188:191], a[28:31], v[24:27], v[188:191]\n"
    "v_mfma_f32_16x16x32_bf16 a[204:207], a[32:35], v[24:27], a[204:207]\n"
    "v_mfma_f32_16x16x32_bf16 a[220:223], a[36:39], v[24:27], a[220:223]\n"
    "v_mfma_f32_16x16x32_bf16 a[236:239], a[40:43], v[24:27], a[236:239]\n"
    "v_mfma_f32_16x16x32_bf16 a[252:255], a[44:47], v[24:27], a[252:255]\n"
    "v_mfma_f32_16x16x32_bf16 v[204:207], v[208:211], v[24:27], v[204:207]\n"
    "s_waitcnt vmcnt(14) lgkmcnt(0)\n"
    "s_barrier\n"
    "L_ph48:\n"
    "v_mfma_f32_16x16x32_bf16 v[0:3], a[48:51], a[96:99], 0\n"
    "buffer_load_dwordx4 v231, s[24:27], s42 offen nt lds\n"
    "v_mfma_f32_16x16x32_bf16 v[0:3], a[52:55], a[100:103], v[0:3]\n"
    "ds_read_b64_tr_b16 a[0:1], v218 offset:36864\n"
    "v_mfma_f32_16x16x32_bf16 v[0:3], a[56:59], a[104:107], v[0:3]\n"
    "buffer_load_dwordx4 v232, s[24:27], s42 offen offset:1024 nt lds\n"
    "v_mfma_f32_16x16x32_bf16 v[0:3], a[60:63], a[108:111], v[0:3]\n"
    "ds_read_b64_tr_b16 a[2:3], v218 offset:43008\n"
    "v_mfma_f32_16x16x32_bf16 v[0:3], a[64:67], a[112:115], v[0:3]\n"
    "buffer_load_dwordx4 v233, s[24:27], s42 offen offset:2048 nt lds\n"
    "v_mfma_f32_16x16x32_bf16 v[0:3], a[68:71], a[116:119], v[0:3]\n"
    "ds_read_b64_tr_b16 a[4:5], v219 offset:36864\n"
    "s_add_u32 s43, s40, 9216\n"
    "v_mfma_f32_16x16x32_bf16 v[4:7], a[72:75], a[96:99], 0\n"
    "buffer_store_dwordx4 v[250:253], v236, s[28:31], s43 offen offset:2048 sc1\n"
    "v_mfma_f32_16x16x32_bf16 v[4:7], a[76:79], a[100:103], v[4:7]\n"
    "ds_read_b64_tr_b16 a[6:7], v219 offset:43008\n"
    "s_add_u32 m0, s37, 36864\n"
    "v_mfma_f32_16x16x32_bf16 v[4:7], a[80:83], a[104:107], v[4:7]\n"
    "ds_read_b64_tr_b16 a[8:9], v220 offset:36864\n"
    "s_add_u32 s42, s39, 135168\n"
    "v_mfma_f32_16x16x32_bf16 v[4:7], a[84:87], a[108:111], v[4:7]\n"
    "ds_read_b64_tr_b16 a[10:11], v220 offset:43008\n"
    "v_mfma_f32_16x16x32_bf16 v[4:7], a[88:91], a[112:115], v[4:7]\n"
    "ds_read_b64_tr_b16 a[12:13], v221 offset:36864\n"
    "v_mfma_f32_16x16x32_bf16 v[4:7], a[92:95], a[116:119], v[4:7]\n"
    "ds_read_b64_tr_b16 a[14:15], v221 offset:43008\n"
    "v_mfma_f32_16x16x32_bf16 v[8:11], a[48:51], a[120:123], 0\n"
    "ds_read_b64_tr_b16 a[16:17], v218 offset:36992\n"
    "v_mfma_f32_16x16x32_bf16 v[8:11], a[52:55], a[124:127], v[8:11]\n"
    "ds_read_b64_tr_b16 a[18:19], v218 offset:43136\n"
    "v_fma_f32 v0, v0, s36, v212\n"
    "v_mfma_f32_16x16x32_bf16 v[8:11], a[56:59], a[128:131], v[8:11]\n"
    "ds_read_b64_tr_b16 a[20:21], v219 offset:36992\n"
    "v_fma_f32 v1, v1, s36, v212\n"
    "v_mfma_f32_16x16x32_bf16 v[8:11], a[60:63], a[132:135], v[8:11]\n"
    "ds_read_b64_tr_b16 a[22:23], v219 offset:43136\n"
    "v_fma_f32 v2, v2, s36, v212\n"
    "v_mfma_f32_16x16x32_bf16 v[8:11], a[64:67], a[136:139], v[8:11]\n"
    "ds_read_b64_tr_b16 a[24:25], v220 offset:36992\n"
    "v_fma_f32 v3, v3, s36, v212\n"
    "v_mfma_f32_16x16x32_bf16 v[8:11], a[68:71], a[140:143], v[8:11]\n"
    "ds_read_b64_tr_b16 a[26:27], v220 offset:43136\n"
    "v_mfma_f32_16x16x32_bf16 v[12:15], a[72:75], a[120:123], 0\n"
    "ds_read_b64_tr_b16 a[28:29], v221 offset:36992\n"
    "v_mfma_f32_16x16x32_bf16 v[12:15], a[76:79], a[124:127], v[12:15]\n"
    "ds_read_b64_tr_b16 a[30:31], v221 offset:43136\n"
    "v_fma_f32 v4, v4, s36, v212\n"
    "v_mfma_f32_16x16x32_bf16 v[12:15], a[80:83], a[128:131], v[12:15]\n"
    "ds_read_b64_tr_b16 a[32:33], v218 offset:37120\n"
    "v_fma_f32 v5, v5, s36, v212\n"
    "v_mfma_f32_16x16x32_bf16 v[12:15], a[84:87], a[132:135], v[12:15]\n"
    "ds_read_b64_tr_b16 a[34:35], v218 offset:43264\n"
    "v_fma_f32 v6, v6, s36, v212\n"
    "v_mfma_f32_16x16x32_bf16 v[12:15], a[88:91], a[136:139], v[12:15]\n"
    "ds_read_b64_tr_b16 a[36:37], v219 offset:37120\n"
    "v_fma_f32 v7, v7, s36, v212\n"
    "v_mfma_f32_16x16x32_bf16 v[12:15], a[92:95], a[140:143], v[12:15]\n"
    "ds_read_b64_tr_b16 a[38:39], v219 offset:43264\n"
    "v_mfma_f32_16x16x32_bf16 v[16:19], a[48:51], a[144:147], 0\n"
    "ds_read_b64_tr_b16 a[40:41], v220 offset:37120\n"
    "v_mfma_f32_16x16x32_bf16 v[16:19], a[52:55], a[148:151], v[16:19]\n"
    "ds_read_b64_tr_b16 a[42:43], v220 offset:43264\n"
    "v_fma_f32 v8, v8, s36, v213\n"
    "v_mfma_f32_16x16x32_bf16 v[16:19], a[56:59], a[152:155], v[16:19]\n"
    "ds_read_b64_tr_b16 a[44:45], v221 offset:37120\n"
    "v_fma_f32 v9, v9, s36, v213\n"
    "v_mfma_f32_16x16x32_bf16 v[16:19], a[60:63], a[156:159], v[16:19]\n"
    "ds_read_b64_tr_b16 a[46:47], v221 offset:43264\n"
    "v_fma_f32 v10, v10, s36, v213\n"
    "v_mfma_f32_16x16x32_bf16 v[16:19], a[64:67], a[160:163], v[16:19]\n"
    "ds_read_b128 v[250:253], v241 offset:12288\n"
    "v_fma_f32 v11, v11, s36, v213\n"
    "v_mfma_f32_16x16x32_bf16 v[16:19], a[68:71], a[164:167], v[16:19]\n"
    "v_exp_f32 v0, v0\n"
    "v_mfma_f32_16x16x32_bf16 v[20:23], a[72:75], a[144:147], 0\n"
    "v_exp_f32 v1, v1\n"
    "v_mfma_f32_16x16x32_bf16 v[20:23], a[76:79], a[148:151], v[20:23]\n"
    "v_exp_f32 v2, v2\n"
    "v_mfma_f32_16x16x32_bf16 v[20:23], a[80:83], a[152:155], v[20:23]\n"
    "v_exp_f32 v3, v3\n"
    "v_mfma_f32_16x16x32_bf16 v[20:23], a[84:87], a[156:159], v[20:23]\n"
    "v_exp_f32 v4, v4\n"
    "v_mfma_f32_16x16x32_bf16 v[20:23], a[88:91], a[160:163], v[20:23]\n"
    "v_exp_f32 v5, v5\n"
    "v_mfma_f32_16x16x32_bf16 v[20:23], a[92:95], a[164:167], v[20:23]\n"
    "v_exp_f32 v6, v6\n"
    "v_mfma_f32_16x16x32_bf16 v[24:27], a[48:51], a[168:171], 0\n"
    "v_exp_f32 v7, v7\n"
    "v_mfma_f32_16x16x32_bf16 v[24:27], a[52:55], a[172:175], v[24:27]\n"
    "v_cvt_pk_bf16_f32 v0, v0, v1\n"
    "v_cvt_pk_bf16_f32 v1, v2, v3\n"
    "v_mfma_f32_16x16x32_bf16 v[24:27], a[56:59], a[176:179], v[24:27]\n"
    "v_cvt_pk_bf16_f32 v2, v4, v5\n"
    "v_cvt_pk_bf16_f32 v3, v6, v7\n"
    "v_mfma_f32_16x16x32_bf16 v[24:27], a[60:63], a[180:183], v[24:27]\n"
    "v_exp_f32 v8, v8\n"
    "v_mfma_f32_16x16x32_bf16 v[24:27], a[64:67], a[184:187], v[24:27]\n"
    "v_exp_f32 v9, v9\n"
    "v_mfma_f32_16x16x32_bf16 v[24:27], a[68:71], a[188:191], v[24:27]\n"
    "v_exp_f32 v10, v10\n"
    "v_mfma_f32_16x16x32_bf16 v[28:31], a[72:75], a[168:171], 0\n"
    "v_exp_f32 v11, v11\n"
    "v_mfma_f32_16x16x32_bf16 v[28:31], a[76:79], a[172:175], v[28:31]\n"
    "v_fma_f32 v12, v12, s36, v213\n"
    "v_fma_f32 v13, v13, s36, v213\n"
    "v_mfma_f32_16x16x32_bf16 v[28:31], a[80:83], a[176:179], v[28:31]\n"
    "v_exp_f32 v12, v12\n"
    "v_mfma_f32_16x16x32_bf16 v[28:31], a[84:87], a[180:183], v[28:31]\n"
    "v_exp_f32 v13, v13\n"
    "v_mfma_f32_16x16x32_bf16 v[28:31], a[88:91], a[184:187], v[28:31]\n"
    "v_fma_f32 v14, v14, s36, v213\n"
    "v_fma_f32 v15, v15, s36, v213\n"
    "v_mfma_f32_16x16x32_bf16 v[28:31], a[92:95], a[188:191], v[28:31]\n"
    "s_waitcnt vmcnt(14) lgkmcnt(0)\n"
    "s_barrier\n"
    "L_ph49:\n"
    "v_mfma_f32_16x16x32_bf16 v[64:67], a[0:3], v[32:35], v[64:67]\n"
    "buffer_load_dwordx4 v222, s[20:23], s42 offen nt lds\n"
    "v_mfma_f32_16x16x32_bf16 v[80:83], a[4:7], v[32:35], v[80:83]\n"
    "ds_read_b128 a[48:51], v216 offset:49152\n"
    "v_cvt_pk_bf16_f32 v8, v8, v9\n"
    "v_mfma_f32_16x16x32_bf16 v[96:99], a[8:11], v[32:35], v[96:99]\n"
    "buffer_load_dwordx4 v223, s[20:23], s42 offen offset:1024 nt lds\n"
    "v_mfma_f32_16x16x32_bf16 v[112:115], a[12:15], v[32:35], v[112:115]\n"
    "ds_read_b128 a[52:55], v217 offset:49152\n"
    "v_cvt_pk_bf16_f32 v9, v10, v11\n"
    "v_mfma_f32_16x16x32_bf16 v[128:131], a[16:19], v[32:35], v[128:131]\n"
    "buffer_load_dwordx4 v224, s[20:23], s42 offen offset:2048 nt lds\n"
    "v_mfma_f32_16x16x32_bf16 v[144:147], a[20:23], v[32:35], v[144:147]\n"
    "ds_read_b128 a[56:59], v216 offset:49280\n"
    "s_add_u32 s43, s40, 12288\n"
    "v_mfma_f32_16x16x32_bf16 v[160:163], a[24:27], v[32:35], v[160:163]\n"
    "buffer_store_dwordx4 v[250:253], v236, s[28:31], s43 offen sc1\n"
    "v_mfma_f32_16x16x32_bf16 v[176:179], a[28:31], v[32:35], v[176:179]\n"
    "ds_read_b128 a[60:63], v217 offset:49280\n"
    "s_add_u32 m0, s37, 49152\n"
    "v_mfma_f32_16x16x32_bf16 a[192:195], a[32:35], v[32:35], a[192:195]\n"
    "ds_read_b128 a[64:67], v216 offset:49408\n"
    "s_add_u32 s42, s39, 122880\n"
    "v_mfma_f32_16x16x32_bf16 a[208:211], a[36:39], v[32:35], a[208:211]\n"
    "ds_read_b128 a[68:71], v217 offset:49408\n"
    "v_cvt_pk_bf16_f32 v10, v12, v13\n"
    "v_mfma_f32_16x16x32_bf16 a[224:227], a[40:43], v[32:35], a[224:227]\n"
    "ds_read_b128 a[72:75], v216 offset:55296\n"
    "v_fma_f32 v16, v16, s36, v214\n"
    "v_mfma_f32_16x16x32_bf16 a[240:243], a[44:47], v[32:35], a[240:243]\n"
    "ds_read_b128 a[76:79], v217 offset:55296\n"
    "v_fma_f32 v17, v17, s36, v214\n"
    "v_mfma_f32_16x16x32_bf16 v[192:195], v[208:211], v[32:35], v[192:195]\n"
    "ds_read_b128 a[80:83], v216 offset:55424\n"
    "v_fma_f32 v18, v18, s36, v214\n"
    "v_mfma_f32_16x16x32_bf16 v[68:71], a[0:3], v[40:43], v[68:71]\n"
    "ds_read_b128 a[84:87], v217 offset:55424\n"
    "v_fma_f32 v19, v19, s36, v214\n"
    "v_mfma_f32_16x16x32_bf16 v[84:87], a[4:7], v[40:43], v[84:87]\n"
    "ds_read_b128 a[88:91], v216 offset:55552\n"
    "v_fma_f32 v20, v20, s36, v214\n"
    "v_mfma_f32_16x16x32_bf16 v[100:103], a[8:11], v[40:43], v[100:103]\n"
    "ds_read_b128 a[92:95], v217 offset:55552\n"
    "v_fma_f32 v21, v21, s36, v214\n"
    "v_mfma_f32_16x16x32_bf16 v[116:119], a[12:15], v[40:43], v[116:119]\n"
    "ds_read_b128 v[250:253], v242 offset:13312\n"
    "v_fma_f32 v22, v22, s36, v214\n"
    "v_mfma_f32_16x16x32_bf16 v[132:135], a[16:19], v[40:43], v[132:135]\n"
    "v_exp_f32 v14, v14\n"
    "v_mfma_f32_16x16x32_bf16 v[148:151], a[20:23], v[40:43], v[148:151]\n"
    "v_exp_f32 v15, v15\n"
    "v_mfma_f32_16x16x32_bf16 v[164:167], a[24:27], v[40:43], v[164:167]\n"
    "v_cvt_pk_bf16_f32 v11, v14, v15\n"
    "v_fma_f32 v23, v23, s36, v214\n"
    "v_mfma_f32_16x16x32_bf16 v[180:183], a[28:31], v[40:43], v[180:183]\n"
    "v_exp_f32 v16, v16\n"
    "v_mfma_f32_16x16x32_bf16 a[196:199], a[32:35], v[40:43], a[196:199]\n"
    "v_exp_f32 v17, v17\n"
    "v_mfma_f32_16x16x32_bf16 a[212:215], a[36:39], v[40:43], a[212:215]\n"
    "v_exp_f32 v18, v18\n"
    "v_mfma_f32_16x16x32_bf16 a[228:231], a[40:43], v[40:43], a[228:231]\n"
    "v_exp_f32 v19, v19\n"
    "v_mfma_f32_16x16x32_bf16 a[244:247], a[44:47], v[40:43], a[244:247]\n"
    "v_exp_f32 v20, v20\n"
    "v_mfma_f32_16x16x32_bf16 v[196:199], v[208:211], v[40:43], v[196:199]\n"
    "v_exp_f32 v21, v21\n"
    "v_mfma_f32_16x16x32_bf16 v[72:75], a[0:3], v[48:51], v[72:75]\n"
    "v_exp_f32 v22, v22\n"
    "v_mfma_f32_16x16x32_bf16 v[88:91], a[4:7], v[48:51], v[88:91]\n"
    "v_exp_f32 v23, v23\n"
    "v_mfma_f32_16x16x32_bf16 v[104:107], a[8:11], v[48:51], v[104:107]\n"
    "v_cvt_pk_bf16_f32 v16, v16, v17\n"
    "v_cvt_pk_bf16_f32 v17, v18, v19\n"
    "v_mfma_f32_16x16x32_bf16 v[120:123], a[12:15], v[48:51], v[120:123]\n"
    "v_cvt_pk_bf16_f32 v18, v20, v21\n"
    "v_cvt_pk_bf16_f32 v19, v22, v23\n"
    "v_mfma_f32_16x16x32_bf16 v[136:139], a[16:19], v[48:51], v[136:139]\n"
    "v_fma_f32 v24, v24, s36, v215\n"
    "v_fma_f32 v25, v25, s36, v215\n"
    "v_mfma_f32_16x16x32_bf16 v[152:155], a[20:23], v[48:51], v[152:155]\n"
    "v_exp_f32 v24, v24\n"
    "v_mfma_f32_16x16x32_bf16 v[168:171], a[24:27], v[48:51], v[168:171]\n"
    "v_exp_f32 v25, v25\n"
    "v_mfma_f32_16x16x32_bf16 v[184:187], a[28:31], v[48:51], v[184:187]\n"
    "v_fma_f32 v26, v26, s36, v215\n"
    "v_fma_f32 v27, v27, s36, v215\n"
    "v_mfma_f32_16x16x32_bf16 a[200:203], a[32:35], v[48:51], a[200:203]\n"
    "v_exp_f32 v26, v26\n"
    "v_mfma_f32_16x16x32_bf16 a[216:219], a[36:39], v[48:51], a[216:219]\n"
    "v_exp_f32 v27, v27\n"
    "v_mfma_f32_16x16x32_bf16 a[232:235], a[40:43], v[48:51], a[232:235]\n"
    "v_fma_f32 v28, v28, s36, v215\n"
    "v_fma_f32 v29, v29, s36, v215\n"
    "v_mfma_f32_16x16x32_bf16 a[248:251], a[44:47], v[48:51], a[248:251]\n"
    "v_exp_f32 v28, v28\n"
    "v_mfma_f32_16x16x32_bf16 v[200:203], v[208:211], v[48:51], v[200:203]\n"
    "v_exp_f32 v29, v29\n"
    "v_mfma_f32_16x16x32_bf16 v[76:79], a[0:3], v[56:59], v[76:79]\n"
    "v_fma_f32 v30, v30, s36, v215\n"
    "v_fma_f32 v31, v31, s36, v215\n"
    "v_mfma_f32_16x16x32_bf16 v[92:95], a[4:7], v[56:59], v[92:95]\n"
    "v_exp_f32 v30, v30\n"
    "v_mfma_f32_16x16x32_bf16 v[108:111], a[8:11], v[56:59], v[108:111]\n"
    "v_exp_f32 v31, v31\n"
    "v_mfma_f32_16x16x32_bf16 v[124:127], a[12:15], v[56:59], v[124:127]\n"
    "v_cvt_pk_bf16_f32 v24, v24, v25\n"
    "v_cvt_pk_bf16_f32 v25, v26, v27\n"
    "v_mfma_f32_16x16x32_bf16 v[140:143], a[16:19], v[56:59], v[140:143]\n"
    "v_cvt_pk_bf16_f32 v26, v28, v29\n"
    "v_cvt_pk_bf16_f32 v27, v30, v31\n"
    "v_mfma_f32_16x16x32_bf16 v[156:159], a[20:23], v[56:59], v[156:159]\n"
    "v_mfma_f32_16x16x32_bf16 v[172:175], a[24:27], v[56:59], v[172:175]\n"
    "v_mfma_f32_16x16x32_bf16 v[188:191], a[28:31], v[56:59], v[188:191]\n"
    "v_mfma_f32_16x16x32_bf16 a[204:207], a[32:35], v[56:59], a[204:207]\n"
    "v_mfma_f32_16x16x32_bf16 a[220:223], a[36:39], v[56:59], a[220:223]\n"
    "v_mfma_f32_16x16x32_bf16 a[236:239], a[40:43], v[56:59], a[236:239]\n"
    "v_mfma_f32_16x16x32_bf16 a[252:255], a[44:47], v[56:59], a[252:255]\n"
    "v_mfma_f32_16x16x32_bf16 v[204:207], v[208:211], v[56:59], v[204:207]\n"
    "s_waitcnt vmcnt(14) lgkmcnt(0)\n"
    "s_barrier\n"
    "L_ph50:\n"
    "v_mfma_f32_16x16x32_bf16 v[32:35], a[48:51], a[96:99], 0\n"
    "buffer_load_dwordx4 v231, s[24:27], s42 offen nt lds\n"
    "v_mfma_f32_16x16x32_bf16 v[32:35], a[52:55], a[100:103], v[32:35]\n"
    "ds_read_b64_tr_b16 a[0:1], v218 offset:0\n"
    "v_mfma_f32_16x16x32_bf16 v[32:35], a[56:59], a[104:107], v[32:35]\n"
    "buffer_load_dwordx4 v232, s[24:27], s42 offen offset:1024 nt lds\n"
    "v_mfma_f32_16x16x32_bf16 v[32:35], a[60:63], a[108:111], v[32:35]\n"
    "ds_read_b64_tr_b16 a[2:3], v218 offset:6144\n"
    "v_mfma_f32_16x16x32_bf16 v[32:35], a[64:67], a[112:115], v[32:35]\n"
    "buffer_load_dwordx4 v233, s[24:27], s42 offen offset:2048 nt lds\n"
    "v_mfma_f32_16x16x32_bf16 v[32:35], a[68:71], a[116:119], v[32:35]\n"
    "ds_read_b64_tr_b16 a[4:5], v219 offset:0\n"
    "s_add_u32 s43, s40, 12288\n"
    "v_mfma_f32_16x16x32_bf16 v[36:39], a[72:75], a[96:99], 0\n"
    "buffer_store_dwordx4 v[250:253], v236, s[28:31], s43 offen offset:1024 sc1\n"
    "v_mfma_f32_16x16x32_bf16 v[36:39], a[76:79], a[100:103], v[36:39]\n"
    "ds_read_b64_tr_b16 a[6:7], v219 offset:6144\n"
    "s_add_u32 m0, s37, 0\n"
    "v_mfma_f32_16x16x32_bf16 v[36:39], a[80:83], a[104:107], v[36:39]\n"
    "ds_read_b64_tr_b16 a[8:9], v220 offset:0\n"
    "s_add_u32 s42, s39, 147456\n"
    "v_mfma_f32_16x16x32_bf16 v[36:39], a[84:87], a[108:111], v[36:39]\n"
    "ds_read_b64_tr_b16 a[10:11], v220 offset:6144\n"
    "v_mfma_f32_16x16x32_bf16 v[36:39], a[88:91], a[112:115], v[36:39]\n"
    "ds_read_b64_tr_b16 a[12:13], v221 offset:0\n"
    "v_mfma_f32_16x16x32_bf16 v[36:39], a[92:95], a[116:119], v[36:39]\n"
    "ds_read_b64_tr_b16 a[14:15], v221 offset:6144\n"
    "v_mfma_f32_16x16x32_bf16 v[40:43], a[48:51], a[120:123], 0\n"
    "ds_read_b64_tr_b16 a[16:17], v218 offset:128\n"
    "v_mfma_f32_16x16x32_bf16 v[40:43], a[52:55], a[124:127], v[40:43]\n"
    "ds_read_b64_tr_b16 a[18:19], v218 offset:6272\n"
    "v_fma_f32 v32, v32, s36, v212\n"
    "v_mfma_f32_16x16x32_bf16 v[40:43], a[56:59], a[128:131], v[40:43]\n"
    "ds_read_b64_tr_b16 a[20:21], v219 offset:128\n"
    "v_fma_f32 v33, v33, s36, v212\n"
    "v_mfma_f32_16x16x32_bf16 v[40:43], a[60:63], a[132:135], v[40:43]\n"
    "ds_read_b64_tr_b16 a[22:23], v219 offset:6272\n"
    "v_fma_f32 v34, v34, s36, v212\n"
    "v_mfma_f32_16x16x32_bf16 v[40:43], a[64:67], a[136:139], v[40:43]\n"
    "ds_read_b64_tr_b16 a[24:25], v220 offset:128\n"
    "v_fma_f32 v35, v35, s36, v212\n"
    "v_mfma_f32_16x16x32_bf16 v[40:43], a[68:71], a[140:143], v[40:43]\n"
    "ds_read_b64_tr_b16 a[26:27], v220 offset:6272\n"
    "v_mfma_f32_16x16x32_bf16 v[44:47], a[72:75], a[120:123], 0\n"
    "ds_read_b64_tr_b16 a[28:29], v221 offset:128\n"
    "v_mfma_f32_16x16x32_bf16 v[44:47], a[76:79], a[124:127], v[44:47]\n"
    "ds_read_b64_tr_b16 a[30:31], v221 offset:6272\n"
    "v_fma_f32 v36, v36, s36, v212\n"
    "v_mfma_f32_16x16x32_bf16 v[44:47], a[80:83], a[128:131], v[44:47]\n"
    "ds_read_b64_tr_b16 a[32:33], v218 offset:256\n"
    "v_fma_f32 v37, v37, s36, v212\n"
    "v_mfma_f32_16x16x32_bf16 v[44:47], a[84:87], a[132:135], v[44:47]\n"
    "ds_read_b64_tr_b16 a[34:35], v218 offset:6400\n"
    "v_fma_f32 v38, v38, s36, v212\n"
    "v_mfma_f32_16x16x32_bf16 v[44:47], a[88:91], a[136:139], v[44:47]\n"
    "ds_read_b64_tr_b16 a[36:37], v219 offset:256\n"
    "v_fma_f32 v39, v39, s36, v212\n"
    "v_mfma_f32_16x16x32_bf16 v[44:47], a[92:95], a[140:143], v[44:47]\n"
    "ds_read_b64_tr_b16 a[38:39], v219 offset:6400\n"
    "v_mfma_f32_16x16x32_bf16 v[48:51], a[48:51], a[144:147], 0\n"
    "ds_read_b64_tr_b16 a[40:41], v220 offset:256\n"
    "v_mfma_f32_16x16x32_bf16 v[48:51], a[52:55], a[148:151], v[48:51]\n"
    "ds_read_b64_tr_b16 a[42:43], v220 offset:6400\n"
    "v_fma_f32 v40, v40, s36, v213\n"
    "v_mfma_f32_16x16x32_bf16 v[48:51], a[56:59], a[152:155], v[48:51]\n"
    "ds_read_b64_tr_b16 a[44:45], v221 offset:256\n"
    "v_fma_f32 v41, v41, s36, v213\n"
    "v_mfma_f32_16x16x32_bf16 v[48:51], a[60:63], a[156:159], v[48:51]\n"
    "ds_read_b64_tr_b16 a[46:47], v221 offset:6400\n"
    "v_fma_f32 v42, v42, s36, v213\n"
    "v_mfma_f32_16x16x32_bf16 v[48:51], a[64:67], a[160:163], v[48:51]\n"
    "ds_read_b128 v[250:253], v243 offset:14336\n"
    "v_fma_f32 v43, v43, s36, v213\n"
    "v_mfma_f32_16x16x32_bf16 v[48:51], a[68:71], a[164:167], v[48:51]\n"
    "v_exp_f32 v32, v32\n"
    "v_mfma_f32_16x16x32_bf16 v[52:55], a[72:75], a[144:147], 0\n"
    "v_exp_f32 v33, v33\n"
    "v_mfma_f32_16x16x32_bf16 v[52:55], a[76:79], a[148:151], v[52:55]\n"
    "v_exp_f32 v34, v34\n"
    "v_mfma_f32_16x16x32_bf16 v[52:55], a[80:83], a[152:155], v[52:55]\n"
    "v_exp_f32 v35, v35\n"
    "v_mfma_f32_16x16x32_bf16 v[52:55], a[84:87], a[156:159], v[52:55]\n"
    "v_exp_f32 v36, v36\n"
    "v_mfma_f32_16x16x32_bf16 v[52:55], a[88:91], a[160:163], v[52:55]\n"
    "v_exp_f32 v37, v37\n"
    "v_mfma_f32_16x16x32_bf16 v[52:55], a[92:95], a[164:167], v[52:55]\n"
    "v_exp_f32 v38, v38\n"
    "v_mfma_f32_16x16x32_bf16 v[56:59], a[48:51], a[168:171], 0\n"
    "v_exp_f32 v39, v39\n"
    "v_mfma_f32_16x16x32_bf16 v[56:59], a[52:55], a[172:175], v[56:59]\n"
    "v_cvt_pk_bf16_f32 v32, v32, v33\n"
    "v_cvt_pk_bf16_f32 v33, v34, v35\n"
    "v_mfma_f32_16x16x32_bf16 v[56:59], a[56:59], a[176:179], v[56:59]\n"
    "v_cvt_pk_bf16_f32 v34, v36, v37\n"
    "v_cvt_pk_bf16_f32 v35, v38, v39\n"
    "v_mfma_f32_16x16x32_bf16 v[56:59], a[60:63], a[180:183], v[56:59]\n"
    "v_exp_f32 v40, v40\n"
    "v_mfma_f32_16x16x32_bf16 v[56:59], a[64:67], a[184:187], v[56:59]\n"
    "v_exp_f32 v41, v41\n"
    "v_mfma_f32_16x16x32_bf16 v[56:59], a[68:71], a[188:191], v[56:59]\n"
    "v_exp_f32 v42, v42\n"
    "v_mfma_f32_16x16x32_bf16 v[60:63], a[72:75], a[168:171], 0\n"
    "v_exp_f32 v43, v43\n"
    "v_mfma_f32_16x16x32_bf16 v[60:63], a[76:79], a[172:175], v[60:63]\n"
    "v_fma_f32 v44, v44, s36, v213\n"
    "v_fma_f32 v45, v45, s36, v213\n"
    "v_mfma_f32_16x16x32_bf16 v[60:63], a[80:83], a[176:179], v[60:63]\n"
    "v_exp_f32 v44, v44\n"
    "v_mfma_f32_16x16x32_bf16 v[60:63], a[84:87], a[180:183], v[60:63]\n"
    "v_exp_f32 v45, v45\n"
    "v_mfma_f32_16x16x32_bf16 v[60:63], a[88:91], a[184:187], v[60:63]\n"
    "v_fma_f32 v46, v46, s36, v213\n"
    "v_fma_f32 v47, v47, s36, v213\n"
    "v_mfma_f32_16x16x32_bf16 v[60:63], a[92:95], a[188:191], v[60:63]\n"
    "s_waitcnt vmcnt(14) lgkmcnt(0)\n"
    "s_barrier\n"
    "L_ph51:\n"
    "v_mfma_f32_16x16x32_bf16 v[64:67], a[0:3], v[0:3], v[64:67]\n"
    "buffer_load_dwordx4 v222, s[20:23], s42 offen nt lds\n"
    "v_mfma_f32_16x16x32_bf16 v[80:83], a[4:7], v[0:3], v[80:83]\n"
    "ds_read_b128 a[48:51], v216 offset:12288\n"
    "v_cvt_pk_bf16_f32 v40, v40, v41\n"
    "v_mfma_f32_16x16x32_bf16 v[96:99], a[8:11], v[0:3], v[96:99]\n"
    "buffer_load_dwordx4 v223, s[20:23], s42 offen offset:1024 nt lds\n"
    "v_mfma_f32_16x16x32_bf16 v[112:115], a[12:15], v[0:3], v[112:115]\n"
    "ds_read_b128 a[52:55], v217 offset:12288\n"
    "v_cvt_pk_bf16_f32 v41, v42, v43\n"
    "v_mfma_f32_16x16x32_bf16 v[128:131], a[16:19], v[0:3], v[128:131]\n"
    "buffer_load_dwordx4 v224, s[20:23], s42 offen offset:2048 nt lds\n"
    "v_mfma_f32_16x16x32_bf16 v[144:147], a[20:23], v[0:3], v[144:147]\n"
    "ds_read_b128 a[56:59], v216 offset:12416\n"
    "s_add_u32 s43, s40, 12288\n"
    "v_mfma_f32_16x16x32_bf16 v[160:163], a[24:27], v[0:3], v[160:163]\n"
    "buffer_store_dwordx4 v[250:253], v236, s[28:31], s43 offen offset:2048 sc1\n"
    "v_mfma_f32_16x16x32_bf16 v[176:179], a[28:31], v[0:3], v[176:179]\n"
    "s_mov_b32 m0, 159744\n"
    "s_mov_b32 s51, L_ph55-L_code_start\n"
    "v_mfma_f32_16x16x32_bf16 a[192:195], a[32:35], v[0:3], a[192:195]\n"
    "buffer_load_dword v249, s[52:55], s51 offen lds\n"
    "v_mfma_f32_16x16x32_bf16 a[208:211], a[36:39], v[0:3], a[208:211]\n"
    "ds_read_b128 a[60:63], v217 offset:12416\n"
    "s_add_u32 m0, s37, 12288\n"
    "v_mfma_f32_16x16x32_bf16 a[224:227], a[40:43], v[0:3], a[224:227]\n"
    "ds_read_b128 a[64:67], v216 offset:12544\n"
    "s_add_u32 s42, s39, 135168\n"
    "v_mfma_f32_16x16x32_bf16 a[240:243], a[44:47], v[0:3], a[240:243]\n"
    "ds_read_b128 a[68:71], v217 offset:12544\n"
    "v_cvt_pk_bf16_f32 v42, v44, v45\n"
    "v_mfma_f32_16x16x32_bf16 v[192:195], v[208:211], v[0:3], v[192:195]\n"
    "ds_read_b128 a[72:75], v216 offset:18432\n"
    "v_fma_f32 v48, v48, s36, v214\n"
    "v_mfma_f32_16x16x32_bf16 v[68:71], a[0:3], v[8:11], v[68:71]\n"
    "ds_read_b128 a[76:79], v217 offset:18432\n"
    "v_fma_f32 v49, v49, s36, v214\n"
    "v_mfma_f32_16x16x32_bf16 v[84:87], a[4:7], v[8:11], v[84:87]\n"
    "ds_read_b128 a[80:83], v216 offset:18560\n"
    "v_fma_f32 v50, v50, s36, v214\n"
    "v_mfma_f32_16x16x32_bf16 v[100:103], a[8:11], v[8:11], v[100:103]\n"
    "ds_read_b128 a[84:87], v217 offset:18560\n"
    "v_fma_f32 v51, v51, s36, v214\n"
    "v_mfma_f32_16x16x32_bf16 v[116:119], a[12:15], v[8:11], v[116:119]\n"
    "ds_read_b128 a[88:91], v216 offset:18688\n"
    "v_fma_f32 v52, v52, s36, v214\n"
    "v_mfma_f32_16x16x32_bf16 v[132:135], a[16:19], v[8:11], v[132:135]\n"
    "ds_read_b128 a[92:95], v217 offset:18688\n"
    "v_fma_f32 v53, v53, s36, v214\n"
    "v_mfma_f32_16x16x32_bf16 v[148:151], a[20:23], v[8:11], v[148:151]\n"
    "ds_read_b128 v[250:253], v244 offset:15360\n"
    "v_fma_f32 v54, v54, s36, v214\n"
    "v_mfma_f32_16x16x32_bf16 v[164:167], a[24:27], v[8:11], v[164:167]\n"
    "v_exp_f32 v46, v46\n"
    "v_mfma_f32_16x16x32_bf16 v[180:183], a[28:31], v[8:11], v[180:183]\n"
    "v_exp_f32 v47, v47\n"
    "v_mfma_f32_16x16x32_bf16 a[196:199], a[32:35], v[8:11], a[196:199]\n"
    "v_cvt_pk_bf16_f32 v43, v46, v47\n"
    "v_fma_f32 v55, v55, s36, v214\n"
    "v_mfma_f32_16x16x32_bf16 a[212:215], a[36:39], v[8:11], a[212:215]\n"
    "v_exp_f32 v48, v48\n"
    "v_mfma_f32_16x16x32_bf16 a[228:231], a[40:43], v[8:11], a[228:231]\n"
    "v_exp_f32 v49, v49\n"
    "v_mfma_f32_16x16x32_bf16 a[244:247], a[44:47], v[8:11], a[244:247]\n"
    "v_exp_f32 v50, v50\n"
    "v_mfma_f32_16x16x32_bf16 v[196:199], v[208:211], v[8:11], v[196:199]\n"
    "v_exp_f32 v51, v51\n"
    "v_mfma_f32_16x16x32_bf16 v[72:75], a[0:3], v[16:19], v[72:75]\n"
    "v_exp_f32 v52, v52\n"
    "v_mfma_f32_16x16x32_bf16 v[88:91], a[4:7], v[16:19], v[88:91]\n"
    "v_exp_f32 v53, v53\n"
    "v_mfma_f32_16x16x32_bf16 v[104:107], a[8:11], v[16:19], v[104:107]\n"
    "v_exp_f32 v54, v54\n"
    "v_mfma_f32_16x16x32_bf16 v[120:123], a[12:15], v[16:19], v[120:123]\n"
    "v_exp_f32 v55, v55\n"
    "v_mfma_f32_16x16x32_bf16 v[136:139], a[16:19], v[16:19], v[136:139]\n"
    "v_cvt_pk_bf16_f32 v48, v48, v49\n"
    "v_cvt_pk_bf16_f32 v49, v50, v51\n"
    "v_mfma_f32_16x16x32_bf16 v[152:155], a[20:23], v[16:19], v[152:155]\n"
    "v_cvt_pk_bf16_f32 v50, v52, v53\n"
    "v_cvt_pk_bf16_f32 v51, v54, v55\n"
    "v_mfma_f32_16x16x32_bf16 v[168:171], a[24:27], v[16:19], v[168:171]\n"
    "v_fma_f32 v56, v56, s36, v215\n"
    "v_fma_f32 v57, v57, s36, v215\n"
    "v_mfma_f32_16x16x32_bf16 v[184:187], a[28:31], v[16:19], v[184:187]\n"
    "v_exp_f32 v56, v56\n"
    "v_mfma_f32_16x16x32_bf16 a[200:203], a[32:35], v[16:19], a[200:203]\n"
    "v_exp_f32 v57, v57\n"
    "v_mfma_f32_16x16x32_bf16 a[216:219], a[36:39], v[16:19], a[216:219]\n"
    "v_fma_f32 v58, v58, s36, v215\n"
    "v_fma_f32 v59, v59, s36, v215\n"
    "v_mfma_f32_16x16x32_bf16 a[232:235], a[40:43], v[16:19], a[232:235]\n"
    "v_exp_f32 v58, v58\n"
    "v_mfma_f32_16x16x32_bf16 a[248:251], a[44:47], v[16:19], a[248:251]\n"
    "v_exp_f32 v59, v59\n"
    "v_mfma_f32_16x16x32_bf16 v[200:203], v[208:211], v[16:19], v[200:203]\n"
    "v_fma_f32 v60, v60, s36, v215\n"
    "v_fma_f32 v61, v61, s36, v215\n"
    "v_mfma_f32_16x16x32_bf16 v[76:79], a[0:3], v[24:27], v[76:79]\n"
    "v_exp_f32 v60, v60\n"
    "v_mfma_f32_16x16x32_bf16 v[92:95], a[4:7], v[24:27], v[92:95]\n"
    "v_exp_f32 v61, v61\n"
    "v_mfma_f32_16x16x32_bf16 v[108:111], a[8:11], v[24:27], v[108:111]\n"
    "v_fma_f32 v62, v62, s36, v215\n"
    "v_fma_f32 v63, v63, s36, v215\n"
    "v_mfma_f32_16x16x32_bf16 v[124:127], a[12:15], v[24:27], v[124:127]\n"
    "v_exp_f32 v62, v62\n"
    "v_mfma_f32_16x16x32_bf16 v[140:143], a[16:19], v[24:27], v[140:143]\n"
    "v_exp_f32 v63, v63\n"
    "v_mfma_f32_16x16x32_bf16 v[156:159], a[20:23], v[24:27], v[156:159]\n"
    "v_cvt_pk_bf16_f32 v56, v56, v57\n"
    "v_cvt_pk_bf16_f32 v57, v58, v59\n"
    "v_mfma_f32_16x16x32_bf16 v[172:175], a[24:27], v[24:27], v[172:175]\n"
    "v_cvt_pk_bf16_f32 v58, v60, v61\n"
    "v_cvt_pk_bf16_f32 v59, v62, v63\n"
    "v_mfma_f32_16x16x32_bf16 v[188:191], a[28:31], v[24:27], v[188:191]\n"
    "v_mfma_f32_16x16x32_bf16 a[204:207], a[32:35], v[24:27], a[204:207]\n"
    "v_mfma_f32_16x16x32_bf16 a[220:223], a[36:39], v[24:27], a[220:223]\n"
    "v_mfma_f32_16x16x32_bf16 a[236:239], a[40:43], v[24:27], a[236:239]\n"
    "v_mfma_f32_16x16x32_bf16 a[252:255], a[44:47], v[24:27], a[252:255]\n"
    "v_mfma_f32_16x16x32_bf16 v[204:207], v[208:211], v[24:27], v[204:207]\n"
    "s_waitcnt vmcnt(14) lgkmcnt(0)\n"
    "s_barrier\n"
    "L_ph52:\n"
    "v_mfma_f32_16x16x32_bf16 v[0:3], a[48:51], a[96:99], 0\n"
    "buffer_load_dwordx4 v231, s[24:27], s42 offen nt lds\n"
    "v_mfma_f32_16x16x32_bf16 v[0:3], a[52:55], a[100:103], v[0:3]\n"
    "ds_read_b64_tr_b16 a[0:1], v218 offset:24576\n"
    "v_mfma_f32_16x16x32_bf16 v[0:3], a[56:59], a[104:107], v[0:3]\n"
    "buffer_load_dwordx4 v232, s[24:27], s42 offen offset:1024 nt lds\n"
    "v_mfma_f32_16x16x32_bf16 v[0:3], a[60:63], a[108:111], v[0:3]\n"
    "ds_read_b64_tr_b16 a[2:3], v218 offset:30720\n"
    "v_mfma_f32_16x16x32_bf16 v[0:3], a[64:67], a[112:115], v[0:3]\n"
    "buffer_load_dwordx4 v233, s[24:27], s42 offen offset:2048 nt lds\n"
    "v_mfma_f32_16x16x32_bf16 v[0:3], a[68:71], a[116:119], v[0:3]\n"
    "ds_read_b64_tr_b16 a[4:5], v219 offset:24576\n"
    "s_add_u32 s43, s40, 15360\n"
    "v_mfma_f32_16x16x32_bf16 v[4:7], a[72:75], a[96:99], 0\n"
    "buffer_store_dwordx4 v[250:253], v236, s[28:31], s43 offen sc1\n"
    "v_mfma_f32_16x16x32_bf16 v[4:7], a[76:79], a[100:103], v[4:7]\n"
    "ds_read_b64_tr_b16 a[6:7], v219 offset:30720\n"
    "s_add_u32 m0, s37, 24576\n"
    "v_mfma_f32_16x16x32_bf16 v[4:7], a[80:83], a[104:107], v[4:7]\n"
    "ds_read_b64_tr_b16 a[8:9], v220 offset:24576\n"
    "s_add_u32 s42, s39, 159744\n"
    "v_mfma_f32_16x16x32_bf16 v[4:7], a[84:87], a[108:111], v[4:7]\n"
    "ds_read_b64_tr_b16 a[10:11], v220 offset:30720\n"
    "v_mfma_f32_16x16x32_bf16 v[4:7], a[88:91], a[112:115], v[4:7]\n"
    "ds_read_b64_tr_b16 a[12:13], v221 offset:24576\n"
    "v_mfma_f32_16x16x32_bf16 v[4:7], a[92:95], a[116:119], v[4:7]\n"
    "ds_read_b64_tr_b16 a[14:15], v221 offset:30720\n"
    "v_mfma_f32_16x16x32_bf16 v[8:11], a[48:51], a[120:123], 0\n"
    "ds_read_b64_tr_b16 a[16:17], v218 offset:24704\n"
    "v_mfma_f32_16x16x32_bf16 v[8:11], a[52:55], a[124:127], v[8:11]\n"
    "ds_read_b64_tr_b16 a[18:19], v218 offset:30848\n"
    "v_fma_f32 v0, v0, s36, v212\n"
    "v_mfma_f32_16x16x32_bf16 v[8:11], a[56:59], a[128:131], v[8:11]\n"
    "ds_read_b64_tr_b16 a[20:21], v219 offset:24704\n"
    "v_fma_f32 v1, v1, s36, v212\n"
    "v_mfma_f32_16x16x32_bf16 v[8:11], a[60:63], a[132:135], v[8:11]\n"
    "ds_read_b64_tr_b16 a[22:23], v219 offset:30848\n"
    "v_fma_f32 v2, v2, s36, v212\n"
    "v_mfma_f32_16x16x32_bf16 v[8:11], a[64:67], a[136:139], v[8:11]\n"
    "ds_read_b64_tr_b16 a[24:25], v220 offset:24704\n"
    "v_fma_f32 v3, v3, s36, v212\n"
    "v_mfma_f32_16x16x32_bf16 v[8:11], a[68:71], a[140:143], v[8:11]\n"
    "ds_read_b64_tr_b16 a[26:27], v220 offset:30848\n"
    "v_mfma_f32_16x16x32_bf16 v[12:15], a[72:75], a[120:123], 0\n"
    "ds_read_b64_tr_b16 a[28:29], v221 offset:24704\n"
    "v_mfma_f32_16x16x32_bf16 v[12:15], a[76:79], a[124:127], v[12:15]\n"
    "ds_read_b64_tr_b16 a[30:31], v221 offset:30848\n"
    "v_fma_f32 v4, v4, s36, v212\n"
    "v_mfma_f32_16x16x32_bf16 v[12:15], a[80:83], a[128:131], v[12:15]\n"
    "ds_read_b64_tr_b16 a[32:33], v218 offset:24832\n"
    "v_fma_f32 v5, v5, s36, v212\n"
    "v_mfma_f32_16x16x32_bf16 v[12:15], a[84:87], a[132:135], v[12:15]\n"
    "ds_read_b64_tr_b16 a[34:35], v218 offset:30976\n"
    "v_fma_f32 v6, v6, s36, v212\n"
    "v_mfma_f32_16x16x32_bf16 v[12:15], a[88:91], a[136:139], v[12:15]\n"
    "ds_read_b64_tr_b16 a[36:37], v219 offset:24832\n"
    "v_fma_f32 v7, v7, s36, v212\n"
    "v_mfma_f32_16x16x32_bf16 v[12:15], a[92:95], a[140:143], v[12:15]\n"
    "ds_read_b64_tr_b16 a[38:39], v219 offset:30976\n"
    "v_mfma_f32_16x16x32_bf16 v[16:19], a[48:51], a[144:147], 0\n"
    "ds_read_b64_tr_b16 a[40:41], v220 offset:24832\n"
    "v_mfma_f32_16x16x32_bf16 v[16:19], a[52:55], a[148:151], v[16:19]\n"
    "ds_read_b64_tr_b16 a[42:43], v220 offset:30976\n"
    "v_fma_f32 v8, v8, s36, v213\n"
    "v_mfma_f32_16x16x32_bf16 v[16:19], a[56:59], a[152:155], v[16:19]\n"
    "ds_read_b64_tr_b16 a[44:45], v221 offset:24832\n"
    "v_fma_f32 v9, v9, s36, v213\n"
    "v_mfma_f32_16x16x32_bf16 v[16:19], a[60:63], a[156:159], v[16:19]\n"
    "ds_read_b64_tr_b16 a[46:47], v221 offset:30976\n"
    "v_fma_f32 v10, v10, s36, v213\n"
    "v_mfma_f32_16x16x32_bf16 v[16:19], a[64:67], a[160:163], v[16:19]\n"
    "ds_read_b128 v[250:253], v245 offset:16384\n"
    "v_fma_f32 v11, v11, s36, v213\n"
    "v_mfma_f32_16x16x32_bf16 v[16:19], a[68:71], a[164:167], v[16:19]\n"
    "v_exp_f32 v0, v0\n"
    "v_mfma_f32_16x16x32_bf16 v[20:23], a[72:75], a[144:147], 0\n"
    "v_exp_f32 v1, v1\n"
    "v_mfma_f32_16x16x32_bf16 v[20:23], a[76:79], a[148:151], v[20:23]\n"
    "v_exp_f32 v2, v2\n"
    "v_mfma_f32_16x16x32_bf16 v[20:23], a[80:83], a[152:155], v[20:23]\n"
    "v_exp_f32 v3, v3\n"
    "v_mfma_f32_16x16x32_bf16 v[20:23], a[84:87], a[156:159], v[20:23]\n"
    "v_exp_f32 v4, v4\n"
    "v_mfma_f32_16x16x32_bf16 v[20:23], a[88:91], a[160:163], v[20:23]\n"
    "v_exp_f32 v5, v5\n"
    "v_mfma_f32_16x16x32_bf16 v[20:23], a[92:95], a[164:167], v[20:23]\n"
    "v_exp_f32 v6, v6\n"
    "v_mfma_f32_16x16x32_bf16 v[24:27], a[48:51], a[168:171], 0\n"
    "v_exp_f32 v7, v7\n"
    "v_mfma_f32_16x16x32_bf16 v[24:27], a[52:55], a[172:175], v[24:27]\n"
    "v_cvt_pk_bf16_f32 v0, v0, v1\n"
    "v_cvt_pk_bf16_f32 v1, v2, v3\n"
    "v_mfma_f32_16x16x32_bf16 v[24:27], a[56:59], a[176:179], v[24:27]\n"
    "v_cvt_pk_bf16_f32 v2, v4, v5\n"
    "v_cvt_pk_bf16_f32 v3, v6, v7\n"
    "v_mfma_f32_16x16x32_bf16 v[24:27], a[60:63], a[180:183], v[24:27]\n"
    "v_exp_f32 v8, v8\n"
    "v_mfma_f32_16x16x32_bf16 v[24:27], a[64:67], a[184:187], v[24:27]\n"
    "v_exp_f32 v9, v9\n"
    "v_mfma_f32_16x16x32_bf16 v[24:27], a[68:71], a[188:191], v[24:27]\n"
    "v_exp_f32 v10, v10\n"
    "v_mfma_f32_16x16x32_bf16 v[28:31], a[72:75], a[168:171], 0\n"
    "v_exp_f32 v11, v11\n"
    "v_mfma_f32_16x16x32_bf16 v[28:31], a[76:79], a[172:175], v[28:31]\n"
    "v_fma_f32 v12, v12, s36, v213\n"
    "v_fma_f32 v13, v13, s36, v213\n"
    "v_mfma_f32_16x16x32_bf16 v[28:31], a[80:83], a[176:179], v[28:31]\n"
    "v_exp_f32 v12, v12\n"
    "v_mfma_f32_16x16x32_bf16 v[28:31], a[84:87], a[180:183], v[28:31]\n"
    "v_exp_f32 v13, v13\n"
    "v_mfma_f32_16x16x32_bf16 v[28:31], a[88:91], a[184:187], v[28:31]\n"
    "v_fma_f32 v14, v14, s36, v213\n"
    "v_fma_f32 v15, v15, s36, v213\n"
    "v_mfma_f32_16x16x32_bf16 v[28:31], a[92:95], a[188:191], v[28:31]\n"
    "s_waitcnt vmcnt(14) lgkmcnt(0)\n"
    "s_barrier\n"
    "L_ph53:\n"
    "v_mfma_f32_16x16x32_bf16 v[64:67], a[0:3], v[32:35], v[64:67]\n"
    "buffer_load_dwordx4 v222, s[20:23], s42 offen nt lds\n"
    "v_mfma_f32_16x16x32_bf16 v[80:83], a[4:7], v[32:35], v[80:83]\n"
    "ds_read_b128 a[48:51], v216 offset:36864\n"
    "v_cvt_pk_bf16_f32 v8, v8, v9\n"
    "v_mfma_f32_16x16x32_bf16 v[96:99], a[8:11], v[32:35], v[96:99]\n"
    "buffer_load_dwordx4 v223, s[20:23], s42 offen offset:1024 nt lds\n"
    "v_mfma_f32_16x16x32_bf16 v[112:115], a[12:15], v[32:35], v[112:115]\n"
    "ds_read_b128 a[52:55], v217 offset:36864\n"
    "v_cvt_pk_bf16_f32 v9, v10, v11\n"
    "v_mfma_f32_16x16x32_bf16 v[128:131], a[16:19], v[32:35], v[128:131]\n"
    "buffer_load_dwordx4 v224, s[20:23], s42 offen offset:2048 nt lds\n"
    "v_mfma_f32_16x16x32_bf16 v[144:147], a[20:23], v[32:35], v[144:147]\n"
    "ds_read_b128 a[56:59], v216 offset:36992\n"
    "s_add_u32 s43, s40, 15360\n"
    "v_mfma_f32_16x16x32_bf16 v[160:163], a[24:27], v[32:35], v[160:163]\n"
    "buffer_store_dwordx4 v[250:253], v236, s[28:31], s43 offen offset:1024 sc1\n"
    "v_mfma_f32_16x16x32_bf16 v[176:179], a[28:31], v[32:35], v[176:179]\n"
    "ds_read_b128 a[60:63], v217 offset:36992\n"
    "s_add_u32 m0, s37, 36864\n"
    "v_mfma_f32_16x16x32_bf16 a[192:195], a[32:35], v[32:35], a[192:195]\n"
    "ds_read_b128 a[64:67], v216 offset:37120\n"
    "s_add_u32 s42, s39, 147456\n"
    "v_mfma_f32_16x16x32_bf16 a[208:211], a[36:39], v[32:35], a[208:211]\n"
    "ds_read_b128 a[68:71], v217 offset:37120\n"
    "v_cvt_pk_bf16_f32 v10, v12, v13\n"
    "v_mfma_f32_16x16x32_bf16 a[224:227], a[40:43], v[32:35], a[224:227]\n"
    "ds_read_b128 a[72:75], v216 offset:43008\n"
    "v_fma_f32 v16, v16, s36, v214\n"
    "v_mfma_f32_16x16x32_bf16 a[240:243], a[44:47], v[32:35], a[240:243]\n"
    "ds_read_b128 a[76:79], v217 offset:43008\n"
    "v_fma_f32 v17, v17, s36, v214\n"
    "v_mfma_f32_16x16x32_bf16 v[192:195], v[208:211], v[32:35], v[192:195]\n"
    "ds_read_b128 a[80:83], v216 offset:43136\n"
    "v_fma_f32 v18, v18, s36, v214\n"
    "v_mfma_f32_16x16x32_bf16 v[68:71], a[0:3], v[40:43], v[68:71]\n"
    "ds_read_b128 a[84:87], v217 offset:43136\n"
    "v_fma_f32 v19, v19, s36, v214\n"
    "v_mfma_f32_16x16x32_bf16 v[84:87], a[4:7], v[40:43], v[84:87]\n"
    "ds_read_b128 a[88:91], v216 offset:43264\n"
    "v_fma_f32 v20, v20, s36, v214\n"
    "v_mfma_f32_16x16x32_bf16 v[100:103], a[8:11], v[40:43], v[100:103]\n"
    "ds_read_b128 a[92:95], v217 offset:43264\n"
    "v_fma_f32 v21, v21, s36, v214\n"
    "v_mfma_f32_16x16x32_bf16 v[116:119], a[12:15], v[40:43], v[116:119]\n"
    "ds_read_b128 v[250:253], v246 offset:17408\n"
    "v_fma_f32 v22, v22, s36, v214\n"
    "v_mfma_f32_16x16x32_bf16 v[132:135], a[16:19], v[40:43], v[132:135]\n"
    "v_exp_f32 v14, v14\n"
    "v_mfma_f32_16x16x32_bf16 v[148:151], a[20:23], v[40:43], v[148:151]\n"
    "v_exp_f32 v15, v15\n"
    "v_mfma_f32_16x16x32_bf16 v[164:167], a[24:27], v[40:43], v[164:167]\n"
    "v_cvt_pk_bf16_f32 v11, v14, v15\n"
    "v_fma_f32 v23, v23, s36, v214\n"
    "v_mfma_f32_16x16x32_bf16 v[180:183], a[28:31], v[40:43], v[180:183]\n"
    "v_exp_f32 v16, v16\n"
    "v_mfma_f32_16x16x32_bf16 a[196:199], a[32:35], v[40:43], a[196:199]\n"
    "v_exp_f32 v17, v17\n"
    "v_mfma_f32_16x16x32_bf16 a[212:215], a[36:39], v[40:43], a[212:215]\n"
    "v_exp_f32 v18, v18\n"
    "v_mfma_f32_16x16x32_bf16 a[228:231], a[40:43], v[40:43], a[228:231]\n"
    "v_exp_f32 v19, v19\n"
    "v_mfma_f32_16x16x32_bf16 a[244:247], a[44:47], v[40:43], a[244:247]\n"
    "v_exp_f32 v20, v20\n"
    "v_mfma_f32_16x16x32_bf16 v[196:199], v[208:211], v[40:43], v[196:199]\n"
    "v_exp_f32 v21, v21\n"
    "v_mfma_f32_16x16x32_bf16 v[72:75], a[0:3], v[48:51], v[72:75]\n"
    "v_exp_f32 v22, v22\n"
    "v_mfma_f32_16x16x32_bf16 v[88:91], a[4:7], v[48:51], v[88:91]\n"
    "v_exp_f32 v23, v23\n"
    "v_mfma_f32_16x16x32_bf16 v[104:107], a[8:11], v[48:51], v[104:107]\n"
    "v_cvt_pk_bf16_f32 v16, v16, v17\n"
    "v_cvt_pk_bf16_f32 v17, v18, v19\n"
    "v_mfma_f32_16x16x32_bf16 v[120:123], a[12:15], v[48:51], v[120:123]\n"
    "v_cvt_pk_bf16_f32 v18, v20, v21\n"
    "v_cvt_pk_bf16_f32 v19, v22, v23\n"
    "v_mfma_f32_16x16x32_bf16 v[136:139], a[16:19], v[48:51], v[136:139]\n"
    "v_fma_f32 v24, v24, s36, v215\n"
    "v_fma_f32 v25, v25, s36, v215\n"
    "v_mfma_f32_16x16x32_bf16 v[152:155], a[20:23], v[48:51], v[152:155]\n"
    "v_exp_f32 v24, v24\n"
    "v_mfma_f32_16x16x32_bf16 v[168:171], a[24:27], v[48:51], v[168:171]\n"
    "v_exp_f32 v25, v25\n"
    "v_mfma_f32_16x16x32_bf16 v[184:187], a[28:31], v[48:51], v[184:187]\n"
    "v_fma_f32 v26, v26, s36, v215\n"
    "v_fma_f32 v27, v27, s36, v215\n"
    "v_mfma_f32_16x16x32_bf16 a[200:203], a[32:35], v[48:51], a[200:203]\n"
    "v_exp_f32 v26, v26\n"
    "v_mfma_f32_16x16x32_bf16 a[216:219], a[36:39], v[48:51], a[216:219]\n"
    "v_exp_f32 v27, v27\n"
    "v_mfma_f32_16x16x32_bf16 a[232:235], a[40:43], v[48:51], a[232:235]\n"
    "v_fma_f32 v28, v28, s36, v215\n"
    "v_fma_f32 v29, v29, s36, v215\n"
    "v_mfma_f32_16x16x32_bf16 a[248:251], a[44:47], v[48:51], a[248:251]\n"
    "v_exp_f32 v28, v28\n"
    "v_mfma_f32_16x16x32_bf16 v[200:203], v[208:211], v[48:51], v[200:203]\n"
    "v_exp_f32 v29, v29\n"
    "v_mfma_f32_16x16x32_bf16 v[76:79], a[0:3], v[56:59], v[76:79]\n"
    "v_fma_f32 v30, v30, s36, v215\n"
    "v_fma_f32 v31, v31, s36, v215\n"
    "v_mfma_f32_16x16x32_bf16 v[92:95], a[4:7], v[56:59], v[92:95]\n"
    "v_exp_f32 v30, v30\n"
    "v_mfma_f32_16x16x32_bf16 v[108:111], a[8:11], v[56:59], v[108:111]\n"
    "v_exp_f32 v31, v31\n"
    "v_mfma_f32_16x16x32_bf16 v[124:127], a[12:15], v[56:59], v[124:127]\n"
    "v_cvt_pk_bf16_f32 v24, v24, v25\n"
    "v_cvt_pk_bf16_f32 v25, v26, v27\n"
    "v_mfma_f32_16x16x32_bf16 v[140:143], a[16:19], v[56:59], v[140:143]\n"
    "v_cvt_pk_bf16_f32 v26, v28, v29\n"
    "v_cvt_pk_bf16_f32 v27, v30, v31\n"
    "v_mfma_f32_16x16x32_bf16 v[156:159], a[20:23], v[56:59], v[156:159]\n"
    "v_mfma_f32_16x16x32_bf16 v[172:175], a[24:27], v[56:59], v[172:175]\n"
    "v_mfma_f32_16x16x32_bf16 v[188:191], a[28:31], v[56:59], v[188:191]\n"
    "v_mfma_f32_16x16x32_bf16 a[204:207], a[32:35], v[56:59], a[204:207]\n"
    "v_mfma_f32_16x16x32_bf16 a[220:223], a[36:39], v[56:59], a[220:223]\n"
    "v_mfma_f32_16x16x32_bf16 a[236:239], a[40:43], v[56:59], a[236:239]\n"
    "v_mfma_f32_16x16x32_bf16 a[252:255], a[44:47], v[56:59], a[252:255]\n"
    "v_mfma_f32_16x16x32_bf16 v[204:207], v[208:211], v[56:59], v[204:207]\n"
    "s_waitcnt vmcnt(14) lgkmcnt(0)\n"
    "s_barrier\n"
    "L_ph54:\n"
    "v_mfma_f32_16x16x32_bf16 v[32:35], a[48:51], a[96:99], 0\n"
    "buffer_load_dwordx4 v231, s[24:27], s42 offen nt lds\n"
    "v_mfma_f32_16x16x32_bf16 v[32:35], a[52:55], a[100:103], v[32:35]\n"
    "ds_read_b64_tr_b16 a[0:1], v218 offset:49152\n"
    "v_mfma_f32_16x16x32_bf16 v[32:35], a[56:59], a[104:107], v[32:35]\n"
    "buffer_load_dwordx4 v232, s[24:27], s42 offen offset:1024 nt lds\n"
    "v_mfma_f32_16x16x32_bf16 v[32:35], a[60:63], a[108:111], v[32:35]\n"
    "ds_read_b64_tr_b16 a[2:3], v218 offset:55296\n"
    "v_mfma_f32_16x16x32_bf16 v[32:35], a[64:67], a[112:115], v[32:35]\n"
    "buffer_load_dwordx4 v233, s[24:27], s42 offen offset:2048 nt lds\n"
    "v_mfma_f32_16x16x32_bf16 v[32:35], a[68:71], a[116:119], v[32:35]\n"
    "ds_read_b64_tr_b16 a[4:5], v219 offset:49152\n"
    "s_add_u32 s43, s40, 15360\n"
    "v_mfma_f32_16x16x32_bf16 v[36:39], a[72:75], a[96:99], 0\n"
    "buffer_store_dwordx4 v[250:253], v236, s[28:31], s43 offen offset:2048 sc1\n"
    "v_mfma_f32_16x16x32_bf16 v[36:39], a[76:79], a[100:103], v[36:39]\n"
    "ds_read_b64_tr_b16 a[6:7], v219 offset:55296\n"
    "s_add_u32 m0, s37, 49152\n"
    "v_mfma_f32_16x16x32_bf16 v[36:39], a[80:83], a[104:107], v[36:39]\n"
    "ds_read_b64_tr_b16 a[8:9], v220 offset:49152\n"
    "s_add_u32 s42, s39, 172032\n"
    "v_mfma_f32_16x16x32_bf16 v[36:39], a[84:87], a[108:111], v[36:39]\n"
    "ds_read_b64_tr_b16 a[10:11], v220 offset:55296\n"
    "v_mfma_f32_16x16x32_bf16 v[36:39], a[88:91], a[112:115], v[36:39]\n"
    "ds_read_b64_tr_b16 a[12:13], v221 offset:49152\n"
    "v_mfma_f32_16x16x32_bf16 v[36:39], a[92:95], a[116:119], v[36:39]\n"
    "ds_read_b64_tr_b16 a[14:15], v221 offset:55296\n"
    "v_mfma_f32_16x16x32_bf16 v[40:43], a[48:51], a[120:123], 0\n"
    "ds_read_b64_tr_b16 a[16:17], v218 offset:49280\n"
    "v_mfma_f32_16x16x32_bf16 v[40:43], a[52:55], a[124:127], v[40:43]\n"
    "ds_read_b64_tr_b16 a[18:19], v218 offset:55424\n"
    "v_fma_f32 v32, v32, s36, v212\n"
    "v_mfma_f32_16x16x32_bf16 v[40:43], a[56:59], a[128:131], v[40:43]\n"
    "ds_read_b64_tr_b16 a[20:21], v219 offset:49280\n"
    "v_fma_f32 v33, v33, s36, v212\n"
    "v_mfma_f32_16x16x32_bf16 v[40:43], a[60:63], a[132:135], v[40:43]\n"
    "ds_read_b64_tr_b16 a[22:23], v219 offset:55424\n"
    "v_fma_f32 v34, v34, s36, v212\n"
    "v_mfma_f32_16x16x32_bf16 v[40:43], a[64:67], a[136:139], v[40:43]\n"
    "ds_read_b64_tr_b16 a[24:25], v220 offset:49280\n"
    "v_fma_f32 v35, v35, s36, v212\n"
    "v_mfma_f32_16x16x32_bf16 v[40:43], a[68:71], a[140:143], v[40:43]\n"
    "ds_read_b64_tr_b16 a[26:27], v220 offset:55424\n"
    "v_mfma_f32_16x16x32_bf16 v[44:47], a[72:75], a[120:123], 0\n"
    "ds_read_b64_tr_b16 a[28:29], v221 offset:49280\n"
    "v_mfma_f32_16x16x32_bf16 v[44:47], a[76:79], a[124:127], v[44:47]\n"
    "ds_read_b64_tr_b16 a[30:31], v221 offset:55424\n"
    "v_fma_f32 v36, v36, s36, v212\n"
    "v_mfma_f32_16x16x32_bf16 v[44:47], a[80:83], a[128:131], v[44:47]\n"
    "ds_read_b64_tr_b16 a[32:33], v218 offset:49408\n"
    "v_fma_f32 v37, v37, s36, v212\n"
    "v_mfma_f32_16x16x32_bf16 v[44:47], a[84:87], a[132:135], v[44:47]\n"
    "ds_read_b64_tr_b16 a[34:35], v218 offset:55552\n"
    "v_fma_f32 v38, v38, s36, v212\n"
    "v_mfma_f32_16x16x32_bf16 v[44:47], a[88:91], a[136:139], v[44:47]\n"
    "ds_read_b64_tr_b16 a[36:37], v219 offset:49408\n"
    "v_fma_f32 v39, v39, s36, v212\n"
    "v_mfma_f32_16x16x32_bf16 v[44:47], a[92:95], a[140:143], v[44:47]\n"
    "ds_read_b64_tr_b16 a[38:39], v219 offset:55552\n"
    "v_mfma_f32_16x16x32_bf16 v[48:51], a[48:51], a[144:147], 0\n"
    "ds_read_b64_tr_b16 a[40:41], v220 offset:49408\n"
    "v_mfma_f32_16x16x32_bf16 v[48:51], a[52:55], a[148:151], v[48:51]\n"
    "ds_read_b64_tr_b16 a[42:43], v220 offset:55552\n"
    "v_fma_f32 v40, v40, s36, v213\n"
    "v_mfma_f32_16x16x32_bf16 v[48:51], a[56:59], a[152:155], v[48:51]\n"
    "ds_read_b64_tr_b16 a[44:45], v221 offset:49408\n"
    "v_fma_f32 v41, v41, s36, v213\n"
    "v_mfma_f32_16x16x32_bf16 v[48:51], a[60:63], a[156:159], v[48:51]\n"
    "ds_read_b64_tr_b16 a[46:47], v221 offset:55552\n"
    "v_fma_f32 v42, v42, s36, v213\n"
    "v_mfma_f32_16x16x32_bf16 v[48:51], a[64:67], a[160:163], v[48:51]\n"
    "ds_read_b128 v[250:253], v241 offset:18432\n"
    "v_fma_f32 v43, v43, s36, v213\n"
    "v_mfma_f32_16x16x32_bf16 v[48:51], a[68:71], a[164:167], v[48:51]\n"
    "v_exp_f32 v32, v32\n"
    "v_mfma_f32_16x16x32_bf16 v[52:55], a[72:75], a[144:147], 0\n"
    "v_exp_f32 v33, v33\n"
    "v_mfma_f32_16x16x32_bf16 v[52:55], a[76:79], a[148:151], v[52:55]\n"
    "v_exp_f32 v34, v34\n"
    "v_mfma_f32_16x16x32_bf16 v[52:55], a[80:83], a[152:155], v[52:55]\n"
    "v_exp_f32 v35, v35\n"
    "v_mfma_f32_16x16x32_bf16 v[52:55], a[84:87], a[156:159], v[52:55]\n"
    "v_exp_f32 v36, v36\n"
    "v_mfma_f32_16x16x32_bf16 v[52:55], a[88:91], a[160:163], v[52:55]\n"
    "v_exp_f32 v37, v37\n"
    "v_mfma_f32_16x16x32_bf16 v[52:55], a[92:95], a[164:167], v[52:55]\n"
    "v_exp_f32 v38, v38\n"
    "v_mfma_f32_16x16x32_bf16 v[56:59], a[48:51], a[168:171], 0\n"
    "v_exp_f32 v39, v39\n"
    "v_mfma_f32_16x16x32_bf16 v[56:59], a[52:55], a[172:175], v[56:59]\n"
    "v_cvt_pk_bf16_f32 v32, v32, v33\n"
    "v_cvt_pk_bf16_f32 v33, v34, v35\n"
    "v_mfma_f32_16x16x32_bf16 v[56:59], a[56:59], a[176:179], v[56:59]\n"
    "v_cvt_pk_bf16_f32 v34, v36, v37\n"
    "v_cvt_pk_bf16_f32 v35, v38, v39\n"
    "v_mfma_f32_16x16x32_bf16 v[56:59], a[60:63], a[180:183], v[56:59]\n"
    "v_exp_f32 v40, v40\n"
    "v_mfma_f32_16x16x32_bf16 v[56:59], a[64:67], a[184:187], v[56:59]\n"
    "v_exp_f32 v41, v41\n"
    "v_mfma_f32_16x16x32_bf16 v[56:59], a[68:71], a[188:191], v[56:59]\n"
    "v_exp_f32 v42, v42\n"
    "v_mfma_f32_16x16x32_bf16 v[60:63], a[72:75], a[168:171], 0\n"
    "v_exp_f32 v43, v43\n"
    "v_mfma_f32_16x16x32_bf16 v[60:63], a[76:79], a[172:175], v[60:63]\n"
    "v_fma_f32 v44, v44, s36, v213\n"
    "v_fma_f32 v45, v45, s36, v213\n"
    "v_mfma_f32_16x16x32_bf16 v[60:63], a[80:83], a[176:179], v[60:63]\n"
    "v_exp_f32 v44, v44\n"
    "v_mfma_f32_16x16x32_bf16 v[60:63], a[84:87], a[180:183], v[60:63]\n"
    "v_exp_f32 v45, v45\n"
    "v_mfma_f32_16x16x32_bf16 v[60:63], a[88:91], a[184:187], v[60:63]\n"
    "v_fma_f32 v46, v46, s36, v213\n"
    "v_fma_f32 v47, v47, s36, v213\n"
    "v_mfma_f32_16x16x32_bf16 v[60:63], a[92:95], a[188:191], v[60:63]\n"
    "s_waitcnt vmcnt(14) lgkmcnt(0)\n"
    "s_barrier\n"
    "L_ph55:\n"
    "v_mfma_f32_16x16x32_bf16 v[64:67], a[0:3], v[0:3], v[64:67]\n"
    "buffer_load_dwordx4 v222, s[20:23], s42 offen nt lds\n"
    "v_mfma_f32_16x16x32_bf16 v[80:83], a[4:7], v[0:3], v[80:83]\n"
    "ds_read_b128 a[48:51], v216 offset:0\n"
    "v_cvt_pk_bf16_f32 v40, v40, v41\n"
    "v_mfma_f32_16x16x32_bf16 v[96:99], a[8:11], v[0:3], v[96:99]\n"
    "buffer_load_dwordx4 v223, s[20:23], s42 offen offset:1024 nt lds\n"
    "v_mfma_f32_16x16x32_bf16 v[112:115], a[12:15], v[0:3], v[112:115]\n"
    "ds_read_b128 a[52:55], v217 offset:0\n"
    "v_cvt_pk_bf16_f32 v41, v42, v43\n"
    "v_mfma_f32_16x16x32_bf16 v[128:131], a[16:19], v[0:3], v[128:131]\n"
    "buffer_load_dwordx4 v224, s[20:23], s42 offen offset:2048 nt lds\n"
    "v_mfma_f32_16x16x32_bf16 v[144:147], a[20:23], v[0:3], v[144:147]\n"
    "ds_read_b128 a[56:59], v216 offset:128\n"
    "s_add_u32 s43, s40, 18432\n"
    "v_mfma_f32_16x16x32_bf16 v[160:163], a[24:27], v[0:3], v[160:163]\n"
    "buffer_store_dwordx4 v[250:253], v236, s[28:31], s43 offen sc1\n"
    "v_mfma_f32_16x16x32_bf16 v[176:179], a[28:31], v[0:3], v[176:179]\n"
    "s_mov_b32 m0, 159744\n"
    "s_mov_b32 s51, L_ph59-L_code_start\n"
    "v_mfma_f32_16x16x32_bf16 a[192:195], a[32:35], v[0:3], a[192:195]\n"
    "buffer_load_dword v249, s[52:55], s51 offen lds\n"
    "v_mfma_f32_16x16x32_bf16 a[208:211], a[36:39], v[0:3], a[208:211]\n"
    "ds_read_b128 a[60:63], v217 offset:128\n"
    "s_add_u32 m0, s37, 0\n"
    "v_mfma_f32_16x16x32_bf16 a[224:227], a[40:43], v[0:3], a[224:227]\n"
    "ds_read_b128 a[64:67], v216 offset:256\n"
    "s_add_u32 s42, s39, 159744\n"
    "v_mfma_f32_16x16x32_bf16 a[240:243], a[44:47], v[0:3], a[240:243]\n"
    "ds_read_b128 a[68:71], v217 offset:256\n"
    "v_cvt_pk_bf16_f32 v42, v44, v45\n"
    "v_mfma_f32_16x16x32_bf16 v[192:195], v[208:211], v[0:3], v[192:195]\n"
    "ds_read_b128 a[72:75], v216 offset:6144\n"
    "v_fma_f32 v48, v48, s36, v214\n"
    "v_mfma_f32_16x16x32_bf16 v[68:71], a[0:3], v[8:11], v[68:71]\n"
    "ds_read_b128 a[76:79], v217 offset:6144\n"
    "v_fma_f32 v49, v49, s36, v214\n"
    "v_mfma_f32_16x16x32_bf16 v[84:87], a[4:7], v[8:11], v[84:87]\n"
    "ds_read_b128 a[80:83], v216 offset:6272\n"
    "v_fma_f32 v50, v50, s36, v214\n"
    "v_mfma_f32_16x16x32_bf16 v[100:103], a[8:11], v[8:11], v[100:103]\n"
    "ds_read_b128 a[84:87], v217 offset:6272\n"
    "v_fma_f32 v51, v51, s36, v214\n"
    "v_mfma_f32_16x16x32_bf16 v[116:119], a[12:15], v[8:11], v[116:119]\n"
    "ds_read_b128 a[88:91], v216 offset:6400\n"
    "v_fma_f32 v52, v52, s36, v214\n"
    "v_mfma_f32_16x16x32_bf16 v[132:135], a[16:19], v[8:11], v[132:135]\n"
    "ds_read_b128 a[92:95], v217 offset:6400\n"
    "v_fma_f32 v53, v53, s36, v214\n"
    "v_mfma_f32_16x16x32_bf16 v[148:151], a[20:23], v[8:11], v[148:151]\n"
    "ds_read_b128 v[250:253], v242 offset:19456\n"
    "v_fma_f32 v54, v54, s36, v214\n"
    "v_mfma_f32_16x16x32_bf16 v[164:167], a[24:27], v[8:11], v[164:167]\n"
    "v_exp_f32 v46, v46\n"
    "v_mfma_f32_16x16x32_bf16 v[180:183], a[28:31], v[8:11], v[180:183]\n"
    "v_exp_f32 v47, v47\n"
    "v_mfma_f32_16x16x32_bf16 a[196:199], a[32:35], v[8:11], a[196:199]\n"
    "v_cvt_pk_bf16_f32 v43, v46, v47\n"
    "v_fma_f32 v55, v55, s36, v214\n"
    "v_mfma_f32_16x16x32_bf16 a[212:215], a[36:39], v[8:11], a[212:215]\n"
    "v_exp_f32 v48, v48\n"
    "v_mfma_f32_16x16x32_bf16 a[228:231], a[40:43], v[8:11], a[228:231]\n"
    "v_exp_f32 v49, v49\n"
    "v_mfma_f32_16x16x32_bf16 a[244:247], a[44:47], v[8:11], a[244:247]\n"
    "v_exp_f32 v50, v50\n"
    "v_mfma_f32_16x16x32_bf16 v[196:199], v[208:211], v[8:11], v[196:199]\n"
    "v_exp_f32 v51, v51\n"
    "v_mfma_f32_16x16x32_bf16 v[72:75], a[0:3], v[16:19], v[72:75]\n"
    "v_exp_f32 v52, v52\n"
    "v_mfma_f32_16x16x32_bf16 v[88:91], a[4:7], v[16:19], v[88:91]\n"
    "v_exp_f32 v53, v53\n"
    "v_mfma_f32_16x16x32_bf16 v[104:107], a[8:11], v[16:19], v[104:107]\n"
    "v_exp_f32 v54, v54\n"
    "v_mfma_f32_16x16x32_bf16 v[120:123], a[12:15], v[16:19], v[120:123]\n"
    "v_exp_f32 v55, v55\n"
    "v_mfma_f32_16x16x32_bf16 v[136:139], a[16:19], v[16:19], v[136:139]\n"
    "v_cvt_pk_bf16_f32 v48, v48, v49\n"
    "v_cvt_pk_bf16_f32 v49, v50, v51\n"
    "v_mfma_f32_16x16x32_bf16 v[152:155], a[20:23], v[16:19], v[152:155]\n"
    "v_cvt_pk_bf16_f32 v50, v52, v53\n"
    "v_cvt_pk_bf16_f32 v51, v54, v55\n"
    "v_mfma_f32_16x16x32_bf16 v[168:171], a[24:27], v[16:19], v[168:171]\n"
    "v_fma_f32 v56, v56, s36, v215\n"
    "v_fma_f32 v57, v57, s36, v215\n"
    "v_mfma_f32_16x16x32_bf16 v[184:187], a[28:31], v[16:19], v[184:187]\n"
    "v_exp_f32 v56, v56\n"
    "v_mfma_f32_16x16x32_bf16 a[200:203], a[32:35], v[16:19], a[200:203]\n"
    "v_exp_f32 v57, v57\n"
    "v_mfma_f32_16x16x32_bf16 a[216:219], a[36:39], v[16:19], a[216:219]\n"
    "v_fma_f32 v58, v58, s36, v215\n"
    "v_fma_f32 v59, v59, s36, v215\n"
    "v_mfma_f32_16x16x32_bf16 a[232:235], a[40:43], v[16:19], a[232:235]\n"
    "v_exp_f32 v58, v58\n"
    "v_mfma_f32_16x16x32_bf16 a[248:251], a[44:47], v[16:19], a[248:251]\n"
    "v_exp_f32 v59, v59\n"
    "v_mfma_f32_16x16x32_bf16 v[200:203], v[208:211], v[16:19], v[200:203]\n"
    "v_fma_f32 v60, v60, s36, v215\n"
    "v_fma_f32 v61, v61, s36, v215\n"
    "v_mfma_f32_16x16x32_bf16 v[76:79], a[0:3], v[24:27], v[76:79]\n"
    "v_exp_f32 v60, v60\n"
    "v_mfma_f32_16x16x32_bf16 v[92:95], a[4:7], v[24:27], v[92:95]\n"
    "v_exp_f32 v61, v61\n"
    "v_mfma_f32_16x16x32_bf16 v[108:111], a[8:11], v[24:27], v[108:111]\n"
    "v_fma_f32 v62, v62, s36, v215\n"
    "v_fma_f32 v63, v63, s36, v215\n"
    "v_mfma_f32_16x16x32_bf16 v[124:127], a[12:15], v[24:27], v[124:127]\n"
    "v_exp_f32 v62, v62\n"
    "v_mfma_f32_16x16x32_bf16 v[140:143], a[16:19], v[24:27], v[140:143]\n"
    "v_exp_f32 v63, v63\n"
    "v_mfma_f32_16x16x32_bf16 v[156:159], a[20:23], v[24:27], v[156:159]\n"
    "v_cvt_pk_bf16_f32 v56, v56, v57\n"
    "v_cvt_pk_bf16_f32 v57, v58, v59\n"
    "v_mfma_f32_16x16x32_bf16 v[172:175], a[24:27], v[24:27], v[172:175]\n"
    "v_cvt_pk_bf16_f32 v58, v60, v61\n"
    "v_cvt_pk_bf16_f32 v59, v62, v63\n"
    "v_mfma_f32_16x16x32_bf16 v[188:191], a[28:31], v[24:27], v[188:191]\n"
    "v_mfma_f32_16x16x32_bf16 a[204:207], a[32:35], v[24:27], a[204:207]\n"
    "v_mfma_f32_16x16x32_bf16 a[220:223], a[36:39], v[24:27], a[220:223]\n"
    "v_mfma_f32_16x16x32_bf16 a[236:239], a[40:43], v[24:27], a[236:239]\n"
    "v_mfma_f32_16x16x32_bf16 a[252:255], a[44:47], v[24:27], a[252:255]\n"
    "v_mfma_f32_16x16x32_bf16 v[204:207], v[208:211], v[24:27], v[204:207]\n"
    "s_waitcnt vmcnt(14) lgkmcnt(0)\n"
    "s_barrier\n"
    "L_ph56:\n"
    "v_mfma_f32_16x16x32_bf16 v[0:3], a[48:51], a[96:99], 0\n"
    "buffer_load_dwordx4 v231, s[24:27], s42 offen nt lds\n"
    "v_mfma_f32_16x16x32_bf16 v[0:3], a[52:55], a[100:103], v[0:3]\n"
    "ds_read_b64_tr_b16 a[0:1], v218 offset:12288\n"
    "v_mfma_f32_16x16x32_bf16 v[0:3], a[56:59], a[104:107], v[0:3]\n"
    "buffer_load_dwordx4 v232, s[24:27], s42 offen offset:1024 nt lds\n"
    "v_mfma_f32_16x16x32_bf16 v[0:3], a[60:63], a[108:111], v[0:3]\n"
    "ds_read_b64_tr_b16 a[2:3], v218 offset:18432\n"
    "v_mfma_f32_16x16x32_bf16 v[0:3], a[64:67], a[112:115], v[0:3]\n"
    "buffer_load_dwordx4 v233, s[24:27], s42 offen offset:2048 nt lds\n"
    "v_mfma_f32_16x16x32_bf16 v[0:3], a[68:71], a[116:119], v[0:3]\n"
    "ds_read_b64_tr_b16 a[4:5], v219 offset:12288\n"
    "s_add_u32 s43, s40, 18432\n"
    "v_mfma_f32_16x16x32_bf16 v[4:7], a[72:75], a[96:99], 0\n"
    "buffer_store_dwordx4 v[250:253], v236, s[28:31], s43 offen offset:1024 sc1\n"
    "v_mfma_f32_16x16x32_bf16 v[4:7], a[76:79], a[100:103], v[4:7]\n"
    "ds_read_b64_tr_b16 a[6:7], v219 offset:18432\n"
    "s_add_u32 m0, s37, 12288\n"
    "v_mfma_f32_16x16x32_bf16 v[4:7], a[80:83], a[104:107], v[4:7]\n"
    "ds_read_b64_tr_b16 a[8:9], v220 offset:12288\n"
    "s_add_u32 s42, s39, 184320\n"
    "v_mfma_f32_16x16x32_bf16 v[4:7], a[84:87], a[108:111], v[4:7]\n"
    "ds_read_b64_tr_b16 a[10:11], v220 offset:18432\n"
    "v_mfma_f32_16x16x32_bf16 v[4:7], a[88:91], a[112:115], v[4:7]\n"
    "ds_read_b64_tr_b16 a[12:13], v221 offset:12288\n"
    "v_mfma_f32_16x16x32_bf16 v[4:7], a[92:95], a[116:119], v[4:7]\n"
    "ds_read_b64_tr_b16 a[14:15], v221 offset:18432\n"
    "v_mfma_f32_16x16x32_bf16 v[8:11], a[48:51], a[120:123], 0\n"
    "ds_read_b64_tr_b16 a[16:17], v218 offset:12416\n"
    "v_mfma_f32_16x16x32_bf16 v[8:11], a[52:55], a[124:127], v[8:11]\n"
    "ds_read_b64_tr_b16 a[18:19], v218 offset:18560\n"
    "v_fma_f32 v0, v0, s36, v212\n"
    "v_mfma_f32_16x16x32_bf16 v[8:11], a[56:59], a[128:131], v[8:11]\n"
    "ds_read_b64_tr_b16 a[20:21], v219 offset:12416\n"
    "v_fma_f32 v1, v1, s36, v212\n"
    "v_mfma_f32_16x16x32_bf16 v[8:11], a[60:63], a[132:135], v[8:11]\n"
    "ds_read_b64_tr_b16 a[22:23], v219 offset:18560\n"
    "v_fma_f32 v2, v2, s36, v212\n"
    "v_mfma_f32_16x16x32_bf16 v[8:11], a[64:67], a[136:139], v[8:11]\n"
    "ds_read_b64_tr_b16 a[24:25], v220 offset:12416\n"
    "v_fma_f32 v3, v3, s36, v212\n"
    "v_mfma_f32_16x16x32_bf16 v[8:11], a[68:71], a[140:143], v[8:11]\n"
    "ds_read_b64_tr_b16 a[26:27], v220 offset:18560\n"
    "v_mfma_f32_16x16x32_bf16 v[12:15], a[72:75], a[120:123], 0\n"
    "ds_read_b64_tr_b16 a[28:29], v221 offset:12416\n"
    "v_mfma_f32_16x16x32_bf16 v[12:15], a[76:79], a[124:127], v[12:15]\n"
    "ds_read_b64_tr_b16 a[30:31], v221 offset:18560\n"
    "v_fma_f32 v4, v4, s36, v212\n"
    "v_mfma_f32_16x16x32_bf16 v[12:15], a[80:83], a[128:131], v[12:15]\n"
    "ds_read_b64_tr_b16 a[32:33], v218 offset:12544\n"
    "v_fma_f32 v5, v5, s36, v212\n"
    "v_mfma_f32_16x16x32_bf16 v[12:15], a[84:87], a[132:135], v[12:15]\n"
    "ds_read_b64_tr_b16 a[34:35], v218 offset:18688\n"
    "v_fma_f32 v6, v6, s36, v212\n"
    "v_mfma_f32_16x16x32_bf16 v[12:15], a[88:91], a[136:139], v[12:15]\n"
    "ds_read_b64_tr_b16 a[36:37], v219 offset:12544\n"
    "v_fma_f32 v7, v7, s36, v212\n"
    "v_mfma_f32_16x16x32_bf16 v[12:15], a[92:95], a[140:143], v[12:15]\n"
    "ds_read_b64_tr_b16 a[38:39], v219 offset:18688\n"
    "v_mfma_f32_16x16x32_bf16 v[16:19], a[48:51], a[144:147], 0\n"
    "ds_read_b64_tr_b16 a[40:41], v220 offset:12544\n"
    "v_mfma_f32_16x16x32_bf16 v[16:19], a[52:55], a[148:151], v[16:19]\n"
    "ds_read_b64_tr_b16 a[42:43], v220 offset:18688\n"
    "v_fma_f32 v8, v8, s36, v213\n"
    "v_mfma_f32_16x16x32_bf16 v[16:19], a[56:59], a[152:155], v[16:19]\n"
    "ds_read_b64_tr_b16 a[44:45], v221 offset:12544\n"
    "v_fma_f32 v9, v9, s36, v213\n"
    "v_mfma_f32_16x16x32_bf16 v[16:19], a[60:63], a[156:159], v[16:19]\n"
    "ds_read_b64_tr_b16 a[46:47], v221 offset:18688\n"
    "v_fma_f32 v10, v10, s36, v213\n"
    "v_mfma_f32_16x16x32_bf16 v[16:19], a[64:67], a[160:163], v[16:19]\n"
    "ds_read_b128 v[250:253], v243 offset:20480\n"
    "v_fma_f32 v11, v11, s36, v213\n"
    "v_mfma_f32_16x16x32_bf16 v[16:19], a[68:71], a[164:167], v[16:19]\n"
    "v_exp_f32 v0, v0\n"
    "v_mfma_f32_16x16x32_bf16 v[20:23], a[72:75], a[144:147], 0\n"
    "v_exp_f32 v1, v1\n"
    "v_mfma_f32_16x16x32_bf16 v[20:23], a[76:79], a[148:151], v[20:23]\n"
    "v_exp_f32 v2, v2\n"
    "v_mfma_f32_16x16x32_bf16 v[20:23], a[80:83], a[152:155], v[20:23]\n"
    "v_exp_f32 v3, v3\n"
    "v_mfma_f32_16x16x32_bf16 v[20:23], a[84:87], a[156:159], v[20:23]\n"
    "v_exp_f32 v4, v4\n"
    "v_mfma_f32_16x16x32_bf16 v[20:23], a[88:91], a[160:163], v[20:23]\n"
    "v_exp_f32 v5, v5\n"
    "v_mfma_f32_16x16x32_bf16 v[20:23], a[92:95], a[164:167], v[20:23]\n"
    "v_exp_f32 v6, v6\n"
    "v_mfma_f32_16x16x32_bf16 v[24:27], a[48:51], a[168:171], 0\n"
    "v_exp_f32 v7, v7\n"
    "v_mfma_f32_16x16x32_bf16 v[24:27], a[52:55], a[172:175], v[24:27]\n"
    "v_cvt_pk_bf16_f32 v0, v0, v1\n"
    "v_cvt_pk_bf16_f32 v1, v2, v3\n"
    "v_mfma_f32_16x16x32_bf16 v[24:27], a[56:59], a[176:179], v[24:27]\n"
    "v_cvt_pk_bf16_f32 v2, v4, v5\n"
    "v_cvt_pk_bf16_f32 v3, v6, v7\n"
    "v_mfma_f32_16x16x32_bf16 v[24:27], a[60:63], a[180:183], v[24:27]\n"
    "v_exp_f32 v8, v8\n"
    "v_mfma_f32_16x16x32_bf16 v[24:27], a[64:67], a[184:187], v[24:27]\n"
    "v_exp_f32 v9, v9\n"
    "v_mfma_f32_16x16x32_bf16 v[24:27], a[68:71], a[188:191], v[24:27]\n"
    "v_exp_f32 v10, v10\n"
    "v_mfma_f32_16x16x32_bf16 v[28:31], a[72:75], a[168:171], 0\n"
    "v_exp_f32 v11, v11\n"
    "v_mfma_f32_16x16x32_bf16 v[28:31], a[76:79], a[172:175], v[28:31]\n"
    "v_fma_f32 v12, v12, s36, v213\n"
    "v_fma_f32 v13, v13, s36, v213\n"
    "v_mfma_f32_16x16x32_bf16 v[28:31], a[80:83], a[176:179], v[28:31]\n"
    "v_exp_f32 v12, v12\n"
    "v_mfma_f32_16x16x32_bf16 v[28:31], a[84:87], a[180:183], v[28:31]\n"
    "v_exp_f32 v13, v13\n"
    "v_mfma_f32_16x16x32_bf16 v[28:31], a[88:91], a[184:187], v[28:31]\n"
    "v_fma_f32 v14, v14, s36, v213\n"
    "v_fma_f32 v15, v15, s36, v213\n"
    "v_mfma_f32_16x16x32_bf16 v[28:31], a[92:95], a[188:191], v[28:31]\n"
    "s_waitcnt vmcnt(14) lgkmcnt(0)\n"
    "s_barrier\n"
    "L_ph57:\n"
    "v_mfma_f32_16x16x32_bf16 v[64:67], a[0:3], v[32:35], v[64:67]\n"
    "buffer_load_dwordx4 v222, s[20:23], s42 offen nt lds\n"
    "v_mfma_f32_16x16x32_bf16 v[80:83], a[4:7], v[32:35], v[80:83]\n"
    "ds_read_b128 a[48:51], v216 offset:24576\n"
    "v_cvt_pk_bf16_f32 v8, v8, v9\n"
    "v_mfma_f32_16x16x32_bf16 v[96:99], a[8:11], v[32:35], v[96:99]\n"
    "buffer_load_dwordx4 v223, s[20:23], s42 offen offset:1024 nt lds\n"
    "v_mfma_f32_16x16x32_bf16 v[112:115], a[12:15], v[32:35], v[112:115]\n"
    "ds_read_b128 a[52:55], v217 offset:24576\n"
    "v_cvt_pk_bf16_f32 v9, v10, v11\n"
    "v_mfma_f32_16x16x32_bf16 v[128:131], a[16:19], v[32:35], v[128:131]\n"
    "buffer_load_dwordx4 v224, s[20:23], s42 offen offset:2048 nt lds\n"
    "v_mfma_f32_16x16x32_bf16 v[144:147], a[20:23], v[32:35], v[144:147]\n"
    "ds_read_b128 a[56:59], v216 offset:24704\n"
    "s_add_u32 s43, s40, 18432\n"
    "v_mfma_f32_16x16x32_bf16 v[160:163], a[24:27], v[32:35], v[160:163]\n"
    "buffer_store_dwordx4 v[250:253], v236, s[28:31], s43 offen offset:2048 sc1\n"
    "v_mfma_f32_16x16x32_bf16 v[176:179], a[28:31], v[32:35], v[176:179]\n"
    "ds_read_b128 a[60:63], v217 offset:24704\n"
    "s_add_u32 m0, s37, 24576\n"
    "v_mfma_f32_16x16x32_bf16 a[192:195], a[32:35], v[32:35], a[192:195]\n"
    "ds_read_b128 a[64:67], v216 offset:24832\n"
    "s_add_u32 s42, s39, 172032\n"
    "v_mfma_f32_16x16x32_bf16 a[208:211], a[36:39], v[32:35], a[208:211]\n"
    "ds_read_b128 a[68:71], v217 offset:24832\n"
    "v_cvt_pk_bf16_f32 v10, v12, v13\n"
    "v_mfma_f32_16x16x32_bf16 a[224:227], a[40:43], v[32:35], a[224:227]\n"
    "ds_read_b128 a[72:75], v216 offset:30720\n"
    "v_fma_f32 v16, v16, s36, v214\n"
    "v_mfma_f32_16x16x32_bf16 a[240:243], a[44:47], v[32:35], a[240:243]\n"
    "ds_read_b128 a[76:79], v217 offset:30720\n"
    "v_fma_f32 v17, v17, s36, v214\n"
    "v_mfma_f32_16x16x32_bf16 v[192:195], v[208:211], v[32:35], v[192:195]\n"
    "ds_read_b128 a[80:83], v216 offset:30848\n"
    "v_fma_f32 v18, v18, s36, v214\n"
    "v_mfma_f32_16x16x32_bf16 v[68:71], a[0:3], v[40:43], v[68:71]\n"
    "ds_read_b128 a[84:87], v217 offset:30848\n"
    "v_fma_f32 v19, v19, s36, v214\n"
    "v_mfma_f32_16x16x32_bf16 v[84:87], a[4:7], v[40:43], v[84:87]\n"
    "ds_read_b128 a[88:91], v216 offset:30976\n"
    "v_fma_f32 v20, v20, s36, v214\n"
    "v_mfma_f32_16x16x32_bf16 v[100:103], a[8:11], v[40:43], v[100:103]\n"
    "ds_read_b128 a[92:95], v217 offset:30976\n"
    "v_fma_f32 v21, v21, s36, v214\n"
    "v_mfma_f32_16x16x32_bf16 v[116:119], a[12:15], v[40:43], v[116:119]\n"
    "ds_read_b128 v[250:253], v244 offset:21504\n"
    "v_fma_f32 v22, v22, s36, v214\n"
    "v_mfma_f32_16x16x32_bf16 v[132:135], a[16:19], v[40:43], v[132:135]\n"
    "v_exp_f32 v14, v14\n"
    "v_mfma_f32_16x16x32_bf16 v[148:151], a[20:23], v[40:43], v[148:151]\n"
    "v_exp_f32 v15, v15\n"
    "v_mfma_f32_16x16x32_bf16 v[164:167], a[24:27], v[40:43], v[164:167]\n"
    "v_cvt_pk_bf16_f32 v11, v14, v15\n"
    "v_fma_f32 v23, v23, s36, v214\n"
    "v_mfma_f32_16x16x32_bf16 v[180:183], a[28:31], v[40:43], v[180:183]\n"
    "v_exp_f32 v16, v16\n"
    "v_mfma_f32_16x16x32_bf16 a[196:199], a[32:35], v[40:43], a[196:199]\n"
    "v_exp_f32 v17, v17\n"
    "v_mfma_f32_16x16x32_bf16 a[212:215], a[36:39], v[40:43], a[212:215]\n"
    "v_exp_f32 v18, v18\n"
    "v_mfma_f32_16x16x32_bf16 a[228:231], a[40:43], v[40:43], a[228:231]\n"
    "v_exp_f32 v19, v19\n"
    "v_mfma_f32_16x16x32_bf16 a[244:247], a[44:47], v[40:43], a[244:247]\n"
    "v_exp_f32 v20, v20\n"
    "v_mfma_f32_16x16x32_bf16 v[196:199], v[208:211], v[40:43], v[196:199]\n"
    "v_exp_f32 v21, v21\n"
    "v_mfma_f32_16x16x32_bf16 v[72:75], a[0:3], v[48:51], v[72:75]\n"
    "v_exp_f32 v22, v22\n"
    "v_mfma_f32_16x16x32_bf16 v[88:91], a[4:7], v[48:51], v[88:91]\n"
    "v_exp_f32 v23, v23\n"
    "v_mfma_f32_16x16x32_bf16 v[104:107], a[8:11], v[48:51], v[104:107]\n"
    "v_cvt_pk_bf16_f32 v16, v16, v17\n"
    "v_cvt_pk_bf16_f32 v17, v18, v19\n"
    "v_mfma_f32_16x16x32_bf16 v[120:123], a[12:15], v[48:51], v[120:123]\n"
    "v_cvt_pk_bf16_f32 v18, v20, v21\n"
    "v_cvt_pk_bf16_f32 v19, v22, v23\n"
    "v_mfma_f32_16x16x32_bf16 v[136:139], a[16:19], v[48:51], v[136:139]\n"
    "v_fma_f32 v24, v24, s36, v215\n"
    "v_fma_f32 v25, v25, s36, v215\n"
    "v_mfma_f32_16x16x32_bf16 v[152:155], a[20:23], v[48:51], v[152:155]\n"
    "v_exp_f32 v24, v24\n"
    "v_mfma_f32_16x16x32_bf16 v[168:171], a[24:27], v[48:51], v[168:171]\n"
    "v_exp_f32 v25, v25\n"
    "v_mfma_f32_16x16x32_bf16 v[184:187], a[28:31], v[48:51], v[184:187]\n"
    "v_fma_f32 v26, v26, s36, v215\n"
    "v_fma_f32 v27, v27, s36, v215\n"
    "v_mfma_f32_16x16x32_bf16 a[200:203], a[32:35], v[48:51], a[200:203]\n"
    "v_exp_f32 v26, v26\n"
    "v_mfma_f32_16x16x32_bf16 a[216:219], a[36:39], v[48:51], a[216:219]\n"
    "v_exp_f32 v27, v27\n"
    "v_mfma_f32_16x16x32_bf16 a[232:235], a[40:43], v[48:51], a[232:235]\n"
    "v_fma_f32 v28, v28, s36, v215\n"
    "v_fma_f32 v29, v29, s36, v215\n"
    "v_mfma_f32_16x16x32_bf16 a[248:251], a[44:47], v[48:51], a[248:251]\n"
    "v_exp_f32 v28, v28\n"
    "v_mfma_f32_16x16x32_bf16 v[200:203], v[208:211], v[48:51], v[200:203]\n"
    "v_exp_f32 v29, v29\n"
    "v_mfma_f32_16x16x32_bf16 v[76:79], a[0:3], v[56:59], v[76:79]\n"
    "v_fma_f32 v30, v30, s36, v215\n"
    "v_fma_f32 v31, v31, s36, v215\n"
    "v_mfma_f32_16x16x32_bf16 v[92:95], a[4:7], v[56:59], v[92:95]\n"
    "v_exp_f32 v30, v30\n"
    "v_mfma_f32_16x16x32_bf16 v[108:111], a[8:11], v[56:59], v[108:111]\n"
    "v_exp_f32 v31, v31\n"
    "v_mfma_f32_16x16x32_bf16 v[124:127], a[12:15], v[56:59], v[124:127]\n"
    "v_cvt_pk_bf16_f32 v24, v24, v25\n"
    "v_cvt_pk_bf16_f32 v25, v26, v27\n"
    "v_mfma_f32_16x16x32_bf16 v[140:143], a[16:19], v[56:59], v[140:143]\n"
    "v_cvt_pk_bf16_f32 v26, v28, v29\n"
    "v_cvt_pk_bf16_f32 v27, v30, v31\n"
    "v_mfma_f32_16x16x32_bf16 v[156:159], a[20:23], v[56:59], v[156:159]\n"
    "v_mfma_f32_16x16x32_bf16 v[172:175], a[24:27], v[56:59], v[172:175]\n"
    "v_mfma_f32_16x16x32_bf16 v[188:191], a[28:31], v[56:59], v[188:191]\n"
    "v_mfma_f32_16x16x32_bf16 a[204:207], a[32:35], v[56:59], a[204:207]\n"
    "v_mfma_f32_16x16x32_bf16 a[220:223], a[36:39], v[56:59], a[220:223]\n"
    "v_mfma_f32_16x16x32_bf16 a[236:239], a[40:43], v[56:59], a[236:239]\n"
    "v_mfma_f32_16x16x32_bf16 a[252:255], a[44:47], v[56:59], a[252:255]\n"
    "v_mfma_f32_16x16x32_bf16 v[204:207], v[208:211], v[56:59], v[204:207]\n"
    "s_waitcnt vmcnt(14) lgkmcnt(0)\n"
    "s_barrier\n"
    "L_ph58:\n"
    "v_mfma_f32_16x16x32_bf16 v[32:35], a[48:51], a[96:99], 0\n"
    "buffer_load_dwordx4 v231, s[24:27], s42 offen nt lds\n"
    "v_mfma_f32_16x16x32_bf16 v[32:35], a[52:55], a[100:103], v[32:35]\n"
    "ds_read_b64_tr_b16 a[0:1], v218 offset:36864\n"
    "v_mfma_f32_16x16x32_bf16 v[32:35], a[56:59], a[104:107], v[32:35]\n"
    "buffer_load_dwordx4 v232, s[24:27], s42 offen offset:1024 nt lds\n"
    "v_mfma_f32_16x16x32_bf16 v[32:35], a[60:63], a[108:111], v[32:35]\n"
    "ds_read_b64_tr_b16 a[2:3], v218 offset:43008\n"
    "v_mfma_f32_16x16x32_bf16 v[32:35], a[64:67], a[112:115], v[32:35]\n"
    "buffer_load_dwordx4 v233, s[24:27], s42 offen offset:2048 nt lds\n"
    "v_mfma_f32_16x16x32_bf16 v[32:35], a[68:71], a[116:119], v[32:35]\n"
    "ds_read_b64_tr_b16 a[4:5], v219 offset:36864\n"
    "s_add_u32 s43, s40, 21504\n"
    "v_mfma_f32_16x16x32_bf16 v[36:39], a[72:75], a[96:99], 0\n"
    "buffer_store_dwordx4 v[250:253], v236, s[28:31], s43 offen sc1\n"
    "v_mfma_f32_16x16x32_bf16 v[36:39], a[76:79], a[100:103], v[36:39]\n"
    "ds_read_b64_tr_b16 a[6:7], v219 offset:43008\n"
    "s_add_u32 m0, s37, 36864\n"
    "v_mfma_f32_16x16x32_bf16 v[36:39], a[80:83], a[104:107], v[36:39]\n"
    "ds_read_b64_tr_b16 a[8:9], v220 offset:36864\n"
    "s_add_u32 s42, s39, 184320\n"
    "v_mfma_f32_16x16x32_bf16 v[36:39], a[84:87], a[108:111], v[36:39]\n"
    "ds_read_b64_tr_b16 a[10:11], v220 offset:43008\n"
    "v_mfma_f32_16x16x32_bf16 v[36:39], a[88:91], a[112:115], v[36:39]\n"
    "ds_read_b64_tr_b16 a[12:13], v221 offset:36864\n"
    "v_mfma_f32_16x16x32_bf16 v[36:39], a[92:95], a[116:119], v[36:39]\n"
    "ds_read_b64_tr_b16 a[14:15], v221 offset:43008\n"
    "v_mfma_f32_16x16x32_bf16 v[40:43], a[48:51], a[120:123], 0\n"
    "ds_read_b64_tr_b16 a[16:17], v218 offset:36992\n"
    "v_mfma_f32_16x16x32_bf16 v[40:43], a[52:55], a[124:127], v[40:43]\n"
    "ds_read_b64_tr_b16 a[18:19], v218 offset:43136\n"
    "v_fma_f32 v32, v32, s36, v212\n"
    "v_mfma_f32_16x16x32_bf16 v[40:43], a[56:59], a[128:131], v[40:43]\n"
    "ds_read_b64_tr_b16 a[20:21], v219 offset:36992\n"
    "v_fma_f32 v33, v33, s36, v212\n"
    "v_mfma_f32_16x16x32_bf16 v[40:43], a[60:63], a[132:135], v[40:43]\n"
    "ds_read_b64_tr_b16 a[22:23], v219 offset:43136\n"
    "v_fma_f32 v34, v34, s36, v212\n"
    "v_mfma_f32_16x16x32_bf16 v[40:43], a[64:67], a[136:139], v[40:43]\n"
    "ds_read_b64_tr_b16 a[24:25], v220 offset:36992\n"
    "v_fma_f32 v35, v35, s36, v212\n"
    "v_mfma_f32_16x16x32_bf16 v[40:43], a[68:71], a[140:143], v[40:43]\n"
    "ds_read_b64_tr_b16 a[26:27], v220 offset:43136\n"
    "v_mfma_f32_16x16x32_bf16 v[44:47], a[72:75], a[120:123], 0\n"
    "ds_read_b64_tr_b16 a[28:29], v221 offset:36992\n"
    "v_mfma_f32_16x16x32_bf16 v[44:47], a[76:79], a[124:127], v[44:47]\n"
    "ds_read_b64_tr_b16 a[30:31], v221 offset:43136\n"
    "v_fma_f32 v36, v36, s36, v212\n"
    "v_mfma_f32_16x16x32_bf16 v[44:47], a[80:83], a[128:131], v[44:47]\n"
    "ds_read_b64_tr_b16 a[32:33], v218 offset:37120\n"
    "v_fma_f32 v37, v37, s36, v212\n"
    "v_mfma_f32_16x16x32_bf16 v[44:47], a[84:87], a[132:135], v[44:47]\n"
    "ds_read_b64_tr_b16 a[34:35], v218 offset:43264\n"
    "v_fma_f32 v38, v38, s36, v212\n"
    "v_mfma_f32_16x16x32_bf16 v[44:47], a[88:91], a[136:139], v[44:47]\n"
    "ds_read_b64_tr_b16 a[36:37], v219 offset:37120\n"
    "v_fma_f32 v39, v39, s36, v212\n"
    "v_mfma_f32_16x16x32_bf16 v[44:47], a[92:95], a[140:143], v[44:47]\n"
    "ds_read_b64_tr_b16 a[38:39], v219 offset:43264\n"
    "v_mfma_f32_16x16x32_bf16 v[48:51], a[48:51], a[144:147], 0\n"
    "ds_read_b64_tr_b16 a[40:41], v220 offset:37120\n"
    "v_mfma_f32_16x16x32_bf16 v[48:51], a[52:55], a[148:151], v[48:51]\n"
    "ds_read_b64_tr_b16 a[42:43], v220 offset:43264\n"
    "v_fma_f32 v40, v40, s36, v213\n"
    "v_mfma_f32_16x16x32_bf16 v[48:51], a[56:59], a[152:155], v[48:51]\n"
    "ds_read_b64_tr_b16 a[44:45], v221 offset:37120\n"
    "v_fma_f32 v41, v41, s36, v213\n"
    "v_mfma_f32_16x16x32_bf16 v[48:51], a[60:63], a[156:159], v[48:51]\n"
    "ds_read_b64_tr_b16 a[46:47], v221 offset:43264\n"
    "v_fma_f32 v42, v42, s36, v213\n"
    "v_mfma_f32_16x16x32_bf16 v[48:51], a[64:67], a[160:163], v[48:51]\n"
    "ds_read_b128 v[250:253], v245 offset:22528\n"
    "v_fma_f32 v43, v43, s36, v213\n"
    "v_mfma_f32_16x16x32_bf16 v[48:51], a[68:71], a[164:167], v[48:51]\n"
    "v_exp_f32 v32, v32\n"
    "v_mfma_f32_16x16x32_bf16 v[52:55], a[72:75], a[144:147], 0\n"
    "v_exp_f32 v33, v33\n"
    "v_mfma_f32_16x16x32_bf16 v[52:55], a[76:79], a[148:151], v[52:55]\n"
    "v_exp_f32 v34, v34\n"
    "v_mfma_f32_16x16x32_bf16 v[52:55], a[80:83], a[152:155], v[52:55]\n"
    "v_exp_f32 v35, v35\n"
    "v_mfma_f32_16x16x32_bf16 v[52:55], a[84:87], a[156:159], v[52:55]\n"
    "v_exp_f32 v36, v36\n"
    "v_mfma_f32_16x16x32_bf16 v[52:55], a[88:91], a[160:163], v[52:55]\n"
    "v_exp_f32 v37, v37\n"
    "v_mfma_f32_16x16x32_bf16 v[52:55], a[92:95], a[164:167], v[52:55]\n"
    "v_exp_f32 v38, v38\n"
    "v_mfma_f32_16x16x32_bf16 v[56:59], a[48:51], a[168:171], 0\n"
    "v_exp_f32 v39, v39\n"
    "v_mfma_f32_16x16x32_bf16 v[56:59], a[52:55], a[172:175], v[56:59]\n"
    "v_cvt_pk_bf16_f32 v32, v32, v33\n"
    "v_cvt_pk_bf16_f32 v33, v34, v35\n"
    "v_mfma_f32_16x16x32_bf16 v[56:59], a[56:59], a[176:179], v[56:59]\n"
    "v_cvt_pk_bf16_f32 v34, v36, v37\n"
    "v_cvt_pk_bf16_f32 v35, v38, v39\n"
    "v_mfma_f32_16x16x32_bf16 v[56:59], a[60:63], a[180:183], v[56:59]\n"
    "v_exp_f32 v40, v40\n"
    "v_mfma_f32_16x16x32_bf16 v[56:59], a[64:67], a[184:187], v[56:59]\n"
    "v_exp_f32 v41, v41\n"
    "v_mfma_f32_16x16x32_bf16 v[56:59], a[68:71], a[188:191], v[56:59]\n"
    "v_exp_f32 v42, v42\n"
    "v_mfma_f32_16x16x32_bf16 v[60:63], a[72:75], a[168:171], 0\n"
    "v_exp_f32 v43, v43\n"
    "v_mfma_f32_16x16x32_bf16 v[60:63], a[76:79], a[172:175], v[60:63]\n"
    "v_fma_f32 v44, v44, s36, v213\n"
    "v_fma_f32 v45, v45, s36, v213\n"
    "v_mfma_f32_16x16x32_bf16 v[60:63], a[80:83], a[176:179], v[60:63]\n"
    "v_exp_f32 v44, v44\n"
    "v_mfma_f32_16x16x32_bf16 v[60:63], a[84:87], a[180:183], v[60:63]\n"
    "v_exp_f32 v45, v45\n"
    "v_mfma_f32_16x16x32_bf16 v[60:63], a[88:91], a[184:187], v[60:63]\n"
    "v_fma_f32 v46, v46, s36, v213\n"
    "v_fma_f32 v47, v47, s36, v213\n"
    "v_mfma_f32_16x16x32_bf16 v[60:63], a[92:95], a[188:191], v[60:63]\n"
    "s_waitcnt vmcnt(14) lgkmcnt(0)\n"
    "s_barrier\n"
    "L_ph59:\n"
    "v_mfma_f32_16x16x32_bf16 v[64:67], a[0:3], v[0:3], v[64:67]\n"
    "buffer_load_dwordx4 v231, s[24:27], s42 offen nt lds\n"
    "v_mfma_f32_16x16x32_bf16 v[80:83], a[4:7], v[0:3], v[80:83]\n"
    "ds_read_b128 a[48:51], v216 offset:49152\n"
    "v_cvt_pk_bf16_f32 v40, v40, v41\n"
    "v_mfma_f32_16x16x32_bf16 v[96:99], a[8:11], v[0:3], v[96:99]\n"
    "buffer_load_dwordx4 v232, s[24:27], s42 offen offset:1024 nt lds\n"
    "v_mfma_f32_16x16x32_bf16 v[112:115], a[12:15], v[0:3], v[112:115]\n"
    "ds_read_b128 a[52:55], v217 offset:49152\n"
    "v_cvt_pk_bf16_f32 v41, v42, v43\n"
    "v_mfma_f32_16x16x32_bf16 v[128:131], a[16:19], v[0:3], v[128:131]\n"
    "buffer_load_dwordx4 v233, s[24:27], s42 offen offset:2048 nt lds\n"
    "v_mfma_f32_16x16x32_bf16 v[144:147], a[20:23], v[0:3], v[144:147]\n"
    "ds_read_b128 a[56:59], v216 offset:49280\n"
    "s_add_u32 s43, s40, 21504\n"
    "v_mfma_f32_16x16x32_bf16 v[160:163], a[24:27], v[0:3], v[160:163]\n"
    "buffer_store_dwordx4 v[250:253], v236, s[28:31], s43 offen offset:1024 sc1\n"
    "v_mfma_f32_16x16x32_bf16 v[176:179], a[28:31], v[0:3], v[176:179]\n"
    "s_mov_b32 m0, 159744\n"
    "s_mov_b32 s51, L_ph63-L_code_start\n"
    "v_mfma_f32_16x16x32_bf16 a[192:195], a[32:35], v[0:3], a[192:195]\n"
    "buffer_load_dword v249, s[52:55], s51 offen lds\n"
    "v_mfma_f32_16x16x32_bf16 a[208:211], a[36:39], v[0:3], a[208:211]\n"
    "ds_read_b128 a[60:63], v217 offset:49280\n"
    "v_cvt_pk_bf16_f32 v42, v44, v45\n"
    "v_mfma_f32_16x16x32_bf16 a[224:227], a[40:43], v[0:3], a[224:227]\n"
    "ds_read_b128 a[64:67], v216 offset:49408\n"
    "v_fma_f32 v48, v48, s36, v214\n"
    "v_mfma_f32_16x16x32_bf16 a[240:243], a[44:47], v[0:3], a[240:243]\n"
    "ds_read_b128 a[68:71], v217 offset:49408\n"
    "v_fma_f32 v49, v49, s36, v214\n"
    "v_mfma_f32_16x16x32_bf16 v[192:195], v[208:211], v[0:3], v[192:195]\n"
    "ds_read_b128 a[72:75], v216 offset:55296\n"
    "v_fma_f32 v50, v50, s36, v214\n"
    "v_mfma_f32_16x16x32_bf16 v[68:71], a[0:3], v[8:11], v[68:71]\n"
    "ds_read_b128 a[76:79], v217 offset:55296\n"
    "v_fma_f32 v51, v51, s36, v214\n"
    "v_mfma_f32_16x16x32_bf16 v[84:87], a[4:7], v[8:11], v[84:87]\n"
    "ds_read_b128 a[80:83], v216 offset:55424\n"
    "v_fma_f32 v52, v52, s36, v214\n"
    "v_mfma_f32_16x16x32_bf16 v[100:103], a[8:11], v[8:11], v[100:103]\n"
    "ds_read_b128 a[84:87], v217 offset:55424\n"
    "v_fma_f32 v53, v53, s36, v214\n"
    "v_mfma_f32_16x16x32_bf16 v[116:119], a[12:15], v[8:11], v[116:119]\n"
    "ds_read_b128 a[88:91], v216 offset:55552\n"
    "v_fma_f32 v54, v54, s36, v214\n"
    "v_mfma_f32_16x16x32_bf16 v[132:135], a[16:19], v[8:11], v[132:135]\n"
    "ds_read_b128 a[92:95], v217 offset:55552\n"
    "v_fma_f32 v55, v55, s36, v214\n"
    "v_mfma_f32_16x16x32_bf16 v[148:151], a[20:23], v[8:11], v[148:151]\n"
    "ds_read_b128 v[250:253], v246 offset:23552\n"
    "v_fma_f32 v56, v56, s36, v215\n"
    "v_mfma_f32_16x16x32_bf16 v[164:167], a[24:27], v[8:11], v[164:167]\n"
    "v_exp_f32 v46, v46\n"
    "v_mfma_f32_16x16x32_bf16 v[180:183], a[28:31], v[8:11], v[180:183]\n"
    "v_exp_f32 v47, v47\n"
    "v_mfma_f32_16x16x32_bf16 a[196:199], a[32:35], v[8:11], a[196:199]\n"
    "v_cvt_pk_bf16_f32 v43, v46, v47\n"
    "v_fma_f32 v57, v57, s36, v215\n"
    "v_mfma_f32_16x16x32_bf16 a[212:215], a[36:39], v[8:11], a[212:215]\n"
    "v_exp_f32 v48, v48\n"
    "v_mfma_f32_16x16x32_bf16 a[228:231], a[40:43], v[8:11], a[228:231]\n"
    "v_exp_f32 v49, v49\n"
    "v_mfma_f32_16x16x32_bf16 a[244:247], a[44:47], v[8:11], a[244:247]\n"
    "v_exp_f32 v50, v50\n"
    "v_mfma_f32_16x16x32_bf16 v[196:199], v[208:211], v[8:11], v[196:199]\n"
    "v_exp_f32 v51, v51\n"
    "v_mfma_f32_16x16x32_bf16 v[72:75], a[0:3], v[16:19], v[72:75]\n"
    "v_exp_f32 v52, v52\n"
    "v_mfma_f32_16x16x32_bf16 v[88:91], a[4:7], v[16:19], v[88:91]\n"
    "v_exp_f32 v53, v53\n"
    "v_mfma_f32_16x16x32_bf16 v[104:107], a[8:11], v[16:19], v[104:107]\n"
    "v_exp_f32 v54, v54\n"
    "v_mfma_f32_16x16x32_bf16 v[120:123], a[12:15], v[16:19], v[120:123]\n"
    "v_exp_f32 v55, v55\n"
    "v_mfma_f32_16x16x32_bf16 v[136:139], a[16:19], v[16:19], v[136:139]\n"
    "v_cvt_pk_bf16_f32 v48, v48, v49\n"
    "v_cvt_pk_bf16_f32 v49, v50, v51\n"
    "v_mfma_f32_16x16x32_bf16 v[152:155], a[20:23], v[16:19], v[152:155]\n"
    "v_cvt_pk_bf16_f32 v50, v52, v53\n"
    "v_cvt_pk_bf16_f32 v51, v54, v55\n"
    "v_mfma_f32_16x16x32_bf16 v[168:171], a[24:27], v[16:19], v[168:171]\n"
    "v_exp_f32 v56, v56\n"
    "v_mfma_f32_16x16x32_bf16 v[184:187], a[28:31], v[16:19], v[184:187]\n"
    "v_exp_f32 v57, v57\n"
    "v_mfma_f32_16x16x32_bf16 a[200:203], a[32:35], v[16:19], a[200:203]\n"
    "v_fma_f32 v58, v58, s36, v215\n"
    "v_fma_f32 v59, v59, s36, v215\n"
    "v_mfma_f32_16x16x32_bf16 a[216:219], a[36:39], v[16:19], a[216:219]\n"
    "v_exp_f32 v58, v58\n"
    "v_mfma_f32_16x16x32_bf16 a[232:235], a[40:43], v[16:19], a[232:235]\n"
    "v_exp_f32 v59, v59\n"
    "v_mfma_f32_16x16x32_bf16 a[248:251], a[44:47], v[16:19], a[248:251]\n"
    "v_fma_f32 v60, v60, s36, v215\n"
    "v_fma_f32 v61, v61, s36, v215\n"
    "v_mfma_f32_16x16x32_bf16 v[200:203], v[208:211], v[16:19], v[200:203]\n"
    "v_exp_f32 v60, v60\n"
    "v_mfma_f32_16x16x32_bf16 v[76:79], a[0:3], v[24:27], v[76:79]\n"
    "v_exp_f32 v61, v61\n"
    "v_mfma_f32_16x16x32_bf16 v[92:95], a[4:7], v[24:27], v[92:95]\n"
    "v_fma_f32 v62, v62, s36, v215\n"
    "v_fma_f32 v63, v63, s36, v215\n"
    "v_mfma_f32_16x16x32_bf16 v[108:111], a[8:11], v[24:27], v[108:111]\n"
    "v_exp_f32 v62, v62\n"
    "v_mfma_f32_16x16x32_bf16 v[124:127], a[12:15], v[24:27], v[124:127]\n"
    "v_exp_f32 v63, v63\n"
    "v_mfma_f32_16x16x32_bf16 v[140:143], a[16:19], v[24:27], v[140:143]\n"
    "v_cvt_pk_bf16_f32 v56, v56, v57\n"
    "v_cvt_pk_bf16_f32 v57, v58, v59\n"
    "v_mfma_f32_16x16x32_bf16 v[156:159], a[20:23], v[24:27], v[156:159]\n"
    "v_cvt_pk_bf16_f32 v58, v60, v61\n"
    "v_cvt_pk_bf16_f32 v59, v62, v63\n"
    "v_mfma_f32_16x16x32_bf16 v[172:175], a[24:27], v[24:27], v[172:175]\n"
    "v_mfma_f32_16x16x32_bf16 v[188:191], a[28:31], v[24:27], v[188:191]\n"
    "v_mfma_f32_16x16x32_bf16 a[204:207], a[32:35], v[24:27], a[204:207]\n"
    "v_mfma_f32_16x16x32_bf16 a[220:223], a[36:39], v[24:27], a[220:223]\n"
    "v_mfma_f32_16x16x32_bf16 a[236:239], a[40:43], v[24:27], a[236:239]\n"
    "v_mfma_f32_16x16x32_bf16 a[252:255], a[44:47], v[24:27], a[252:255]\n"
    "v_mfma_f32_16x16x32_bf16 v[204:207], v[208:211], v[24:27], v[204:207]\n"
    "s_waitcnt vmcnt(14) lgkmcnt(0)\n"
    "s_barrier\n"
    "L_ph60:\n"
    "v_mfma_f32_16x16x32_bf16 v[0:3], a[48:51], a[96:99], 0\n"
    "ds_read_b64_tr_b16 a[0:1], v218 offset:0\n"
    "s_add_u32 s43, s40, 21504\n"
    "v_mfma_f32_16x16x32_bf16 v[0:3], a[52:55], a[100:103], v[0:3]\n"
    "buffer_store_dwordx4 v[250:253], v236, s[28:31], s43 offen offset:2048 sc1\n"
    "v_mfma_f32_16x16x32_bf16 v[0:3], a[56:59], a[104:107], v[0:3]\n"
    "ds_read_b64_tr_b16 a[2:3], v218 offset:6144\n"
    "ds_read_b64_tr_b16 a[40:41], v220 offset:256\n"
    "v_mfma_f32_16x16x32_bf16 v[0:3], a[60:63], a[108:111], v[0:3]\n"
    "ds_read_b64_tr_b16 a[4:5], v219 offset:0\n"
    "v_mfma_f32_16x16x32_bf16 v[0:3], a[64:67], a[112:115], v[0:3]\n"
    "ds_read_b64_tr_b16 a[6:7], v219 offset:6144\n"
    "ds_read_b64_tr_b16 a[42:43], v220 offset:6400\n"
    "v_mfma_f32_16x16x32_bf16 v[0:3], a[68:71], a[116:119], v[0:3]\n"
    "ds_read_b64_tr_b16 a[8:9], v220 offset:0\n"
    "v_mfma_f32_16x16x32_bf16 v[4:7], a[72:75], a[96:99], 0\n"
    "ds_read_b64_tr_b16 a[10:11], v220 offset:6144\n"
    "ds_read_b64_tr_b16 a[44:45], v221 offset:256\n"
    "v_mfma_f32_16x16x32_bf16 v[4:7], a[76:79], a[100:103], v[4:7]\n"
    "ds_read_b64_tr_b16 a[12:13], v221 offset:0\n"
    "v_mfma_f32_16x16x32_bf16 v[4:7], a[80:83], a[104:107], v[4:7]\n"
    "ds_read_b64_tr_b16 a[14:15], v221 offset:6144\n"
    "ds_read_b64_tr_b16 a[46:47], v221 offset:6400\n"
    "v_mfma_f32_16x16x32_bf16 v[4:7], a[84:87], a[108:111], v[4:7]\n"
    "ds_read_b64_tr_b16 a[16:17], v218 offset:128\n"
    "v_mfma_f32_16x16x32_bf16 v[4:7], a[88:91], a[112:115], v[4:7]\n"
    "ds_read_b64_tr_b16 a[18:19], v218 offset:6272\n"
    "v_mfma_f32_16x16x32_bf16 v[4:7], a[92:95], a[116:119], v[4:7]\n"
    "ds_read_b64_tr_b16 a[20:21], v219 offset:128\n"
    "v_mfma_f32_16x16x32_bf16 v[8:11], a[48:51], a[120:123], 0\n"
    "ds_read_b64_tr_b16 a[22:23], v219 offset:6272\n"
    "v_mfma_f32_16x16x32_bf16 v[8:11], a[52:55], a[124:127], v[8:11]\n"
    "ds_read_b64_tr_b16 a[24:25], v220 offset:128\n"
    "v_fma_f32 v0, v0, s36, v212\n"
    "v_mfma_f32_16x16x32_bf16 v[8:11], a[56:59], a[128:131], v[8:11]\n"
    "ds_read_b64_tr_b16 a[26:27], v220 offset:6272\n"
    "v_exp_f32 v0, v0\n"
    "v_mfma_f32_16x16x32_bf16 v[8:11], a[60:63], a[132:135], v[8:11]\n"
    "ds_read_b64_tr_b16 a[28:29], v221 offset:128\n"
    "v_fma_f32 v1, v1, s36, v212\n"
    "v_mfma_f32_16x16x32_bf16 v[8:11], a[64:67], a[136:139], v[8:11]\n"
    "ds_read_b64_tr_b16 a[30:31], v221 offset:6272\n"
    "v_exp_f32 v1, v1\n"
    "v_mfma_f32_16x16x32_bf16 v[8:11], a[68:71], a[140:143], v[8:11]\n"
    "ds_read_b64_tr_b16 a[32:33], v218 offset:256\n"
    "v_fma_f32 v2, v2, s36, v212\n"
    "v_mfma_f32_16x16x32_bf16 v[12:15], a[72:75], a[120:123], 0\n"
    "ds_read_b64_tr_b16 a[34:35], v218 offset:6400\n"
    "v_exp_f32 v2, v2\n"
    "v_mfma_f32_16x16x32_bf16 v[12:15], a[76:79], a[124:127], v[12:15]\n"
    "ds_read_b64_tr_b16 a[36:37], v219 offset:256\n"
    "v_fma_f32 v3, v3, s36, v212\n"
    "v_mfma_f32_16x16x32_bf16 v[12:15], a[80:83], a[128:131], v[12:15]\n"
    "ds_read_b64_tr_b16 a[38:39], v219 offset:6400\n"
    "v_fma_f32 v4, v4, s36, v212\n"
    "v_mfma_f32_16x16x32_bf16 v[12:15], a[84:87], a[132:135], v[12:15]\n"
    "v_exp_f32 v3, v3\n"
    "v_mfma_f32_16x16x32_bf16 v[12:15], a[88:91], a[136:139], v[12:15]\n"
    "v_exp_f32 v4, v4\n"
    "v_mfma_f32_16x16x32_bf16 v[12:15], a[92:95], a[140:143], v[12:15]\n"
    "s_waitcnt vmcnt(11) lgkmcnt(0)\n"
    "s_barrier\n"
    "L_ph61:\n"
    "v_mfma_f32_16x16x32_bf16 v[64:67], a[0:3], v[32:35], v[64:67]\n"
    "ds_read_b128 a[48:51], v216 offset:12288\n"
    "v_fma_f32 v5, v5, s36, v212\n"
    "v_mfma_f32_16x16x32_bf16 v[80:83], a[4:7], v[32:35], v[80:83]\n"
    "ds_read_b128 a[52:55], v217 offset:12288\n"
    "v_exp_f32 v5, v5\n"
    "v_mfma_f32_16x16x32_bf16 v[96:99], a[8:11], v[32:35], v[96:99]\n"
    "ds_read_b128 a[56:59], v216 offset:12416\n"
    "v_fma_f32 v6, v6, s36, v212\n"
    "v_mfma_f32_16x16x32_bf16 v[112:115], a[12:15], v[32:35], v[112:115]\n"
    "ds_read_b128 a[60:63], v217 offset:12416\n"
    "v_exp_f32 v6, v6\n"
    "v_mfma_f32_16x16x32_bf16 v[128:131], a[16:19], v[32:35], v[128:131]\n"
    "ds_read_b128 a[64:67], v216 offset:12544\n"
    "v_fma_f32 v7, v7, s36, v212\n"
    "v_mfma_f32_16x16x32_bf16 v[144:147], a[20:23], v[32:35], v[144:147]\n"
    "ds_read_b128 a[68:71], v217 offset:12544\n"
    "v_exp_f32 v7, v7\n"
    "v_mfma_f32_16x16x32_bf16 v[160:163], a[24:27], v[32:35], v[160:163]\n"
    "ds_read_b128 a[72:75], v216 offset:18432\n"
    "v_cvt_pk_bf16_f32 v0, v0, v1\n"
    "v_mfma_f32_16x16x32_bf16 v[176:179], a[28:31], v[32:35], v[176:179]\n"
    "ds_read_b128 a[76:79], v217 offset:18432\n"
    "v_cvt_pk_bf16_f32 v1, v2, v3\n"
    "v_mfma_f32_16x16x32_bf16 a[192:195], a[32:35], v[32:35], a[192:195]\n"
    "ds_read_b128 a[80:83], v216 offset:18560\n"
    "v_cvt_pk_bf16_f32 v2, v4, v5\n"
    "v_mfma_f32_16x16x32_bf16 a[208:211], a[36:39], v[32:35], a[208:211]\n"
    "ds_read_b128 a[84:87], v217 offset:18560\n"
    "v_cvt_pk_bf16_f32 v3, v6, v7\n"
    "v_mfma_f32_16x16x32_bf16 a[224:227], a[40:43], v[32:35], a[224:227]\n"
    "ds_read_b128 a[88:91], v216 offset:18688\n"
    "v_fma_f32 v8, v8, s36, v213\n"
    "v_mfma_f32_16x16x32_bf16 a[240:243], a[44:47], v[32:35], a[240:243]\n"
    "ds_read_b128 a[92:95], v217 offset:18688\n"
    "v_fma_f32 v9, v9, s36, v213\n"
    "v_mfma_f32_16x16x32_bf16 v[192:195], v[208:211], v[32:35], v[192:195]\n"
    "v_exp_f32 v8, v8\n"
    "v_mfma_f32_16x16x32_bf16 v[68:71], a[0:3], v[40:43], v[68:71]\n"
    "v_exp_f32 v9, v9\n"
    "v_mfma_f32_16x16x32_bf16 v[84:87], a[4:7], v[40:43], v[84:87]\n"
    "v_fma_f32 v10, v10, s36, v213\n"
    "v_fma_f32 v11, v11, s36, v213\n"
    "v_mfma_f32_16x16x32_bf16 v[100:103], a[8:11], v[40:43], v[100:103]\n"
    "v_exp_f32 v10, v10\n"
    "v_mfma_f32_16x16x32_bf16 v[116:119], a[12:15], v[40:43], v[116:119]\n"
    "v_exp_f32 v11, v11\n"
    "v_mfma_f32_16x16x32_bf16 v[132:135], a[16:19], v[40:43], v[132:135]\n"
    "v_fma_f32 v12, v12, s36, v213\n"
    "v_fma_f32 v13, v13, s36, v213\n"
    "v_mfma_f32_16x16x32_bf16 v[148:151], a[20:23], v[40:43], v[148:151]\n"
    "v_exp_f32 v12, v12\n"
    "v_mfma_f32_16x16x32_bf16 v[164:167], a[24:27], v[40:43], v[164:167]\n"
    "v_exp_f32 v13, v13\n"
    "v_mfma_f32_16x16x32_bf16 v[180:183], a[28:31], v[40:43], v[180:183]\n"
    "v_fma_f32 v14, v14, s36, v213\n"
    "v_fma_f32 v15, v15, s36, v213\n"
    "v_mfma_f32_16x16x32_bf16 a[196:199], a[32:35], v[40:43], a[196:199]\n"
    "v_exp_f32 v14, v14\n"
    "v_mfma_f32_16x16x32_bf16 a[212:215], a[36:39], v[40:43], a[212:215]\n"
    "v_exp_f32 v15, v15\n"
    "v_mfma_f32_16x16x32_bf16 a[228:231], a[40:43], v[40:43], a[228:231]\n"
    "v_cvt_pk_bf16_f32 v8, v8, v9\n"
    "v_cvt_pk_bf16_f32 v9, v10, v11\n"
    "v_mfma_f32_16x16x32_bf16 a[244:247], a[44:47], v[40:43], a[244:247]\n"
    "v_cvt_pk_bf16_f32 v10, v12, v13\n"
    "v_cvt_pk_bf16_f32 v11, v14, v15\n"
    "v_mfma_f32_16x16x32_bf16 v[196:199], v[208:211], v[40:43], v[196:199]\n"
    "s_waitcnt vmcnt(7) lgkmcnt(0)\n"
    "s_barrier\n"
    "L_ph62:\n"
    "v_mfma_f32_16x16x32_bf16 v[32:35], a[48:51], a[96:99], 0\n"
    "ds_read_b64_tr_b16 a[0:1], v218 offset:24576\n"
    "ds_read_b64_tr_b16 a[42:43], v220 offset:30976\n"
    "v_mfma_f32_16x16x32_bf16 v[32:35], a[52:55], a[100:103], v[32:35]\n"
    "ds_read_b64_tr_b16 a[2:3], v218 offset:30720\n"
    "v_mfma_f32_16x16x32_bf16 v[32:35], a[56:59], a[104:107], v[32:35]\n"
    "ds_read_b64_tr_b16 a[4:5], v219 offset:24576\n"
    "ds_read_b64_tr_b16 a[44:45], v221 offset:24832\n"
    "v_mfma_f32_16x16x32_bf16 v[32:35], a[60:63], a[108:111], v[32:35]\n"
    "ds_read_b64_tr_b16 a[6:7], v219 offset:30720\n"
    "v_mfma_f32_16x16x32_bf16 v[32:35], a[64:67], a[112:115], v[32:35]\n"
    "ds_read_b64_tr_b16 a[8:9], v220 offset:24576\n"
    "ds_read_b64_tr_b16 a[46:47], v221 offset:30976\n"
    "v_mfma_f32_16x16x32_bf16 v[32:35], a[68:71], a[116:119], v[32:35]\n"
    "ds_read_b64_tr_b16 a[10:11], v220 offset:30720\n"
    "v_mfma_f32_16x16x32_bf16 v[36:39], a[72:75], a[96:99], 0\n"
    "ds_read_b64_tr_b16 a[12:13], v221 offset:24576\n"
    "v_mfma_f32_16x16x32_bf16 v[36:39], a[76:79], a[100:103], v[36:39]\n"
    "ds_read_b64_tr_b16 a[14:15], v221 offset:30720\n"
    "v_mfma_f32_16x16x32_bf16 v[36:39], a[80:83], a[104:107], v[36:39]\n"
    "ds_read_b64_tr_b16 a[16:17], v218 offset:24704\n"
    "v_mfma_f32_16x16x32_bf16 v[36:39], a[84:87], a[108:111], v[36:39]\n"
    "ds_read_b64_tr_b16 a[18:19], v218 offset:30848\n"
    "v_mfma_f32_16x16x32_bf16 v[36:39], a[88:91], a[112:115], v[36:39]\n"
    "ds_read_b64_tr_b16 a[20:21], v219 offset:24704\n"
    "v_mfma_f32_16x16x32_bf16 v[36:39], a[92:95], a[116:119], v[36:39]\n"
    "ds_read_b64_tr_b16 a[22:23], v219 offset:30848\n"
    "v_mfma_f32_16x16x32_bf16 v[40:43], a[48:51], a[120:123], 0\n"
    "ds_read_b64_tr_b16 a[24:25], v220 offset:24704\n"
    "v_mfma_f32_16x16x32_bf16 v[40:43], a[52:55], a[124:127], v[40:43]\n"
    "ds_read_b64_tr_b16 a[26:27], v220 offset:30848\n"
    "v_fma_f32 v32, v32, s36, v212\n"
    "v_mfma_f32_16x16x32_bf16 v[40:43], a[56:59], a[128:131], v[40:43]\n"
    "ds_read_b64_tr_b16 a[28:29], v221 offset:24704\n"
    "v_exp_f32 v32, v32\n"
    "v_mfma_f32_16x16x32_bf16 v[40:43], a[60:63], a[132:135], v[40:43]\n"
    "ds_read_b64_tr_b16 a[30:31], v221 offset:30848\n"
    "v_fma_f32 v33, v33, s36, v212\n"
    "v_mfma_f32_16x16x32_bf16 v[40:43], a[64:67], a[136:139], v[40:43]\n"
    "ds_read_b64_tr_b16 a[32:33], v218 offset:24832\n"
    "v_exp_f32 v33, v33\n"
    "v_mfma_f32_16x16x32_bf16 v[40:43], a[68:71], a[140:143], v[40:43]\n"
    "ds_read_b64_tr_b16 a[34:35], v218 offset:30976\n"
    "v_fma_f32 v34, v34, s36, v212\n"
    "v_mfma_f32_16x16x32_bf16 v[44:47], a[72:75], a[120:123], 0\n"
    "ds_read_b64_tr_b16 a[36:37], v219 offset:24832\n"
    "v_exp_f32 v34, v34\n"
    "v_mfma_f32_16x16x32_bf16 v[44:47], a[76:79], a[124:127], v[44:47]\n"
    "ds_read_b64_tr_b16 a[38:39], v219 offset:30976\n"
    "v_fma_f32 v35, v35, s36, v212\n"
    "v_mfma_f32_16x16x32_bf16 v[44:47], a[80:83], a[128:131], v[44:47]\n"
    "ds_read_b64_tr_b16 a[40:41], v220 offset:24832\n"
    "v_fma_f32 v36, v36, s36, v212\n"
    "v_mfma_f32_16x16x32_bf16 v[44:47], a[84:87], a[132:135], v[44:47]\n"
    "v_exp_f32 v35, v35\n"
    "v_mfma_f32_16x16x32_bf16 v[44:47], a[88:91], a[136:139], v[44:47]\n"
    "v_exp_f32 v36, v36\n"
    "v_mfma_f32_16x16x32_bf16 v[44:47], a[92:95], a[140:143], v[44:47]\n"
    "s_waitcnt vmcnt(3) lgkmcnt(0)\n"
    "s_barrier\n"
    "L_ph63:\n"
    "v_mfma_f32_16x16x32_bf16 v[64:67], a[0:3], v[0:3], v[64:67]\n"
    "s_mov_b32 m0, 159744\n"
    "s_mov_b32 s51, L_ph67-L_code_start\n"
    "v_mfma_f32_16x16x32_bf16 v[80:83], a[4:7], v[0:3], v[80:83]\n"
    "buffer_load_dword v249, s[52:55], s51 offen lds\n"
    "v_mfma_f32_16x16x32_bf16 v[96:99], a[8:11], v[0:3], v[96:99]\n"
    "ds_read_b64_tr_b16 a[48:49], v218 offset:36864\n"
    "v_fma_f32 v37, v37, s36, v212\n"
    "v_mfma_f32_16x16x32_bf16 v[112:115], a[12:15], v[0:3], v[112:115]\n"
    "ds_read_b64_tr_b16 a[50:51], v218 offset:43008\n"
    "v_exp_f32 v37, v37\n"
    "v_mfma_f32_16x16x32_bf16 v[128:131], a[16:19], v[0:3], v[128:131]\n"
    "ds_read_b64_tr_b16 a[52:53], v219 offset:36864\n"
    "v_fma_f32 v38, v38, s36, v212\n"
    "v_mfma_f32_16x16x32_bf16 v[144:147], a[20:23], v[0:3], v[144:147]\n"
    "ds_read_b64_tr_b16 a[54:55], v219 offset:43008\n"
    "v_exp_f32 v38, v38\n"
    "v_mfma_f32_16x16x32_bf16 v[160:163], a[24:27], v[0:3], v[160:163]\n"
    "ds_read_b64_tr_b16 a[56:57], v220 offset:36864\n"
    "v_fma_f32 v39, v39, s36, v212\n"
    "v_mfma_f32_16x16x32_bf16 v[176:179], a[28:31], v[0:3], v[176:179]\n"
    "ds_read_b64_tr_b16 a[58:59], v220 offset:43008\n"
    "v_exp_f32 v39, v39\n"
    "v_mfma_f32_16x16x32_bf16 a[192:195], a[32:35], v[0:3], a[192:195]\n"
    "ds_read_b64_tr_b16 a[60:61], v221 offset:36864\n"
    "v_cvt_pk_bf16_f32 v32, v32, v33\n"
    "v_mfma_f32_16x16x32_bf16 a[208:211], a[36:39], v[0:3], a[208:211]\n"
    "ds_read_b64_tr_b16 a[62:63], v221 offset:43008\n"
    "v_cvt_pk_bf16_f32 v33, v34, v35\n"
    "v_mfma_f32_16x16x32_bf16 a[224:227], a[40:43], v[0:3], a[224:227]\n"
    "ds_read_b64_tr_b16 a[64:65], v218 offset:36992\n"
    "v_cvt_pk_bf16_f32 v34, v36, v37\n"
    "v_mfma_f32_16x16x32_bf16 a[240:243], a[44:47], v[0:3], a[240:243]\n"
    "ds_read_b64_tr_b16 a[66:67], v218 offset:43136\n"
    "v_cvt_pk_bf16_f32 v35, v38, v39\n"
    "v_mfma_f32_16x16x32_bf16 v[192:195], v[208:211], v[0:3], v[192:195]\n"
    "ds_read_b64_tr_b16 a[68:69], v219 offset:36992\n"
    "v_fma_f32 v40, v40, s36, v213\n"
    "v_mfma_f32_16x16x32_bf16 v[68:71], a[0:3], v[8:11], v[68:71]\n"
    "ds_read_b64_tr_b16 a[70:71], v219 offset:43136\n"
    "v_exp_f32 v40, v40\n"
    "v_mfma_f32_16x16x32_bf16 v[84:87], a[4:7], v[8:11], v[84:87]\n"
    "ds_read_b64_tr_b16 a[72:73], v220 offset:36992\n"
    "v_fma_f32 v41, v41, s36, v213\n"
    "v_mfma_f32_16x16x32_bf16 v[100:103], a[8:11], v[8:11], v[100:103]\n"
    "ds_read_b64_tr_b16 a[74:75], v220 offset:43136\n"
    "v_exp_f32 v41, v41\n"
    "v_mfma_f32_16x16x32_bf16 v[116:119], a[12:15], v[8:11], v[116:119]\n"
    "ds_read_b64_tr_b16 a[76:77], v221 offset:36992\n"
    "v_fma_f32 v42, v42, s36, v213\n"
    "v_mfma_f32_16x16x32_bf16 v[132:135], a[16:19], v[8:11], v[132:135]\n"
    "ds_read_b64_tr_b16 a[78:79], v221 offset:43136\n"
    "v_exp_f32 v42, v42\n"
    "v_mfma_f32_16x16x32_bf16 v[148:151], a[20:23], v[8:11], v[148:151]\n"
    "ds_read_b64_tr_b16 a[80:81], v218 offset:37120\n"
    "v_fma_f32 v43, v43, s36, v213\n"
    "v_mfma_f32_16x16x32_bf16 v[164:167], a[24:27], v[8:11], v[164:167]\n"
    "ds_read_b64_tr_b16 a[82:83], v218 offset:43264\n"
    "v_exp_f32 v43, v43\n"
    "v_mfma_f32_16x16x32_bf16 v[180:183], a[28:31], v[8:11], v[180:183]\n"
    "ds_read_b64_tr_b16 a[84:85], v219 offset:37120\n"
    "v_fma_f32 v44, v44, s36, v213\n"
    "v_mfma_f32_16x16x32_bf16 a[196:199], a[32:35], v[8:11], a[196:199]\n"
    "ds_read_b64_tr_b16 a[86:87], v219 offset:43264\n"
    "v_exp_f32 v44, v44\n"
    "v_mfma_f32_16x16x32_bf16 a[212:215], a[36:39], v[8:11], a[212:215]\n"
    "ds_read_b64_tr_b16 a[88:89], v220 offset:37120\n"
    "v_fma_f32 v45, v45, s36, v213\n"
    "v_mfma_f32_16x16x32_bf16 a[228:231], a[40:43], v[8:11], a[228:231]\n"
    "ds_read_b64_tr_b16 a[90:91], v220 offset:43264\n"
    "v_exp_f32 v45, v45\n"
    "v_mfma_f32_16x16x32_bf16 a[244:247], a[44:47], v[8:11], a[244:247]\n"
    "ds_read_b64_tr_b16 a[92:93], v221 offset:37120\n"
    "v_fma_f32 v46, v46, s36, v213\n"
    "v_mfma_f32_16x16x32_bf16 v[196:199], v[208:211], v[8:11], v[196:199]\n"
    "ds_read_b64_tr_b16 a[94:95], v221 offset:43264\n"
    "v_fma_f32 v47, v47, s36, v213\n"
    "v_exp_f32 v46, v46\n"
    "v_exp_f32 v47, v47\n"
    "v_cvt_pk_bf16_f32 v40, v40, v41\n"
    "v_cvt_pk_bf16_f32 v41, v42, v43\n"
    "v_cvt_pk_bf16_f32 v42, v44, v45\n"
    "v_cvt_pk_bf16_f32 v43, v46, v47\n"
    "s_waitcnt lgkmcnt(0)\n"
    "L_ph64:\n"
    "v_mfma_f32_16x16x32_bf16 v[64:67], a[48:51], v[32:35], v[64:67]\n"
    "ds_read_b128 a[0:3], v216 offset:49152\n"
    "v_mfma_f32_16x16x32_bf16 v[80:83], a[52:55], v[32:35], v[80:83]\n"
    "ds_read_b128 a[4:7], v217 offset:49152\n"
    "v_mfma_f32_16x16x32_bf16 v[96:99], a[56:59], v[32:35], v[96:99]\n"
    "ds_read_b128 a[8:11], v216 offset:49280\n"
    "v_mfma_f32_16x16x32_bf16 v[112:115], a[60:63], v[32:35], v[112:115]\n"
    "ds_read_b128 a[12:15], v217 offset:49280\n"
    "v_mfma_f32_16x16x32_bf16 v[128:131], a[64:67], v[32:35], v[128:131]\n"
    "ds_read_b128 a[16:19], v216 offset:49408\n"
    "v_mfma_f32_16x16x32_bf16 v[144:147], a[68:71], v[32:35], v[144:147]\n"
    "ds_read_b128 a[20:23], v217 offset:49408\n"
    "v_mfma_f32_16x16x32_bf16 v[160:163], a[72:75], v[32:35], v[160:163]\n"
    "ds_read_b128 a[24:27], v216 offset:55296\n"
    "v_mfma_f32_16x16x32_bf16 v[176:179], a[76:79], v[32:35], v[176:179]\n"
    "ds_read_b128 a[28:31], v217 offset:55296\n"
    "v_mfma_f32_16x16x32_bf16 a[192:195], a[80:83], v[32:35], a[192:195]\n"
    "ds_read_b128 a[32:35], v216 offset:55424\n"
    "v_mfma_f32_16x16x32_bf16 a[208:211], a[84:87], v[32:35], a[208:211]\n"
    "ds_read_b128 a[36:39], v217 offset:55424\n"
    "v_mfma_f32_16x16x32_bf16 a[224:227], a[88:91], v[32:35], a[224:227]\n"
    "ds_read_b128 a[40:43], v216 offset:55552\n"
    "v_mfma_f32_16x16x32_bf16 a[240:243], a[92:95], v[32:35], a[240:243]\n"
    "ds_read_b128 a[44:47], v217 offset:55552\n"
    "v_mfma_f32_16x16x32_bf16 v[192:195], v[208:211], v[32:35], v[192:195]\n"
    "v_mfma_f32_16x16x32_bf16 v[68:71], a[48:51], v[40:43], v[68:71]\n"
    "v_mfma_f32_16x16x32_bf16 v[84:87], a[52:55], v[40:43], v[84:87]\n"
    "v_mfma_f32_16x16x32_bf16 v[100:103], a[56:59], v[40:43], v[100:103]\n"
    "v_mfma_f32_16x16x32_bf16 v[116:119], a[60:63], v[40:43], v[116:119]\n"
    "v_mfma_f32_16x16x32_bf16 v[132:135], a[64:67], v[40:43], v[132:135]\n"
    "v_mfma_f32_16x16x32_bf16 v[148:151], a[68:71], v[40:43], v[148:151]\n"
    "v_mfma_f32_16x16x32_bf16 v[164:167], a[72:75], v[40:43], v[164:167]\n"
    "v_mfma_f32_16x16x32_bf16 v[180:183], a[76:79], v[40:43], v[180:183]\n"
    "v_rcp_f32 v248, v192\n"
    "v_mfma_f32_16x16x32_bf16 a[196:199], a[80:83], v[40:43], a[196:199]\n"
    "v_cmp_nge_f32 vcc, s48, v192\n"
    "v_accvgpr_read_b32 v0, a192\n"
    "v_mfma_f32_16x16x32_bf16 a[212:215], a[84:87], v[40:43], a[212:215]\n"
    "v_accvgpr_read_b32 v1, a193\n"
    "v_accvgpr_read_b32 v2, a194\n"
    "v_mfma_f32_16x16x32_bf16 a[228:231], a[88:91], v[40:43], a[228:231]\n"
    "v_accvgpr_read_b32 v3, a195\n"
    "v_accvgpr_read_b32 v4, a208\n"
    "v_mfma_f32_16x16x32_bf16 a[244:247], a[92:95], v[40:43], a[244:247]\n"
    "v_accvgpr_read_b32 v5, a209\n"
    "v_accvgpr_read_b32 v6, a210\n"
    "v_mfma_f32_16x16x32_bf16 v[196:199], v[208:211], v[40:43], v[196:199]\n"
    "s_waitcnt lgkmcnt(0)\n"
    "v_accvgpr_read_b32 v7, a211\n"
    "v_accvgpr_read_b32 v8, a224\n"
    "v_accvgpr_read_b32 v9, a225\n"
    "v_accvgpr_read_b32 v10, a226\n"
    "v_accvgpr_read_b32 v11, a227\n"
    "v_accvgpr_read_b32 v12, a240\n"
    "v_accvgpr_read_b32 v13, a241\n"
    "v_accvgpr_read_b32 v14, a242\n"
    "v_accvgpr_read_b32 v15, a243\n"
    "s_or_b64 s[46:47], s[46:47], vcc\n"
    "v_pk_mul_f32 v[64:65], v[64:65], v[248:249] op_sel_hi:[1,0]\n"
    "v_pk_mul_f32 v[66:67], v[66:67], v[248:249] op_sel_hi:[1,0]\n"
    "v_pk_mul_f32 v[80:81], v[80:81], v[248:249] op_sel_hi:[1,0]\n"
    "v_pk_mul_f32 v[82:83], v[82:83], v[248:249] op_sel_hi:[1,0]\n"
    "v_cvt_pk_bf16_f32 v64, v64, v65\n"
    "v_cvt_pk_bf16_f32 v65, v66, v67\n"
    "v_pk_mul_f32 v[96:97], v[96:97], v[248:249] op_sel_hi:[1,0]\n"
    "v_pk_mul_f32 v[98:99], v[98:99], v[248:249] op_sel_hi:[1,0]\n"
    "v_cvt_pk_bf16_f32 v80, v80, v81\n"
    "v_cvt_pk_bf16_f32 v81, v82, v83\n"
    "ds_write_b64 v237, v[64:65] offset:0\n"
    "v_pk_mul_f32 v[112:113], v[112:113], v[248:249] op_sel_hi:[1,0]\n"
    "v_pk_mul_f32 v[114:115], v[114:115], v[248:249] op_sel_hi:[1,0]\n"
    "v_cvt_pk_bf16_f32 v96, v96, v97\n"
    "v_cvt_pk_bf16_f32 v97, v98, v99\n"
    "ds_write_b64 v238, v[80:81] offset:0\n"
    "v_pk_mul_f32 v[128:129], v[128:129], v[248:249] op_sel_hi:[1,0]\n"
    "v_pk_mul_f32 v[130:131], v[130:131], v[248:249] op_sel_hi:[1,0]\n"
    "v_cvt_pk_bf16_f32 v112, v112, v113\n"
    "v_cvt_pk_bf16_f32 v113, v114, v115\n"
    "ds_write_b64 v239, v[96:97] offset:0\n"
    "v_pk_mul_f32 v[144:145], v[144:145], v[248:249] op_sel_hi:[1,0]\n"
    "v_pk_mul_f32 v[146:147], v[146:147], v[248:249] op_sel_hi:[1,0]\n"
    "v_cvt_pk_bf16_f32 v128, v128, v129\n"
    "v_cvt_pk_bf16_f32 v129, v130, v131\n"
    "ds_write_b64 v240, v[112:113] offset:0\n"
    "v_pk_mul_f32 v[160:161], v[160:161], v[248:249] op_sel_hi:[1,0]\n"
    "v_pk_mul_f32 v[162:163], v[162:163], v[248:249] op_sel_hi:[1,0]\n"
    "v_cvt_pk_bf16_f32 v144, v144, v145\n"
    "v_cvt_pk_bf16_f32 v145, v146, v147\n"
    "ds_write_b64 v237, v[128:129] offset:128\n"
    "v_pk_mul_f32 v[176:177], v[176:177], v[248:249] op_sel_hi:[1,0]\n"
    "v_pk_mul_f32 v[178:179], v[178:179], v[248:249] op_sel_hi:[1,0]\n"
    "v_cvt_pk_bf16_f32 v160, v160, v161\n"
    "v_cvt_pk_bf16_f32 v161, v162, v163\n"
    "ds_write_b64 v238, v[144:145] offset:128\n"
    "v_pk_mul_f32 v[0:1], v[0:1], v[248:249] op_sel_hi:[1,0]\n"
    "v_pk_mul_f32 v[2:3], v[2:3], v[248:249] op_sel_hi:[1,0]\n"
    "v_cvt_pk_bf16_f32 v176, v176, v177\n"
    "v_cvt_pk_bf16_f32 v177, v178, v179\n"
    "ds_write_b64 v239, v[160:161] offset:128\n"
    "v_pk_mul_f32 v[4:5], v[4:5], v[248:249] op_sel_hi:[1,0]\n"
    "v_pk_mul_f32 v[6:7], v[6:7], v[248:249] op_sel_hi:[1,0]\n"
    "v_cvt_pk_bf16_f32 v0, v0, v1\n"
    "v_cvt_pk_bf16_f32 v1, v2, v3\n"
    "ds_write_b64 v240, v[176:177] offset:128\n"
    "v_pk_mul_f32 v[8:9], v[8:9], v[248:249] op_sel_hi:[1,0]\n"
    "v_pk_mul_f32 v[10:11], v[10:11], v[248:249] op_sel_hi:[1,0]\n"
    "v_cvt_pk_bf16_f32 v4, v4, v5\n"
    "v_cvt_pk_bf16_f32 v5, v6, v7\n"
    "ds_write_b64 v237, v[0:1] offset:256\n"
    "v_pk_mul_f32 v[12:13], v[12:13], v[248:249] op_sel_hi:[1,0]\n"
    "v_pk_mul_f32 v[14:15], v[14:15], v[248:249] op_sel_hi:[1,0]\n"
    "v_cvt_pk_bf16_f32 v8, v8, v9\n"
    "v_cvt_pk_bf16_f32 v9, v10, v11\n"
    "ds_write_b64 v238, v[4:5] offset:256\n"
    "v_cvt_pk_bf16_f32 v12, v12, v13\n"
    "v_cvt_pk_bf16_f32 v13, v14, v15\n"
    "ds_write_b64 v239, v[8:9] offset:256\n"
    "ds_write_b64 v240, v[12:13] offset:256\n"
    "v_rcp_f32 v248, v196\n"
    "v_cmp_nge_f32 vcc, s48, v196\n"
    "v_accvgpr_read_b32 v32, a196\n"
    "v_accvgpr_read_b32 v33, a197\n"
    "v_accvgpr_read_b32 v34, a198\n"
    "v_accvgpr_read_b32 v35, a199\n"
    "v_accvgpr_read_b32 v36, a212\n"
    "v_accvgpr_read_b32 v37, a213\n"
    "v_accvgpr_read_b32 v38, a214\n"
    "v_accvgpr_read_b32 v39, a215\n"
    "v_accvgpr_read_b32 v40, a228\n"
    "v_accvgpr_read_b32 v41, a229\n"
    "v_accvgpr_read_b32 v42, a230\n"
    "v_accvgpr_read_b32 v43, a231\n"
    "v_accvgpr_read_b32 v44, a244\n"
    "v_accvgpr_read_b32 v45, a245\n"
    "v_accvgpr_read_b32 v46, a246\n"
    "v_accvgpr_read_b32 v47, a247\n"
    "s_or_b64 s[46:47], s[46:47], vcc\n"
    "v_pk_mul_f32 v[68:69], v[68:69], v[248:249] op_sel_hi:[1,0]\n"
    "v_pk_mul_f32 v[70:71], v[70:71], v[248:249] op_sel_hi:[1,0]\n"
    "v_pk_mul_f32 v[84:85], v[84:85], v[248:249] op_sel_hi:[1,0]\n"
    "v_pk_mul_f32 v[86:87], v[86:87], v[248:249] op_sel_hi:[1,0]\n"
    "v_cvt_pk_bf16_f32 v68, v68, v69\n"
    "v_cvt_pk_bf16_f32 v69, v70, v71\n"
    "v_pk_mul_f32 v[100:101], v[100:101], v[248:249] op_sel_hi:[1,0]\n"
    "v_pk_mul_f32 v[102:103], v[102:103], v[248:249] op_sel_hi:[1,0]\n"
    "v_cvt_pk_bf16_f32 v84, v84, v85\n"
    "v_cvt_pk_bf16_f32 v85, v86, v87\n"
    "ds_write_b64 v237, v[68:69] offset:6144\n"
    "v_pk_mul_f32 v[116:117], v[116:117], v[248:249] op_sel_hi:[1,0]\n"
    "v_pk_mul_f32 v[118:119], v[118:119], v[248:249] op_sel_hi:[1,0]\n"
    "v_cvt_pk_bf16_f32 v100, v100, v101\n"
    "v_cvt_pk_bf16_f32 v101, v102, v103\n"
    "ds_write_b64 v238, v[84:85] offset:6144\n"
    "v_pk_mul_f32 v[132:133], v[132:133], v[248:249] op_sel_hi:[1,0]\n"
    "v_pk_mul_f32 v[134:135], v[134:135], v[248:249] op_sel_hi:[1,0]\n"
    "v_cvt_pk_bf16_f32 v116, v116, v117\n"
    "v_cvt_pk_bf16_f32 v117, v118, v119\n"
    "ds_write_b64 v239, v[100:101] offset:6144\n"
    "v_pk_mul_f32 v[148:149], v[148:149], v[248:249] op_sel_hi:[1,0]\n"
    "v_pk_mul_f32 v[150:151], v[150:151], v[248:249] op_sel_hi:[1,0]\n"
    "v_cvt_pk_bf16_f32 v132, v132, v133\n"
    "v_cvt_pk_bf16_f32 v133, v134, v135\n"
    "ds_write_b64 v240, v[116:117] offset:6144\n"
    "v_pk_mul_f32 v[164:165], v[164:165], v[248:249] op_sel_hi:[1,0]\n"
    "v_pk_mul_f32 v[166:167], v[166:167], v[248:249] op_sel_hi:[1,0]\n"
    "v_cvt_pk_bf16_f32 v148, v148, v149\n"
    "v_cvt_pk_bf16_f32 v149, v150, v151\n"
    "ds_write_b64 v237, v[132:133] offset:6272\n"
    "v_pk_mul_f32 v[180:181], v[180:181], v[248:249] op_sel_hi:[1,0]\n"
    "v_pk_mul_f32 v[182:183], v[182:183], v[248:249] op_sel_hi:[1,0]\n"
    "v_cvt_pk_bf16_f32 v164, v164, v165\n"
    "v_cvt_pk_bf16_f32 v165, v166, v167\n"
    "ds_write_b64 v238, v[148:149] offset:6272\n"
    "v_pk_mul_f32 v[32:33], v[32:33], v[248:249] op_sel_hi:[1,0]\n"
    "v_pk_mul_f32 v[34:35], v[34:35], v[248:249] op_sel_hi:[1,0]\n"
    "v_cvt_pk_bf16_f32 v180, v180, v181\n"
    "v_cvt_pk_bf16_f32 v181, v182, v183\n"
    "ds_write_b64 v239, v[164:165] offset:6272\n"
    "v_pk_mul_f32 v[36:37], v[36:37], v[248:249] op_sel_hi:[1,0]\n"
    "v_pk_mul_f32 v[38:39], v[38:39], v[248:249] op_sel_hi:[1,0]\n"
    "v_cvt_pk_bf16_f32 v32, v32, v33\n"
    "v_cvt_pk_bf16_f32 v33, v34, v35\n"
    "ds_write_b64 v240, v[180:181] offset:6272\n"
    "v_pk_mul_f32 v[40:41], v[40:41], v[248:249] op_sel_hi:[1,0]\n"
    "v_pk_mul_f32 v[42:43], v[42:43], v[248:249] op_sel_hi:[1,0]\n"
    "v_cvt_pk_bf16_f32 v36, v36, v37\n"
    "v_cvt_pk_bf16_f32 v37, v38, v39\n"
    "ds_write_b64 v237, v[32:33] offset:6400\n"
    "v_pk_mul_f32 v[44:45], v[44:45], v[248:249] op_sel_hi:[1,0]\n"
    "v_pk_mul_f32 v[46:47], v[46:47], v[248:249] op_sel_hi:[1,0]\n"
    "v_cvt_pk_bf16_f32 v40, v40, v41\n"
    "v_cvt_pk_bf16_f32 v41, v42, v43\n"
    "ds_write_b64 v238, v[36:37] offset:6400\n"
    "v_cvt_pk_bf16_f32 v44, v44, v45\n"
    "v_cvt_pk_bf16_f32 v45, v46, v47\n"
    "ds_write_b64 v239, v[40:41] offset:6400\n"
    "ds_write_b64 v240, v[44:45] offset:6400\n"
    "s_waitcnt lgkmcnt(0)\n"
    "ds_read_b128 v[64:67], v241 offset:0\n"
    "ds_read_b128 v[80:83], v242 offset:1024\n"
    "ds_read_b128 v[96:99], v243 offset:2048\n"
    "ds_read_b128 v[112:115], v244 offset:3072\n"
    "ds_read_b128 v[128:131], v245 offset:4096\n"
    "ds_read_b128 v[144:147], v246 offset:5120\n"
    "ds_read_b128 v[68:71], v241 offset:6144\n"
    "ds_read_b128 v[84:87], v242 offset:7168\n"
    "ds_read_b128 v[100:103], v243 offset:8192\n"
    "ds_read_b128 v[116:119], v244 offset:9216\n"
    "ds_read_b128 v[132:135], v245 offset:10240\n"
    "ds_read_b128 v[148:151], v246 offset:11264\n"
    "s_waitcnt lgkmcnt(0)\n"
    "L_ph65:\n"
    "v_mfma_f32_16x16x32_bf16 v[16:19], a[0:3], a[144:147], 0\n"
    "ds_read_b64_tr_b16 a[48:49], v218 offset:0\n"
    "s_add_u32 s43, s41, 0\n"
    "v_mfma_f32_16x16x32_bf16 v[16:19], a[4:7], a[148:151], v[16:19]\n"
    "buffer_store_dwordx4 v[64:67], v236, s[28:31], s43 offen sc1\n"
    "v_mfma_f32_16x16x32_bf16 v[16:19], a[8:11], a[152:155], v[16:19]\n"
    "ds_read_b64_tr_b16 a[50:51], v218 offset:6144\n"
    "ds_read_b64_tr_b16 a[84:85], v219 offset:256\n"
    "v_mfma_f32_16x16x32_bf16 v[16:19], a[12:15], a[156:159], v[16:19]\n"
    "buffer_store_dwordx4 v[80:83], v236, s[28:31], s43 offen offset:1024 sc1\n"
    "v_mfma_f32_16x16x32_bf16 v[16:19], a[16:19], a[160:163], v[16:19]\n"
    "ds_read_b64_tr_b16 a[52:53], v219 offset:0\n"
    "ds_read_b64_tr_b16 a[86:87], v219 offset:6400\n"
    "v_mfma_f32_16x16x32_bf16 v[16:19], a[20:23], a[164:167], v[16:19]\n"
    "buffer_store_dwordx4 v[96:99], v236, s[28:31], s43 offen offset:2048 sc1\n"
    "v_mfma_f32_16x16x32_bf16 v[20:23], a[24:27], a[144:147], 0\n"
    "ds_read_b64_tr_b16 a[54:55], v219 offset:6144\n"
    "ds_read_b64_tr_b16 a[88:89], v220 offset:256\n"
    "v_mfma_f32_16x16x32_bf16 v[20:23], a[28:31], a[148:151], v[20:23]\n"
    "ds_read_b64_tr_b16 a[56:57], v220 offset:0\n"
    "v_mfma_f32_16x16x32_bf16 v[20:23], a[32:35], a[152:155], v[20:23]\n"
    "ds_read_b64_tr_b16 a[58:59], v220 offset:6144\n"
    "ds_read_b64_tr_b16 a[90:91], v220 offset:6400\n"
    "v_mfma_f32_16x16x32_bf16 v[20:23], a[36:39], a[156:159], v[20:23]\n"
    "ds_read_b64_tr_b16 a[60:61], v221 offset:0\n"
    "v_mfma_f32_16x16x32_bf16 v[20:23], a[40:43], a[160:163], v[20:23]\n"
    "ds_read_b64_tr_b16 a[62:63], v221 offset:6144\n"
    "ds_read_b64_tr_b16 a[92:93], v221 offset:256\n"
    "v_mfma_f32_16x16x32_bf16 v[20:23], a[44:47], a[164:167], v[20:23]\n"
    "ds_read_b64_tr_b16 a[64:65], v218 offset:128\n"
    "v_mfma_f32_16x16x32_bf16 v[24:27], a[0:3], a[168:171], 0\n"
    "ds_read_b64_tr_b16 a[66:67], v218 offset:6272\n"
    "ds_read_b64_tr_b16 a[94:95], v221 offset:6400\n"
    "v_mfma_f32_16x16x32_bf16 v[24:27], a[4:7], a[172:175], v[24:27]\n"
    "ds_read_b64_tr_b16 a[68:69], v219 offset:128\n"
    "v_fma_f32 v16, v16, s36, v214\n"
    "v_mfma_f32_16x16x32_bf16 v[24:27], a[8:11], a[176:179], v[24:27]\n"
    "ds_read_b64_tr_b16 a[70:71], v219 offset:6272\n"
    "v_exp_f32 v16, v16\n"
    "v_mfma_f32_16x16x32_bf16 v[24:27], a[12:15], a[180:183], v[24:27]\n"
    "ds_read_b64_tr_b16 a[72:73], v220 offset:128\n"
    "v_fma_f32 v17, v17, s36, v214\n"
    "v_mfma_f32_16x16x32_bf16 v[24:27], a[16:19], a[184:187], v[24:27]\n"
    "ds_read_b64_tr_b16 a[74:75], v220 offset:6272\n"
    "v_exp_f32 v17, v17\n"
    "v_mfma_f32_16x16x32_bf16 v[24:27], a[20:23], a[188:191], v[24:27]\n"
    "ds_read_b64_tr_b16 a[76:77], v221 offset:128\n"
    "v_fma_f32 v18, v18, s36, v214\n"
    "v_mfma_f32_16x16x32_bf16 v[28:31], a[24:27], a[168:171], 0\n"
    "ds_read_b64_tr_b16 a[78:79], v221 offset:6272\n"
    "v_exp_f32 v18, v18\n"
    "v_mfma_f32_16x16x32_bf16 v[28:31], a[28:31], a[172:175], v[28:31]\n"
    "ds_read_b64_tr_b16 a[80:81], v218 offset:256\n"
    "v_fma_f32 v19, v19, s36, v214\n"
    "v_mfma_f32_16x16x32_bf16 v[28:31], a[32:35], a[176:179], v[28:31]\n"
    "ds_read_b64_tr_b16 a[82:83], v218 offset:6400\n"
    "v_fma_f32 v20, v20, s36, v214\n"
    "v_mfma_f32_16x16x32_bf16 v[28:31], a[36:39], a[180:183], v[28:31]\n"
    "v_exp_f32 v19, v19\n"
    "v_mfma_f32_16x16x32_bf16 v[28:31], a[40:43], a[184:187], v[28:31]\n"
    "v_exp_f32 v20, v20\n"
    "v_mfma_f32_16x16x32_bf16 v[28:31], a[44:47], a[188:191], v[28:31]\n"
    "s_waitcnt lgkmcnt(0)\n"
    "L_ph66:\n"
    "v_mfma_f32_16x16x32_bf16 v[72:75], a[48:51], v[48:51], v[72:75]\n"
    "ds_read_b128 a[0:3], v216 offset:12288\n"
    "s_add_u32 s43, s41, 3072\n"
    "v_mfma_f32_16x16x32_bf16 v[88:91], a[52:55], v[48:51], v[88:91]\n"
    "buffer_store_dwordx4 v[112:115], v236, s[28:31], s43 offen sc1\n"
    "v_mfma_f32_16x16x32_bf16 v[104:107], a[56:59], v[48:51], v[104:107]\n"
    "ds_read_b128 a[4:7], v217 offset:12288\n"
    "v_fma_f32 v21, v21, s36, v214\n"
    "v_mfma_f32_16x16x32_bf16 v[120:123], a[60:63], v[48:51], v[120:123]\n"
    "buffer_store_dwordx4 v[128:131], v236, s[28:31], s43 offen offset:1024 sc1\n"
    "v_mfma_f32_16x16x32_bf16 v[136:139], a[64:67], v[48:51], v[136:139]\n"
    "ds_read_b128 a[8:11], v216 offset:12416\n"
    "v_exp_f32 v21, v21\n"
    "v_mfma_f32_16x16x32_bf16 v[152:155], a[68:71], v[48:51], v[152:155]\n"
    "buffer_store_dwordx4 v[144:147], v236, s[28:31], s43 offen offset:2048 sc1\n"
    "v_mfma_f32_16x16x32_bf16 v[168:171], a[72:75], v[48:51], v[168:171]\n"
    "ds_read_b128 a[12:15], v217 offset:12416\n"
    "v_fma_f32 v22, v22, s36, v214\n"
    "v_mfma_f32_16x16x32_bf16 v[184:187], a[76:79], v[48:51], v[184:187]\n"
    "ds_read_b128 a[16:19], v216 offset:12544\n"
    "v_exp_f32 v22, v22\n"
    "v_mfma_f32_16x16x32_bf16 a[200:203], a[80:83], v[48:51], a[200:203]\n"
    "ds_read_b128 a[20:23], v217 offset:12544\n"
    "v_fma_f32 v23, v23, s36, v214\n"
    "v_mfma_f32_16x16x32_bf16 a[216:219], a[84:87], v[48:51], a[216:219]\n"
    "ds_read_b128 a[24:27], v216 offset:18432\n"
    "v_exp_f32 v23, v23\n"
    "v_mfma_f32_16x16x32_bf16 a[232:235], a[88:91], v[48:51], a[232:235]\n"
    "ds_read_b128 a[28:31], v217 offset:18432\n"
    "v_cvt_pk_bf16_f32 v16, v16, v17\n"
    "v_mfma_f32_16x16x32_bf16 a[248:251], a[92:95], v[48:51], a[248:251]\n"
    "ds_read_b128 a[32:35], v216 offset:18560\n"
    "v_cvt_pk_bf16_f32 v17, v18, v19\n"
    "v_mfma_f32_16x16x32_bf16 v[200:203], v[208:211], v[48:51], v[200:203]\n"
    "ds_read_b128 a[36:39], v217 offset:18560\n"
    "v_cvt_pk_bf16_f32 v18, v20, v21\n"
    "v_mfma_f32_16x16x32_bf16 v[76:79], a[48:51], v[56:59], v[76:79]\n"
    "ds_read_b128 a[40:43], v216 offset:18688\n"
    "v_cvt_pk_bf16_f32 v19, v22, v23\n"
    "v_mfma_f32_16x16x32_bf16 v[92:95], a[52:55], v[56:59], v[92:95]\n"
    "ds_read_b128 a[44:47], v217 offset:18688\n"
    "v_fma_f32 v24, v24, s36, v215\n"
    "v_mfma_f32_16x16x32_bf16 v[108:111], a[56:59], v[56:59], v[108:111]\n"
    "v_exp_f32 v24, v24\n"
    "v_mfma_f32_16x16x32_bf16 v[124:127], a[60:63], v[56:59], v[124:127]\n"
    "v_fma_f32 v25, v25, s36, v215\n"
    "v_fma_f32 v26, v26, s36, v215\n"
    "v_mfma_f32_16x16x32_bf16 v[140:143], a[64:67], v[56:59], v[140:143]\n"
    "v_exp_f32 v25, v25\n"
    "v_mfma_f32_16x16x32_bf16 v[156:159], a[68:71], v[56:59], v[156:159]\n"
    "v_exp_f32 v26, v26\n"
    "v_mfma_f32_16x16x32_bf16 v[172:175], a[72:75], v[56:59], v[172:175]\n"
    "v_fma_f32 v27, v27, s36, v215\n"
    "v_fma_f32 v28, v28, s36, v215\n"
    "v_mfma_f32_16x16x32_bf16 v[188:191], a[76:79], v[56:59], v[188:191]\n"
    "v_exp_f32 v27, v27\n"
    "v_mfma_f32_16x16x32_bf16 a[204:207], a[80:83], v[56:59], a[204:207]\n"
    "v_exp_f32 v28, v28\n"
    "v_mfma_f32_16x16x32_bf16 a[220:223], a[84:87], v[56:59], a[220:223]\n"
    "v_fma_f32 v29, v29, s36, v215\n"
    "v_fma_f32 v30, v30, s36, v215\n"
    "v_mfma_f32_16x16x32_bf16 a[236:239], a[88:91], v[56:59], a[236:239]\n"
    "v_exp_f32 v29, v29\n"
    "v_mfma_f32_16x16x32_bf16 a[252:255], a[92:95], v[56:59], a[252:255]\n"
    "v_exp_f32 v30, v30\n"
    "v_mfma_f32_16x16x32_bf16 v[204:207], v[208:211], v[56:59], v[204:207]\n"
    "s_waitcnt lgkmcnt(0)\n"
    "L_ph67:\n"
    "v_mfma_f32_16x16x32_bf16 v[48:51], a[0:3], a[144:147], 0\n"
    "s_mov_b32 m0, 159744\n"
    "s_mov_b32 s51, L_ph69-L_code_start\n"
    "v_mfma_f32_16x16x32_bf16 v[48:51], a[4:7], a[148:151], v[48:51]\n"
    "buffer_load_dword v249, s[52:55], s51 offen lds\n"
    "v_mfma_f32_16x16x32_bf16 v[48:51], a[8:11], a[152:155], v[48:51]\n"
    "ds_read_b64_tr_b16 a[48:49], v218 offset:24576\n"
    "s_add_u32 s43, s41, 6144\n"
    "v_mfma_f32_16x16x32_bf16 v[48:51], a[12:15], a[156:159], v[48:51]\n"
    "buffer_store_dwordx4 v[68:71], v236, s[28:31], s43 offen sc1\n"
    "v_mfma_f32_16x16x32_bf16 v[48:51], a[16:19], a[160:163], v[48:51]\n"
    "ds_read_b64_tr_b16 a[50:51], v218 offset:30720\n"
    "ds_read_b64_tr_b16 a[80:81], v218 offset:24832\n"
    "v_mfma_f32_16x16x32_bf16 v[48:51], a[20:23], a[164:167], v[48:51]\n"
    "buffer_store_dwordx4 v[84:87], v236, s[28:31], s43 offen offset:1024 sc1\n"
    "v_mfma_f32_16x16x32_bf16 v[52:55], a[24:27], a[144:147], 0\n"
    "ds_read_b64_tr_b16 a[52:53], v219 offset:24576\n"
    "ds_read_b64_tr_b16 a[82:83], v218 offset:30976\n"
    "v_mfma_f32_16x16x32_bf16 v[52:55], a[28:31], a[148:151], v[52:55]\n"
    "buffer_store_dwordx4 v[100:103], v236, s[28:31], s43 offen offset:2048 sc1\n"
    "v_mfma_f32_16x16x32_bf16 v[52:55], a[32:35], a[152:155], v[52:55]\n"
    "ds_read_b64_tr_b16 a[54:55], v219 offset:30720\n"
    "ds_read_b64_tr_b16 a[84:85], v219 offset:24832\n"
    "v_mfma_f32_16x16x32_bf16 v[52:55], a[36:39], a[156:159], v[52:55]\n"
    "ds_read_b64_tr_b16 a[56:57], v220 offset:24576\n"
    "v_fma_f32 v31, v31, s36, v215\n"
    "v_mfma_f32_16x16x32_bf16 v[52:55], a[40:43], a[160:163], v[52:55]\n"
    "ds_read_b64_tr_b16 a[58:59], v220 offset:30720\n"
    "ds_read_b64_tr_b16 a[86:87], v219 offset:30976\n"
    "v_mfma_f32_16x16x32_bf16 v[52:55], a[44:47], a[164:167], v[52:55]\n"
    "ds_read_b64_tr_b16 a[60:61], v221 offset:24576\n"
    "v_exp_f32 v31, v31\n"
    "v_mfma_f32_16x16x32_bf16 v[56:59], a[0:3], a[168:171], 0\n"
    "ds_read_b64_tr_b16 a[62:63], v221 offset:30720\n"
    "ds_read_b64_tr_b16 a[88:89], v220 offset:24832\n"
    "v_mfma_f32_16x16x32_bf16 v[56:59], a[4:7], a[172:175], v[56:59]\n"
    "ds_read_b64_tr_b16 a[64:65], v218 offset:24704\n"
    "v_cvt_pk_bf16_f32 v24, v24, v25\n"
    "v_mfma_f32_16x16x32_bf16 v[56:59], a[8:11], a[176:179], v[56:59]\n"
    "ds_read_b64_tr_b16 a[66:67], v218 offset:30848\n"
    "ds_read_b64_tr_b16 a[90:91], v220 offset:30976\n"
    "v_mfma_f32_16x16x32_bf16 v[56:59], a[12:15], a[180:183], v[56:59]\n"
    "ds_read_b64_tr_b16 a[68:69], v219 offset:24704\n"
    "v_cvt_pk_bf16_f32 v25, v26, v27\n"
    "v_mfma_f32_16x16x32_bf16 v[56:59], a[16:19], a[184:187], v[56:59]\n"
    "ds_read_b64_tr_b16 a[70:71], v219 offset:30848\n"
    "ds_read_b64_tr_b16 a[92:93], v221 offset:24832\n"
    "v_mfma_f32_16x16x32_bf16 v[56:59], a[20:23], a[188:191], v[56:59]\n"
    "ds_read_b64_tr_b16 a[72:73], v220 offset:24704\n"
    "v_cvt_pk_bf16_f32 v26, v28, v29\n"
    "v_mfma_f32_16x16x32_bf16 v[60:63], a[24:27], a[168:171], 0\n"
    "ds_read_b64_tr_b16 a[74:75], v220 offset:30848\n"
    "ds_read_b64_tr_b16 a[94:95], v221 offset:30976\n"
    "v_mfma_f32_16x16x32_bf16 v[60:63], a[28:31], a[172:175], v[60:63]\n"
    "ds_read_b64_tr_b16 a[76:77], v221 offset:24704\n"
    "v_cvt_pk_bf16_f32 v27, v30, v31\n"
    "v_mfma_f32_16x16x32_bf16 v[60:63], a[32:35], a[176:179], v[60:63]\n"
    "ds_read_b64_tr_b16 a[78:79], v221 offset:30848\n"
    "v_fma_f32 v48, v48, s36, v214\n"
    "v_mfma_f32_16x16x32_bf16 v[60:63], a[36:39], a[180:183], v[60:63]\n"
    "v_exp_f32 v48, v48\n"
    "v_mfma_f32_16x16x32_bf16 v[60:63], a[40:43], a[184:187], v[60:63]\n"
    "v_fma_f32 v49, v49, s36, v214\n"
    "v_fma_f32 v50, v50, s36, v214\n"
    "v_mfma_f32_16x16x32_bf16 v[60:63], a[44:47], a[188:191], v[60:63]\n"
    "s_waitcnt lgkmcnt(0)\n"
    "L_ph68:\n"
    "v_mfma_f32_16x16x32_bf16 v[72:75], a[48:51], v[16:19], v[72:75]\n"
    "ds_read_b64_tr_b16 a[0:1], v218 offset:36864\n"
    "s_add_u32 s43, s41, 9216\n"
    "v_mfma_f32_16x16x32_bf16 v[88:91], a[52:55], v[16:19], v[88:91]\n"
    "buffer_store_dwordx4 v[116:119], v236, s[28:31], s43 offen sc1\n"
    "v_mfma_f32_16x16x32_bf16 v[104:107], a[56:59], v[16:19], v[104:107]\n"
    "ds_read_b64_tr_b16 a[2:3], v218 offset:43008\n"
    "v_exp_f32 v49, v49\n"
    "v_mfma_f32_16x16x32_bf16 v[120:123], a[60:63], v[16:19], v[120:123]\n"
    "buffer_store_dwordx4 v[132:135], v236, s[28:31], s43 offen offset:1024 sc1\n"
    "v_mfma_f32_16x16x32_bf16 v[136:139], a[64:67], v[16:19], v[136:139]\n"
    "ds_read_b64_tr_b16 a[4:5], v219 offset:36864\n"
    "v_exp_f32 v50, v50\n"
    "v_mfma_f32_16x16x32_bf16 v[152:155], a[68:71], v[16:19], v[152:155]\n"
    "buffer_store_dwordx4 v[148:151], v236, s[28:31], s43 offen offset:2048 sc1\n"
    "v_mfma_f32_16x16x32_bf16 v[168:171], a[72:75], v[16:19], v[168:171]\n"
    "ds_read_b64_tr_b16 a[6:7], v219 offset:43008\n"
    "v_fma_f32 v51, v51, s36, v214\n"
    "v_mfma_f32_16x16x32_bf16 v[184:187], a[76:79], v[16:19], v[184:187]\n"
    "ds_read_b64_tr_b16 a[8:9], v220 offset:36864\n"
    "v_exp_f32 v51, v51\n"
    "v_mfma_f32_16x16x32_bf16 a[200:203], a[80:83], v[16:19], a[200:203]\n"
    "ds_read_b64_tr_b16 a[10:11], v220 offset:43008\n"
    "v_fma_f32 v52, v52, s36, v214\n"
    "v_mfma_f32_16x16x32_bf16 a[216:219], a[84:87], v[16:19], a[216:219]\n"
    "ds_read_b64_tr_b16 a[12:13], v221 offset:36864\n"
    "v_exp_f32 v52, v52\n"
    "v_mfma_f32_16x16x32_bf16 a[232:235], a[88:91], v[16:19], a[232:235]\n"
    "ds_read_b64_tr_b16 a[14:15], v221 offset:43008\n"
    "v_fma_f32 v53, v53, s36, v214\n"
    "v_mfma_f32_16x16x32_bf16 a[248:251], a[92:95], v[16:19], a[248:251]\n"
    "ds_read_b64_tr_b16 a[16:17], v218 offset:36992\n"
    "v_exp_f32 v53, v53\n"
    "v_mfma_f32_16x16x32_bf16 v[200:203], v[208:211], v[16:19], v[200:203]\n"
    "ds_read_b64_tr_b16 a[18:19], v218 offset:43136\n"
    "v_fma_f32 v54, v54, s36, v214\n"
    "v_mfma_f32_16x16x32_bf16 v[76:79], a[48:51], v[24:27], v[76:79]\n"
    "ds_read_b64_tr_b16 a[20:21], v219 offset:36992\n"
    "v_exp_f32 v54, v54\n"
    "v_mfma_f32_16x16x32_bf16 v[92:95], a[52:55], v[24:27], v[92:95]\n"
    "ds_read_b64_tr_b16 a[22:23], v219 offset:43136\n"
    "v_fma_f32 v55, v55, s36, v214\n"
    "v_mfma_f32_16x16x32_bf16 v[108:111], a[56:59], v[24:27], v[108:111]\n"
    "ds_read_b64_tr_b16 a[24:25], v220 offset:36992\n"
    "v_exp_f32 v55, v55\n"
    "v_mfma_f32_16x16x32_bf16 v[124:127], a[60:63], v[24:27], v[124:127]\n"
    "ds_read_b64_tr_b16 a[26:27], v220 offset:43136\n"
    "v_cvt_pk_bf16_f32 v48, v48, v49\n"
    "v_mfma_f32_16x16x32_bf16 v[140:143], a[64:67], v[24:27], v[140:143]\n"
    "ds_read_b64_tr_b16 a[28:29], v221 offset:36992\n"
    "v_cvt_pk_bf16_f32 v49, v50, v51\n"
    "v_mfma_f32_16x16x32_bf16 v[156:159], a[68:71], v[24:27], v[156:159]\n"
    "ds_read_b64_tr_b16 a[30:31], v221 offset:43136\n"
    "v_cvt_pk_bf16_f32 v50, v52, v53\n"
    "v_mfma_f32_16x16x32_bf16 v[172:175], a[72:75], v[24:27], v[172:175]\n"
    "ds_read_b64_tr_b16 a[32:33], v218 offset:37120\n"
    "v_cvt_pk_bf16_f32 v51, v54, v55\n"
    "v_mfma_f32_16x16x32_bf16 v[188:191], a[76:79], v[24:27], v[188:191]\n"
    "ds_read_b64_tr_b16 a[34:35], v218 offset:43264\n"
    "v_fma_f32 v56, v56, s36, v215\n"
    "v_mfma_f32_16x16x32_bf16 a[204:207], a[80:83], v[24:27], a[204:207]\n"
    "ds_read_b64_tr_b16 a[36:37], v219 offset:37120\n"
    "v_exp_f32 v56, v56\n"
    "v_mfma_f32_16x16x32_bf16 a[220:223], a[84:87], v[24:27], a[220:223]\n"
    "ds_read_b64_tr_b16 a[38:39], v219 offset:43264\n"
    "v_fma_f32 v57, v57, s36, v215\n"
    "v_mfma_f32_16x16x32_bf16 a[236:239], a[88:91], v[24:27], a[236:239]\n"
    "ds_read_b64_tr_b16 a[40:41], v220 offset:37120\n"
    "v_exp_f32 v57, v57\n"
    "v_mfma_f32_16x16x32_bf16 a[252:255], a[92:95], v[24:27], a[252:255]\n"
    "ds_read_b64_tr_b16 a[42:43], v220 offset:43264\n"
    "v_fma_f32 v58, v58, s36, v215\n"
    "v_mfma_f32_16x16x32_bf16 v[204:207], v[208:211], v[24:27], v[204:207]\n"
    "ds_read_b64_tr_b16 a[44:45], v221 offset:37120\n"
    "v_fma_f32 v59, v59, s36, v215\n"
    "ds_read_b64_tr_b16 a[46:47], v221 offset:43264\n"
    "v_fma_f32 v60, v60, s36, v215\n"
    "v_exp_f32 v58, v58\n"
    "v_exp_f32 v59, v59\n"
    "v_exp_f32 v60, v60\n"
    "v_fma_f32 v61, v61, s36, v215\n"
    "v_fma_f32 v62, v62, s36, v215\n"
    "v_exp_f32 v61, v61\n"
    "v_exp_f32 v62, v62\n"
    "v_fma_f32 v63, v63, s36, v215\n"
    "v_cvt_pk_bf16_f32 v56, v56, v57\n"
    "v_exp_f32 v63, v63\n"
    "v_cvt_pk_bf16_f32 v57, v58, v59\n"
    "v_cvt_pk_bf16_f32 v58, v60, v61\n"
    "v_cvt_pk_bf16_f32 v59, v62, v63\n"
    "s_waitcnt lgkmcnt(0)\n"
    "L_ph69:\n"
    "v_mfma_f32_16x16x32_bf16 v[72:75], a[0:3], v[48:51], v[72:75]\n"
    "v_mfma_f32_16x16x32_bf16 v[88:91], a[4:7], v[48:51], v[88:91]\n"
    "v_mfma_f32_16x16x32_bf16 v[104:107], a[8:11], v[48:51], v[104:107]\n"
    "v_mfma_f32_16x16x32_bf16 v[120:123], a[12:15], v[48:51], v[120:123]\n"
    "v_mfma_f32_16x16x32_bf16 v[136:139], a[16:19], v[48:51], v[136:139]\n"
    "v_mfma_f32_16x16x32_bf16 v[152:155], a[20:23], v[48:51], v[152:155]\n"
    "v_mfma_f32_16x16x32_bf16 v[168:171], a[24:27], v[48:51], v[168:171]\n"
    "v_mfma_f32_16x16x32_bf16 v[184:187], a[28:31], v[48:51], v[184:187]\n"
    "v_mfma_f32_16x16x32_bf16 a[200:203], a[32:35], v[48:51], a[200:203]\n"
    "v_mfma_f32_16x16x32_bf16 a[216:219], a[36:39], v[48:51], a[216:219]\n"
    "v_mfma_f32_16x16x32_bf16 a[232:235], a[40:43], v[48:51], a[232:235]\n"
    "v_mfma_f32_16x16x32_bf16 a[248:251], a[44:47], v[48:51], a[248:251]\n"
    "v_mfma_f32_16x16x32_bf16 v[200:203], v[208:211], v[48:51], v[200:203]\n"
    "v_mfma_f32_16x16x32_bf16 v[76:79], a[0:3], v[56:59], v[76:79]\n"
    "v_mfma_f32_16x16x32_bf16 v[92:95], a[4:7], v[56:59], v[92:95]\n"
    "v_mfma_f32_16x16x32_bf16 v[108:111], a[8:11], v[56:59], v[108:111]\n"
    "v_mfma_f32_16x16x32_bf16 v[124:127], a[12:15], v[56:59], v[124:127]\n"
    "v_mfma_f32_16x16x32_bf16 v[140:143], a[16:19], v[56:59], v[140:143]\n"
    "v_mfma_f32_16x16x32_bf16 v[156:159], a[20:23], v[56:59], v[156:159]\n"
    "v_mfma_f32_16x16x32_bf16 v[172:175], a[24:27], v[56:59], v[172:175]\n"
    "v_mfma_f32_16x16x32_bf16 v[188:191], a[28:31], v[56:59], v[188:191]\n"
    "v_rcp_f32 v248, v200\n"
    "v_mfma_f32_16x16x32_bf16 a[204:207], a[32:35], v[56:59], a[204:207]\n"
    "v_cmp_nge_f32 vcc, s48, v200\n"
    "v_accvgpr_read_b32 v16, a200\n"
    "v_mfma_f32_16x16x32_bf16 a[220:223], a[36:39], v[56:59], a[220:223]\n"
    "v_accvgpr_read_b32 v17, a201\n"
    "v_accvgpr_read_b32 v18, a202\n"
    "v_mfma_f32_16x16x32_bf16 a[236:239], a[40:43], v[56:59], a[236:239]\n"
    "v_accvgpr_read_b32 v19, a203\n"
    "v_accvgpr_read_b32 v20, a216\n"
    "v_mfma_f32_16x16x32_bf16 a[252:255], a[44:47], v[56:59], a[252:255]\n"
    "v_accvgpr_read_b32 v21, a217\n"
    "v_accvgpr_read_b32 v22, a218\n"
    "v_mfma_f32_16x16x32_bf16 v[204:207], v[208:211], v[56:59], v[204:207]\n"
    "s_waitcnt lgkmcnt(0)\n"
    "v_accvgpr_read_b32 v23, a219\n"
    "v_accvgpr_read_b32 v24, a232\n"
    "v_accvgpr_read_b32 v25, a233\n"
    "v_accvgpr_read_b32 v26, a234\n"
    "v_accvgpr_read_b32 v27, a235\n"
    "v_accvgpr_read_b32 v28, a248\n"
    "v_accvgpr_read_b32 v29, a249\n"
    "v_accvgpr_read_b32 v30, a250\n"
    "v_accvgpr_read_b32 v31, a251\n"
    "s_or_b64 s[46:47], s[46:47], vcc\n"
    "v_pk_mul_f32 v[72:73], v[72:73], v[248:249] op_sel_hi:[1,0]\n"
    "v_pk_mul_f32 v[74:75], v[74:75], v[248:249] op_sel_hi:[1,0]\n"
    "v_pk_mul_f32 v[88:89], v[88:89], v[248:249] op_sel_hi:[1,0]\n"
    "v_pk_mul_f32 v[90:91], v[90:91], v[248:249] op_sel_hi:[1,0]\n"
    "v_cvt_pk_bf16_f32 v72, v72, v73\n"
    "v_cvt_pk_bf16_f32 v73, v74, v75\n"
    "v_pk_mul_f32 v[104:105], v[104:105], v[248:249] op_sel_hi:[1,0]\n"
    "v_pk_mul_f32 v[106:107], v[106:107], v[248:249] op_sel_hi:[1,0]\n"
    "v_cvt_pk_bf16_f32 v88, v88, v89\n"
    "v_cvt_pk_bf16_f32 v89, v90, v91\n"
    "ds_write_b64 v237, v[72:73] offset:0\n"
    "v_pk_mul_f32 v[120:121], v[120:121], v[248:249] op_sel_hi:[1,0]\n"
    "v_pk_mul_f32 v[122:123], v[122:123], v[248:249] op_sel_hi:[1,0]\n"
    "v_cvt_pk_bf16_f32 v104, v104, v105\n"
    "v_cvt_pk_bf16_f32 v105, v106, v107\n"
    "ds_write_b64 v238, v[88:89] offset:0\n"
    "v_pk_mul_f32 v[136:137], v[136:137], v[248:249] op_sel_hi:[1,0]\n"
    "v_pk_mul_f32 v[138:139], v[138:139], v[248:249] op_sel_hi:[1,0]\n"
    "v_cvt_pk_bf16_f32 v120, v120, v121\n"
    "v_cvt_pk_bf16_f32 v121, v122, v123\n"
    "ds_write_b64 v239, v[104:105] offset:0\n"
    "v_pk_mul_f32 v[152:153], v[152:153], v[248:249] op_sel_hi:[1,0]\n"
    "v_pk_mul_f32 v[154:155], v[154:155], v[248:249] op_sel_hi:[1,0]\n"
    "v_cvt_pk_bf16_f32 v136, v136, v137\n"
    "v_cvt_pk_bf16_f32 v137, v138, v139\n"
    "ds_write_b64 v240, v[120:121] offset:0\n"
    "v_pk_mul_f32 v[168:169], v[168:169], v[248:249] op_sel_hi:[1,0]\n"
    "v_pk_mul_f32 v[170:171], v[170:171], v[248:249] op_sel_hi:[1,0]\n"
    "v_cvt_pk_bf16_f32 v152, v152, v153\n"
    "v_cvt_pk_bf16_f32 v153, v154, v155\n"
    "ds_write_b64 v237, v[136:137] offset:128\n"
    "v_pk_mul_f32 v[184:185], v[184:185], v[248:249] op_sel_hi:[1,0]\n"
    "v_pk_mul_f32 v[186:187], v[186:187], v[248:249] op_sel_hi:[1,0]\n"
    "v_cvt_pk_bf16_f32 v168, v168, v169\n"
    "v_cvt_pk_bf16_f32 v169, v170, v171\n"
    "ds_write_b64 v238, v[152:153] offset:128\n"
    "v_pk_mul_f32 v[16:17], v[16:17], v[248:249] op_sel_hi:[1,0]\n"
    "v_pk_mul_f32 v[18:19], v[18:19], v[248:249] op_sel_hi:[1,0]\n"
    "v_cvt_pk_bf16_f32 v184, v184, v185\n"
    "v_cvt_pk_bf16_f32 v185, v186, v187\n"
    "ds_write_b64 v239, v[168:169] offset:128\n"
    "v_pk_mul_f32 v[20:21], v[20:21], v[248:249] op_sel_hi:[1,0]\n"
    "v_pk_mul_f32 v[22:23], v[22:23], v[248:249] op_sel_hi:[1,0]\n"
    "v_cvt_pk_bf16_f32 v16, v16, v17\n"
    "v_cvt_pk_bf16_f32 v17, v18, v19\n"
    "ds_write_b64 v240, v[184:185] offset:128\n"
    "v_pk_mul_f32 v[24:25], v[24:25], v[248:249] op_sel_hi:[1,0]\n"
    "v_pk_mul_f32 v[26:27], v[26:27], v[248:249] op_sel_hi:[1,0]\n"
    "v_cvt_pk_bf16_f32 v20, v20, v21\n"
    "v_cvt_pk_bf16_f32 v21, v22, v23\n"
    "ds_write_b64 v237, v[16:17] offset:256\n"
    "v_pk_mul_f32 v[28:29], v[28:29], v[248:249] op_sel_hi:[1,0]\n"
    "v_pk_mul_f32 v[30:31], v[30:31], v[248:249] op_sel_hi:[1,0]\n"
    "v_cvt_pk_bf16_f32 v24, v24, v25\n"
    "v_cvt_pk_bf16_f32 v25, v26, v27\n"
    "ds_write_b64 v238, v[20:21] offset:256\n"
    "v_cvt_pk_bf16_f32 v28, v28, v29\n"
    "v_cvt_pk_bf16_f32 v29, v30, v31\n"
    "ds_write_b64 v239, v[24:25] offset:256\n"
    "ds_write_b64 v240, v[28:29] offset:256\n"
    "v_rcp_f32 v248, v204\n"
    "v_cmp_nge_f32 vcc, s48, v204\n"
    "v_accvgpr_read_b32 v48, a204\n"
    "v_accvgpr_read_b32 v49, a205\n"
    "v_accvgpr_read_b32 v50, a206\n"
    "v_accvgpr_read_b32 v51, a207\n"
    "v_accvgpr_read_b32 v52, a220\n"
    "v_accvgpr_read_b32 v53, a221\n"
    "v_accvgpr_read_b32 v54, a222\n"
    "v_accvgpr_read_b32 v55, a223\n"
    "v_accvgpr_read_b32 v56, a236\n"
    "v_accvgpr_read_b32 v57, a237\n"
    "v_accvgpr_read_b32 v58, a238\n"
    "v_accvgpr_read_b32 v59, a239\n"
    "v_accvgpr_read_b32 v60, a252\n"
    "v_accvgpr_read_b32 v61, a253\n"
    "v_accvgpr_read_b32 v62, a254\n"
    "v_accvgpr_read_b32 v63, a255\n"
    "s_or_b64 s[46:47], s[46:47], vcc\n"
    "v_pk_mul_f32 v[76:77], v[76:77], v[248:249] op_sel_hi:[1,0]\n"
    "v_pk_mul_f32 v[78:79], v[78:79], v[248:249] op_sel_hi:[1,0]\n"
    "v_pk_mul_f32 v[92:93], v[92:93], v[248:249] op_sel_hi:[1,0]\n"
    "v_pk_mul_f32 v[94:95], v[94:95], v[248:249] op_sel_hi:[1,0]\n"
    "v_cvt_pk_bf16_f32 v76, v76, v77\n"
    "v_cvt_pk_bf16_f32 v77, v78, v79\n"
    "v_pk_mul_f32 v[108:109], v[108:109], v[248:249] op_sel_hi:[1,0]\n"
    "v_pk_mul_f32 v[110:111], v[110:111], v[248:249] op_sel_hi:[1,0]\n"
    "v_cvt_pk_bf16_f32 v92, v92, v93\n"
    "v_cvt_pk_bf16_f32 v93, v94, v95\n"
    "ds_write_b64 v237, v[76:77] offset:6144\n"
    "v_pk_mul_f32 v[124:125], v[124:125], v[248:249] op_sel_hi:[1,0]\n"
    "v_pk_mul_f32 v[126:127], v[126:127], v[248:249] op_sel_hi:[1,0]\n"
    "v_cvt_pk_bf16_f32 v108, v108, v109\n"
    "v_cvt_pk_bf16_f32 v109, v110, v111\n"
    "ds_write_b64 v238, v[92:93] offset:6144\n"
    "v_pk_mul_f32 v[140:141], v[140:141], v[248:249] op_sel_hi:[1,0]\n"
    "v_pk_mul_f32 v[142:143], v[142:143], v[248:249] op_sel_hi:[1,0]\n"
    "v_cvt_pk_bf16_f32 v124, v124, v125\n"
    "v_cvt_pk_bf16_f32 v125, v126, v127\n"
    "s_waitcnt lgkmcnt(0)\n"
    "ds_read_b128 v[72:75], v241 offset:0\n"
    "ds_read_b128 v[88:91], v242 offset:1024\n"
    "ds_read_b128 v[104:107], v243 offset:2048\n"
    "ds_read_b128 v[120:123], v244 offset:3072\n"
    "ds_read_b128 v[136:139], v245 offset:4096\n"
    "ds_read_b128 v[152:155], v246 offset:5120\n"
    "ds_write_b64 v239, v[108:109] offset:6144\n"
    "v_pk_mul_f32 v[156:157], v[156:157], v[248:249] op_sel_hi:[1,0]\n"
    "v_pk_mul_f32 v[158:159], v[158:159], v[248:249] op_sel_hi:[1,0]\n"
    "v_cvt_pk_bf16_f32 v140, v140, v141\n"
    "v_cvt_pk_bf16_f32 v141, v142, v143\n"
    "ds_write_b64 v240, v[124:125] offset:6144\n"
    "v_pk_mul_f32 v[172:173], v[172:173], v[248:249] op_sel_hi:[1,0]\n"
    "v_pk_mul_f32 v[174:175], v[174:175], v[248:249] op_sel_hi:[1,0]\n"
    "v_cvt_pk_bf16_f32 v156, v156, v157\n"
    "v_cvt_pk_bf16_f32 v157, v158, v159\n"
    "ds_write_b64 v237, v[140:141] offset:6272\n"
    "v_pk_mul_f32 v[188:189], v[188:189], v[248:249] op_sel_hi:[1,0]\n"
    "v_pk_mul_f32 v[190:191], v[190:191], v[248:249] op_sel_hi:[1,0]\n"
    "v_cvt_pk_bf16_f32 v172, v172, v173\n"
    "v_cvt_pk_bf16_f32 v173, v174, v175\n"
    "ds_write_b64 v238, v[156:157] offset:6272\n"
    "v_pk_mul_f32 v[48:49], v[48:49], v[248:249] op_sel_hi:[1,0]\n"
    "v_pk_mul_f32 v[50:51], v[50:51], v[248:249] op_sel_hi:[1,0]\n"
    "v_cvt_pk_bf16_f32 v188, v188, v189\n"
    "v_cvt_pk_bf16_f32 v189, v190, v191\n"
    "ds_write_b64 v239, v[172:173] offset:6272\n"
    "v_pk_mul_f32 v[52:53], v[52:53], v[248:249] op_sel_hi:[1,0]\n"
    "v_pk_mul_f32 v[54:55], v[54:55], v[248:249] op_sel_hi:[1,0]\n"
    "v_cvt_pk_bf16_f32 v48, v48, v49\n"
    "v_cvt_pk_bf16_f32 v49, v50, v51\n"
    "ds_write_b64 v240, v[188:189] offset:6272\n"
    "v_pk_mul_f32 v[56:57], v[56:57], v[248:249] op_sel_hi:[1,0]\n"
    "v_pk_mul_f32 v[58:59], v[58:59], v[248:249] op_sel_hi:[1,0]\n"
    "v_cvt_pk_bf16_f32 v52, v52, v53\n"
    "v_cvt_pk_bf16_f32 v53, v54, v55\n"
    "ds_write_b64 v237, v[48:49] offset:6400\n"
    "v_pk_mul_f32 v[60:61], v[60:61], v[248:249] op_sel_hi:[1,0]\n"
    "v_pk_mul_f32 v[62:63], v[62:63], v[248:249] op_sel_hi:[1,0]\n"
    "v_cvt_pk_bf16_f32 v56, v56, v57\n"
    "v_cvt_pk_bf16_f32 v57, v58, v59\n"
    "ds_write_b64 v238, v[52:53] offset:6400\n"
    "v_cvt_pk_bf16_f32 v60, v60, v61\n"
    "v_cvt_pk_bf16_f32 v61, v62, v63\n"
    "ds_write_b64 v239, v[56:57] offset:6400\n"
    "ds_write_b64 v240, v[60:61] offset:6400\n"
    "s_waitcnt lgkmcnt(0)\n"
    "s_add_u32 s43, s41, 12288\n"
    "s_add_u32 s50, s41, 15360\n"
    "buffer_store_dwordx4 v[72:75], v236, s[28:31], s43 offen sc1\n"
    "buffer_store_dwordx4 v[88:91], v236, s[28:31], s43 offen offset:1024 sc1\n"
    "buffer_store_dwordx4 v[104:107], v236, s[28:31], s43 offen offset:2048 sc1\n"
    "buffer_store_dwordx4 v[120:123], v236, s[28:31], s50 offen sc1\n"
    "buffer_store_dwordx4 v[136:139], v236, s[28:31], s50 offen offset:1024 sc1\n"
    "buffer_store_dwordx4 v[152:155], v236, s[28:31], s50 offen offset:2048 sc1\n"
    "s_waitcnt lgkmcnt(0)\n"
    "ds_read_b128 v[76:79], v241 offset:6144\n"
    "ds_read_b128 v[92:95], v242 offset:7168\n"
    "ds_read_b128 v[108:111], v243 offset:8192\n"
    "ds_read_b128 v[124:127], v244 offset:9216\n"
    "ds_read_b128 v[140:143], v245 offset:10240\n"
    "ds_read_b128 v[156:159], v246 offset:11264\n"
    "s_waitcnt lgkmcnt(0)\n"
    "s_add_u32 s43, s41, 18432\n"
    "s_add_u32 s50, s41, 21504\n"
    "buffer_store_dwordx4 v[76:79], v236, s[28:31], s43 offen sc1\n"
    "buffer_store_dwordx4 v[92:95], v236, s[28:31], s43 offen offset:1024 sc1\n"
    "buffer_store_dwordx4 v[108:111], v236, s[28:31], s43 offen offset:2048 sc1\n"
    "buffer_store_dwordx4 v[124:127], v236, s[28:31], s50 offen sc1\n"
    "buffer_store_dwordx4 v[140:143], v236, s[28:31], s50 offen offset:1024 sc1\n"
    "buffer_store_dwordx4 v[156:159], v236, s[28:31], s50 offen offset:2048 sc1\n"
    "s_cmp_lg_u64 s[46:47], 0\n"
    "s_cselect_b32 s50, 2, 0\n"
    "s_or_b32 s45, s45, s50\n"
    "s_mov_b64 s[46:47], 0\n"
    "s_waitcnt vmcnt(0) lgkmcnt(0)\n"
    "s_mov_b32 %0, s45\n"
    "L_code_end:\n"
    : "=s"(flag)
    : "s"(ka), "s"(blockIdx.x), "v"(threadIdx.x)
    : "memory", "vcc", "scc", "s16", "s17", "s18", "s19", "s20", "s21", "s22", "s23", "s24", "s25", "s26", "s27", "s28", "s29", "s30", "s31", "s36", "s37", "s38", "s39", "s40", "s41", "s42", "s43", "s44", "s45", "s46", "s47", "s48", "s49", "s50", "s51", "s52", "s53", "s54", "s55", "s56", "s57", "s58", "s60", "s61", "s62", "s63", "s64", "s65", "s66", "s67", "s68", "s69", "v0", "v1", "v2", "v3", "v4", "v5", "v6", "v7", "v8", "v9", "v10", "v11", "v12", "v13", "v14", "v15", "v16", "v17", "v18", "v19", "v20", "v21", "v22", "v23", "v24", "v25", "v26", "v27", "v28", "v29", "v30", "v31", "v32", "v33", "v34", "v35", "v36", "v37", "v38", "v39", "v40", "v41", "v42", "v43", "v44", "v45", "v46", "v47", "v48", "v49", "v50", "v51", "v52", "v53", "v54", "v55", "v56", "v57", "v58", "v59", "v60", "v61", "v62", "v63", "v64", "v65", "v66", "v67", "v68", "v69", "v70", "v71", "v72", "v73", "v74", "v75", "v76", "v77", "v78", "v79", "v80", "v81", "v82", "v83", "v84", "v85", "v86", "v87", "v88", "v89", "v90", "v91", "v92", "v93", "v94", "v95", "v96", "v97", "v98", "v99", "v100", "v101", "v102", "v103", "v104", "v105", "v106", "v107", "v108", "v109", "v110", "v111", "v112", "v113", "v114", "v115", "v116", "v117", "v118", "v119", "v120", "v121", "v122", "v123", "v124", "v125", "v126", "v127", "v128", "v129", "v130", "v131", "v132", "v133", "v134", "v135", "v136", "v137", "v138", "v139", "v140", "v141", "v142", "v143", "v144", "v145", "v146", "v147", "v148", "v149", "v150", "v151", "v152", "v153", "v154", "v155", "v156", "v157", "v158", "v159", "v160", "v161", "v162", "v163", "v164", "v165", "v166", "v167", "v168", "v169", "v170", "v171", "v172", "v173", "v174", "v175", "v176", "v177", "v178", "v179", "v180", "v181", "v182", "v183", "v184", "v185", "v186", "v187", "v188", "v189", "v190", "v191", "v192", "v193", "v194", "v195", "v196", "v197", "v198", "v199", "v200", "v201", "v202", "v203", "v204", "v205", "v206", "v207", "v208", "v209", "v210", "v211", "v212", "v213", "v214", "v215", "v216", "v217", "v218", "v219", "v220", "v221", "v222", "v223", "v224", "v225", "v226", "v227", "v228", "v229", "v230", "v231", "v232", "v233", "v234", "v235", "v236", "v237", "v238", "v239", "v240", "v241", "v242", "v243", "v244", "v245", "v246", "v247", "v248", "v249", "v250", "v251", "v252", "v253", "v254", "a0", "a1", "a2", "a3", "a4", "a5", "a6", "a7", "a8", "a9", "a10", "a11", "a12", "a13", "a14", "a15", "a16", "a17", "a18", "a19", "a20", "a21", "a22", "a23", "a24", "a25", "a26", "a27", "a28", "a29", "a30", "a31", "a32", "a33", "a34", "a35", "a36", "a37", "a38", "a39", "a40", "a41", "a42", "a43", "a44", "a45", "a46", "a47", "a48", "a49", "a50", "a51", "a52", "a53", "a54", "a55", "a56", "a57", "a58", "a59", "a60", "a61", "a62", "a63", "a64", "a65", "a66", "a67", "a68", "a69", "a70", "a71", "a72", "a73", "a74", "a75", "a76", "a77", "a78", "a79", "a80", "a81", "a82", "a83", "a84", "a85", "a86", "a87", "a88", "a89", "a90", "a91", "a92", "a93", "a94", "a95", "a96", "a97", "a98", "a99", "a100", "a101", "a102", "a103", "a104", "a105", "a106", "a107", "a108", "a109", "a110", "a111", "a112", "a113", "a114", "a115", "a116", "a117", "a118", "a119", "a120", "a121", "a122", "a123", "a124", "a125", "a126", "a127", "a128", "a129", "a130", "a131", "a132", "a133", "a134", "a135", "a136", "a137", "a138", "a139", "a140", "a141", "a142", "a143", "a144", "a145", "a146", "a147", "a148", "a149", "a150", "a151", "a152", "a153", "a154", "a155", "a156", "a157", "a158", "a159", "a160", "a161", "a162", "a163", "a164", "a165", "a166", "a167", "a168", "a169", "a170", "a171", "a172", "a173", "a174", "a175", "a176", "a177", "a178", "a179", "a180", "a181", "a182", "a183", "a184", "a185", "a186", "a187", "a188", "a189", "a190", "a191", "a192", "a193", "a194", "a195", "a196", "a197", "a198", "a199", "a200", "a201", "a202", "a203", "a204", "a205", "a206", "a207", "a208", "a209", "a210", "a211", "a212", "a213", "a214", "a215", "a216", "a217", "a218", "a219", "a220", "a221", "a222", "a223", "a224", "a225", "a226", "a227", "a228", "a229", "a230", "a231", "a232", "a233", "a234", "a235", "a236", "a237", "a238", "a239", "a240", "a241", "a242", "a243", "a244", "a245", "a246", "a247", "a248", "a249", "a250", "a251", "a252", "a253", "a254", "a255");
  if (flag) {
    const unsigned bid = blockIdx.x, lane = threadIdx.x & 63u, wave = threadIdx.x >> 6;
    const unsigned head0 = (bid & 7u) * 32u + (bid >> 4), row0 = ((bid >> 3) & 1u) * 256u + wave * 64u;
#pragma unroll 1
    for (unsigned b = 0; b < 2; ++b)
      if ((flag >> b) & 1u) safe_rows(Q, K, V, O, head0 + 16u * b, row0, lane);
  }
}

extern "C" void kernel_launch(void* const* d_in, const int* in_sizes, int n_in, void* d_out, int out_size, void* d_ws, size_t ws_size, hipStream_t stream) {
  (void)in_sizes; (void)n_in; (void)out_size; (void)ws_size;
  (void)hipFuncSetAttribute((const void*)attn_fwd_kernel, hipFuncAttributeMaxDynamicSharedMemorySize, 163840);
  hipLaunchKernelGGL(attn_fwd_kernel, dim3(256), dim3(256), 163840, stream, (const unsigned short*)d_in[0], (const unsigned short*)d_in[1],
                     (const unsigned short*)d_in[2], (unsigned short*)d_out, (unsigned*)d_ws);
}
```
